```python
import torch

def _softmax(x, axis=-1):
    return torch.softmax(x, dim=axis)
def _gelu(x):
    return torch.nn.functional.gelu(x, approximate="tanh")
def _silu(x):
    return torch.nn.functional.silu(x)
def _sigmoid(x):
    return torch.sigmoid(x)
def _layernorm(x, axis=-1, eps=1e-5):
    m = x.mean(dim=axis, keepdim=True)
    v = x.var(dim=axis, keepdim=True, unbiased=False)
    return (x - m) / torch.sqrt(v + eps)
def _scatter_add(tgt, idx, src, axis=0):
    return tgt.clone().index_add_(axis, idx, src)
def _segment_sum(src, seg, n):
    out = torch.zeros((n,) + src.shape[1:], dtype=src.dtype)
    return out.index_add_(0, seg, src)
def _pad(x, pw):
    flat = tuple(v for pair in reversed(pw) for v in pair)
    return torch.nn.functional.pad(x, flat)

kernel_name = 'sequential_preLN__ssm_conv_s4__dense_activation_relu__prompt'

def setup_inputs():

    _w = (lambda a: (lambda t: t.to(torch.bfloat16) if t.is_floating_point() else t)(torch.as_tensor(a)))
    def _A_setup():
        B, L, D, N = 1, 2048, 640, 128
        A_re = -(lambda _x: torch.exp(torch.as_tensor(_x)))((lambda *s: torch.randn(*s, dtype=torch.bfloat16) * 3.2)(D, N))
        A_im = torch.pi * (lambda *s: torch.randn(*s, dtype=torch.bfloat16) * 0.05)(D, N)
        B_re = (lambda *s: torch.randn(*s, dtype=torch.bfloat16) * 0.1)(D, N)
        B_im = (lambda *s: torch.randn(*s, dtype=torch.bfloat16) * 0.1)(D, N)
        C_re = (lambda *s: torch.randn(*s, dtype=torch.bfloat16) * 0.1)(D, N)
        C_im = (lambda *s: torch.randn(*s, dtype=torch.bfloat16) * 0.1)(D, N)
        log_dt = (lambda *s: torch.randn(*s, dtype=torch.bfloat16) * 0.05)(D) * 0.5 - 4.0
        Dskip = (lambda *s: torch.randn(*s, dtype=torch.bfloat16) * 0.05)(D)
        return {
            "u": _w((lambda *s: torch.randn(*s, dtype=torch.bfloat16) * 0.05)(B, L, D)),
            "A_re": _w(A_re),
            "A_im": _w(A_im),
            "B_re": _w(B_re),
            "B_im": _w(B_im),
            "C_re": _w(C_re),
            "C_im": _w(C_im),
            "log_dt": _w(log_dt),
            "Dskip": _w(Dskip),
        }
    _A_in = _A_setup()

    def _B_setup():
        B, S, D, Dff = 1, 2048, 640, 1728
        return {
            "x": _w((lambda *s: torch.randn(*s, dtype=torch.bfloat16) * 0.05)(B, S, D)),
            "w1": _w((lambda *s: torch.randn(*s, dtype=torch.bfloat16) * 0.05)(D, Dff)),
            "w2": _w((lambda *s: torch.randn(*s, dtype=torch.bfloat16) * 0.05)(Dff, D)),
        }
    _B_in = _B_setup()

    return {
        "A_u": _A_in["u"],
        "A_A_re": _A_in["A_re"],
        "A_A_im": _A_in["A_im"],
        "A_B_re": _A_in["B_re"],
        "A_B_im": _A_in["B_im"],
        "A_C_re": _A_in["C_re"],
        "A_C_im": _A_in["C_im"],
        "A_log_dt": _A_in["log_dt"],
        "A_Dskip": _A_in["Dskip"],
        "B_x": _B_in["x"],
        "B_w1": _B_in["w1"],
        "B_w2": _B_in["w2"],
    }

def reference(A_u, A_A_re, A_A_im, A_B_re, A_B_im, A_C_re, A_C_im, A_log_dt, A_Dskip, B_x, B_w1, B_w2):
    def _A_ref(u, A_re, A_im, B_re, B_im, C_re, C_im, log_dt, Dskip):
        Bsz, L, D = u.shape
        dt = torch.exp(log_dt)[:, None]
        A = A_re + 1j * A_im
        Bc = B_re + 1j * B_im
        Cc = C_re + 1j * C_im
        logdA = dt * A
        dB = dt * Bc
        l = (lambda x, dt: x.to(dt))(torch.arange(L), A_re.dtype)
        P = torch.exp(logdA[:, :, None] * l[None, None, :])
        K = torch.einsum("dn,dnl,dn->dl", Cc, P, dB).real
        L2 = 2 * L
        Kp = _pad(K, ((0, 0), (0, L)))
        up = (lambda x, *d: x.permute(*d))(u, 0, 2, 1)
        up = _pad(up, ((0, 0), (0, 0), (0, L)))
        Kf = (lambda x, axes=(-2,-1): torch.fft.rfft2(x.float(), dim=axes))(Kp[:, None, :], axes=(-2, -1))
        Uf = (lambda x, axes=(-2,-1): torch.fft.rfft2(x.float(), dim=axes))(up[:, :, None, :], axes=(-2, -1))
        Yf = Uf * Kf[None, :, :, :]
        y = (lambda x, s, axes=(-2,-1): torch.fft.irfft2(x, s=s, dim=axes).to(torch.bfloat16))(Yf, (1, L2), axes=(-2, -1))[:, :, 0, :L]
        y = (lambda x, *d: x.permute(*d))(y, 0, 2, 1) + u * Dskip[None, None, :]
        return y

    def _B_ref(x, w1, w2):
        h = torch.einsum("bsd,df->bsf", x, w1)
        zero = torch.zeros_like(h)
        h = torch.maximum(h, zero)
        return torch.einsum("bsf,fd->bsd", h, w2)

    x = A_u
    h = x + _A_ref(_layernorm(x, axis=-1), A_A_re, A_A_im, A_B_re, A_B_im, A_C_re, A_C_im, A_log_dt, A_Dskip)
    h2 = h + _B_ref(_layernorm(h, axis=-1), B_w1, B_w2)
    return h2
```

```cpp
#include <hip/hip_runtime.h>
#include <hip/hip_bf16.h>
#include <stdint.h>

#define L_ 2048
#define D_ 640
#define N_ 128
#define F_ 1728

typedef unsigned short u16;
__device__ __forceinline__ float bf2f(u16 v) { return __uint_as_float(((unsigned)v) << 16); }
__device__ __forceinline__ u16 f2bf(float f) { unsigned u = __float_as_uint(f); return (u16)((u + 0x7FFF + ((u >> 16) & 1)) >> 16); }
__device__ __forceinline__ float rbf(float f) { return bf2f(f2bf(f)); }

__global__ void k_ln(const u16* __restrict__ x, u16* __restrict__ o) {
    int row = blockIdx.x; int t = threadIdx.x;
    float v[10]; float s = 0;
    for (int i = 0; i < 10; i++) { v[i] = bf2f(x[row * D_ + t + 64 * i]); s += v[i]; }
    for (int m = 32; m >= 1; m >>= 1) s += __shfl_xor(s, m);
    float mean = s / D_; float q = 0;
    for (int i = 0; i < 10; i++) { float d = v[i] - mean; q += d * d; }
    for (int m = 32; m >= 1; m >>= 1) q += __shfl_xor(q, m);
    float var = rbf(q / D_); float mb = rbf(mean);
    float sd = rbf(sqrtf(rbf(var + 1e-5f)));
    for (int i = 0; i < 10; i++) o[row * D_ + t + 64 * i] = f2bf(rbf(v[i] - mb) / sd);
}

__global__ void k_kgen(const u16* Are, const u16* Aim, const u16* Bre, const u16* Bim, const u16* Cre, const u16* Cim, const u16* logdt, float* K) {
    int d = blockIdx.x;
    __shared__ float a[N_], b[N_], wr[N_], wi[N_];
    float dt = rbf(expf(bf2f(logdt[d])));
    if (threadIdx.x < N_) {
        int n = threadIdx.x;
        a[n] = dt * bf2f(Are[d * N_ + n]); b[n] = dt * bf2f(Aim[d * N_ + n]);
        float br = dt * bf2f(Bre[d * N_ + n]), bi = dt * bf2f(Bim[d * N_ + n]);
        float cr = bf2f(Cre[d * N_ + n]), ci = bf2f(Cim[d * N_ + n]);
        wr[n] = cr * br - ci * bi; wi[n] = cr * bi + ci * br;
    }
    __syncthreads();
    for (int l = threadIdx.x; l < L_; l += blockDim.x) {
        float lf = rbf((float)l);
        float acc = 0;
        for (int n = 0; n < N_; n++) {
            float e = expf(a[n] * lf); float s, c; sincosf(b[n] * lf, &s, &c);
            acc += e * (wr[n] * c - wi[n] * s);
        }
        K[d * L_ + l] = acc;
    }
}

__global__ void k_conv(const u16* x, const u16* u, const float* K, const u16* Dskip, u16* h) {
    int d = blockIdx.x;
    __shared__ float ks[L_], us[L_];
    for (int l = threadIdx.x; l < L_; l += blockDim.x) { ks[l] = K[d * L_ + l]; us[l] = bf2f(u[l * D_ + d]); }
    __syncthreads();
    float ds = bf2f(Dskip[d]);
    for (int t = threadIdx.x; t < L_; t += blockDim.x) {
        float acc = 0;
        for (int j = 0; j <= t; j++) acc += ks[j] * us[t - j];
        float z = rbf(rbf(acc) + rbf(us[t] * ds));
        h[t * D_ + d] = f2bf(bf2f(x[t * D_ + d]) + z);
    }
}

template <int MODE>
__global__ void k_gemm(const u16* A, const u16* B, const u16* res, u16* C, int M, int N, int Kd) {
    __shared__ float As[16][17], Bs[16][17];
    int tx = threadIdx.x & 15, ty = threadIdx.x >> 4;
    int row = blockIdx.y * 16 + ty, col = blockIdx.x * 16 + tx;
    float acc = 0;
    for (int k0 = 0; k0 < Kd; k0 += 16) {
        As[ty][tx] = bf2f(A[row * Kd + k0 + tx]);
        Bs[ty][tx] = bf2f(B[(k0 + ty) * N + col]);
        __syncthreads();
        for (int k = 0; k < 16; k++) acc += As[ty][k] * Bs[k][tx];
        __syncthreads();
    }
    if (MODE == 0) C[row * N + col] = f2bf(fmaxf(rbf(acc), 0.f));
    else C[row * N + col] = f2bf(bf2f(res[row * N + col]) + rbf(acc));
}

extern "C" void kernel_launch(void* const* d_in, const int* in_sizes, int n_in,
                              void* d_out, int out_size, void* d_ws, size_t ws_size,
                              hipStream_t stream) {
    const u16* x = (const u16*)d_in[0];
    char* ws = (char*)d_ws;
    u16* u = (u16*)ws; ws += L_ * D_ * 2;
    u16* h = (u16*)ws; ws += L_ * D_ * 2;
    u16* a = (u16*)ws; ws += L_ * D_ * 2;
    u16* H = (u16*)ws; ws += L_ * F_ * 2;
    float* K = (float*)ws; ws += D_ * L_ * 4;
    k_ln<<<L_, 64, 0, stream>>>(x, u);
    k_kgen<<<D_, 256, 0, stream>>>((const u16*)d_in[1], (const u16*)d_in[2], (const u16*)d_in[3], (const u16*)d_in[4], (const u16*)d_in[5], (const u16*)d_in[6], (const u16*)d_in[7], K);
    k_conv<<<D_, 256, 0, stream>>>(x, u, K, (const u16*)d_in[8], h);
    k_ln<<<L_, 64, 0, stream>>>(h, a);
    k_gemm<0><<<dim3(F_ / 16, L_ / 16), 256, 0, stream>>>(a, (const u16*)d_in[10], nullptr, H, L_, F_, D_);
    k_gemm<1><<<dim3(D_ / 16, L_ / 16), 256, 0, stream>>>(H, (const u16*)d_in[11], h, (u16*)d_out, L_, D_, F_);
}
```

```cpp
#include <hip/hip_runtime.h>
#include <stdint.h>

#define L_ 2048
#define D_ 640
#define N_ 128
#define F_ 1728

#define GAS __attribute__((address_space(1)))
#define LAS __attribute__((address_space(3)))
typedef unsigned short u16;
typedef unsigned u32;
typedef u32 u32x2 __attribute__((ext_vector_type(2)));
typedef u32 u32x4 __attribute__((ext_vector_type(4)));
typedef float f32x2 __attribute__((ext_vector_type(2)));
typedef float f32x4 __attribute__((ext_vector_type(4)));
typedef float f32x16 __attribute__((ext_vector_type(16)));
typedef short s16x2 __attribute__((ext_vector_type(2)));
typedef short s16x4 __attribute__((ext_vector_type(4)));
typedef __bf16 bf16x2 __attribute__((ext_vector_type(2)));
typedef __bf16 bf16x8 __attribute__((ext_vector_type(8)));

#define WS_UX   0u
#define WS_HT   (6u << 20)
#define WS_HF   (9u << 20)
#define WS_W1T  (17u << 20)
#define WS_W2T  (20u << 20)
#define WS_RS   (23u << 20)
#define WS_SIZE (24u << 20)

#define DEVI __device__ __forceinline__
DEVI float bflo(u32 v) { return __uint_as_float(v << 16); }
DEVI float bfhi(u32 v) { return __uint_as_float(v & 0xffff0000u); }
DEVI u32 pkbf(float lo, float hi) { f32x2 p = {lo, hi}; return __builtin_bit_cast(u32, __builtin_convertvector(p, bf16x2)); }
DEVI float rbf(float x) { return bflo(pkbf(x, x)); }
DEVI u32x2 trread(const char LAS* p) { return __builtin_bit_cast(u32x2, __builtin_amdgcn_ds_read_tr16_b64_v4i16((LAS s16x4*)p)); }
DEVI u32x4 cat(u32x2 a, u32x2 b) { return u32x4{a.x, a.y, b.x, b.y}; }
DEVI f32x4 mfma16(u32x4 a, u32x4 b, f32x4 c) { return __builtin_amdgcn_mfma_f32_16x16x32_bf16(__builtin_bit_cast(bf16x8, a), __builtin_bit_cast(bf16x8, b), c, 0, 0, 0); }
DEVI f32x16 mfma32(u32x4 a, u32x4 b, f32x16 c) { return __builtin_amdgcn_mfma_f32_32x32x16_bf16(__builtin_bit_cast(bf16x8, a), __builtin_bit_cast(bf16x8, b), c, 0, 0, 0); }
DEVI float dot2(u32 a, u32 b, float c) { return __builtin_amdgcn_fdot2_f32_bf16(__builtin_bit_cast(bf16x2, a), __builtin_bit_cast(bf16x2, b), c, false); }
DEVI void dma16(const char* g, char LAS* l) { __builtin_amdgcn_global_load_lds((const GAS void*)g, (LAS void*)l, 16, 0, 0); }
#define WAITV(n) asm volatile("s_waitcnt vmcnt(" #n ")" ::: "memory")
#define WAITL0() asm volatile("s_waitcnt lgkmcnt(0)" ::: "memory")
#define BAR() __builtin_amdgcn_s_barrier()
#define CFENCE() asm volatile("" ::: "memory")

struct Params {
    const char* x; const char* Are; const char* Aim; const char* Bre; const char* Bim; const char* Cre; const char* Cim;
    const char* logdt; const char* Dskip; const char* w1; const char* w2; char* out; char* ws;
};

DEVI void phase_ln1(const Params& P, char LAS* sm, int b) {
    const int tid = threadIdx.x, lane = tid & 63, wave = tid >> 6;
    const int row = wave * 2 + (lane >> 5), l32 = lane & 31;
    const char* xr = P.x + (size_t)(b * 8 + row) * 1280;
    u32x2 v[5];
#pragma unroll
    for (int j = 0; j < 5; j++) v[j] = *(const u32x2*)(xr + (l32 + 32 * j) * 8);
    float f[20]; float s = 0;
#pragma unroll
    for (int j = 0; j < 5; j++) { f[4 * j] = bflo(v[j].x); f[4 * j + 1] = bfhi(v[j].x); f[4 * j + 2] = bflo(v[j].y); f[4 * j + 3] = bfhi(v[j].y); }
#pragma unroll
    for (int j = 0; j < 20; j++) s += f[j];
#pragma unroll
    for (int m = 16; m >= 1; m >>= 1) s += __shfl_xor(s, m);
    const float mean = s * (1.f / D_);
    float q = 0;
#pragma unroll
    for (int j = 0; j < 20; j++) { float d = f[j] - mean; q += d * d; }
#pragma unroll
    for (int m = 16; m >= 1; m >>= 1) q += __shfl_xor(q, m);
    const float mb = rbf(mean);
    const float sd = rbf(__builtin_amdgcn_sqrtf(rbf(rbf(q * (1.f / D_)) + 1e-5f)));
    const float rs = 1.f / sd;
    constexpr int RS = 1312;
#pragma unroll
    for (int j = 0; j < 5; j++) {
        u32x2 u = {pkbf((f[4 * j] - mb) * rs, (f[4 * j + 1] - mb) * rs), pkbf((f[4 * j + 2] - mb) * rs, (f[4 * j + 3] - mb) * rs)};
        *(LAS u32x2*)(sm + row * RS + (l32 + 32 * j) * 8) = u;
        *(LAS u32x2*)(sm + (8 + row) * RS + (l32 + 32 * j) * 8) = v[j];
    }
    __syncthreads();
    const int grp = lane >> 4, arr = grp & 1, q4 = (lane & 15) >> 2, p4 = lane & 3;
#pragma unroll
    for (int j = 0; j < 5; j++) {
        const int ch0 = (wave * 5 + j) * 32 + (grp >> 1) * 16;
        const char LAS* a = sm + (arr * 8 + q4) * RS + ch0 * 2 + p4 * 8;
        u32x4 o = cat(trread(a), trread(a + 4 * RS));
        const int ch = ch0 + (lane & 15);
        *(u32x4*)(P.ws + WS_UX + ((size_t)(ch >> 2) * 256 + b) * 128 + (arr * 4 + (ch & 3)) * 16) = o;
    }
}

DEVI void phase_w1tile(const Params& P, char LAS* sm, int unit) {
    const int lane = threadIdx.x & 63;
    const int cb = unit / 20, ks = unit - cb * 20;
    u32x4 v[4];
#pragma unroll
    for (int j = 0; j < 4; j++) v[j] = *(const u32x4*)(P.w1 + (size_t)(ks * 32 + j * 8 + (lane >> 3)) * (F_ * 2) + cb * 128 + (lane & 7) * 16);
#pragma unroll
    for (int j = 0; j < 4; j++) *(LAS u32x4*)(sm + (j * 8 + (lane >> 3)) * 160 + (lane & 7) * 16) = v[j];
    CFENCE();
    const int hq = lane >> 4, q4 = (lane & 15) >> 2, p4 = lane & 3;
#pragma unroll
    for (int nf = 0; nf < 4; nf++) {
        const char LAS* a = sm + (4 * hq + q4) * 160 + nf * 32 + p4 * 8;
        u32x4 o = cat(trread(a), trread(a + 16 * 160));
        *(u32x4*)(P.ws + WS_W1T + (size_t)(((cb * 2 + (nf >> 1)) * 20 + ks) * 2 + (nf & 1)) * 1024 + lane * 16) = o;
    }
}
DEVI void phase_w2tile(const Params& P, char LAS* sm, int unit) {
    const int lane = threadIdx.x & 63;
    const int p = unit >> 3, cj = unit & 7;
    u32x4 v[5];
#pragma unroll
    for (int j = 0; j < 5; j++) { int c = j * 64 + lane, r = c / 10, k = c - r * 10; v[j] = *(const u32x4*)(P.w2 + (size_t)(p * 32 + r) * 1280 + cj * 160 + k * 16); }
#pragma unroll
    for (int j = 0; j < 5; j++) *(LAS u32x4*)(sm + (j * 64 + lane) * 16) = v[j];
    CFENCE();
    const int hq = lane >> 4, q4 = (lane & 15) >> 2, p4 = lane & 3;
#pragma unroll
    for (int nf = 0; nf < 5; nf++) {
        const char LAS* a = sm + (4 * hq + q4) * 160 + p4 * 40 + nf * 8;
        u32x4 o = cat(trread(a), trread(a + 16 * 160));
        *(u32x4*)(P.ws + WS_W2T + (size_t)((cj * 54 + p) * 5 + nf) * 1024 + lane * 16) = o;
    }
}

DEVI f32x2 cmul(f32x2 a, f32x2 b) {
    f32x2 t = f32x2{a.x, a.x} * b;
    return __builtin_elementwise_fma(f32x2{a.y, a.y}, f32x2{-b.y, b.x}, t);
}
DEVI f32x2 cexpz(float zr2, float zirev, float l) {
    float e = __builtin_amdgcn_exp2f(zr2 * l);
    float ph = __builtin_amdgcn_fractf(zirev * l);
    return f32x2{e * __builtin_amdgcn_cosf(ph), e * __builtin_amdgcn_sinf(ph)};
}
#define KROW 528
#define KCOPY 4160
#define KREG 16896
DEVI void phase_kgen(const Params& P, char LAS* kr, int d) {
    const int lane = threadIdx.x & 63;
    const u32 are = *(const u32*)(P.Are + d * 256 + lane * 4), aim = *(const u32*)(P.Aim + d * 256 + lane * 4);
    const u32 bre = *(const u32*)(P.Bre + d * 256 + lane * 4), bim = *(const u32*)(P.Bim + d * 256 + lane * 4);
    const u32 cre = *(const u32*)(P.Cre + d * 256 + lane * 4), cim = *(const u32*)(P.Cim + d * 256 + lane * 4);
    const float ldt = bflo(*(const u16*)(P.logdt + d * 2)), dsk = bflo(*(const u16*)(P.Dskip + d * 2));
    const float dt = rbf(__expf(ldt));
    float zr[2], zi[2]; f32x2 W[2];
    zr[0] = dt * bflo(are) * 1.44269504089f; zr[1] = dt * bfhi(are) * 1.44269504089f;
    zi[0] = dt * bflo(aim) * 0.15915494309f; zi[1] = dt * bfhi(aim) * 0.15915494309f;
    W[0] = cmul(f32x2{bflo(cre), bflo(cim)}, f32x2{dt * bflo(bre), dt * bflo(bim)});
    W[1] = cmul(f32x2{bfhi(cre), bfhi(cim)}, f32x2{dt * bfhi(bre), dt * bfhi(bim)});
    f32x2 E8[2] = {cexpz(zr[0], zi[0], 8.f), cexpz(zr[1], zi[1], 8.f)};
    char LAS* wp = kr + lane * 8;
    {
        f32x2 y0 = {1.f, 0.f}, y1 = {1.f, 0.f};
#pragma unroll 4
        for (int b = 0; b < 32; b++) {
            *(LAS u32x2*)(wp + b * KROW) = u32x2{pkbf(y0.x, y0.y), pkbf(y1.x, y1.y)};
            y0 = cmul(y0, E8[0]); y1 = cmul(y1, E8[1]);
        }
    }
    CFENCE();
    const int r = lane & 31, hh = lane >> 5;
    u32x4 yf[16];
#pragma unroll
    for (int s = 0; s < 16; s++) yf[s] = *(const LAS u32x4*)(kr + r * KROW + hh * 16 + s * 32);
    CFENCE();
    {
        f32x2 E1[2] = {cexpz(zr[0], zi[0], 1.f), cexpz(zr[1], zi[1], 1.f)};
        f32x2 G[2] = {cexpz(zr[0], zi[0], 256.f), cexpz(zr[1], zi[1], 256.f)};
        f32x2 E2[2] = {cmul(E1[0], E1[0]), cmul(E1[1], E1[1])};
        f32x2 E4[2] = {cmul(E2[0], E2[0]), cmul(E2[1], E2[1])};
#define XST(row, a, b) *(LAS u32x2*)(wp + (row) * KROW) = u32x2{pkbf((a).x, -(a).y), pkbf((b).x, -(b).y)}
#define RHO(reg, h) (((reg) & 3) + 8 * ((reg) >> 2) + 4 * (h))
        f32x2 a = W[0], b = W[1];
#pragma unroll
        for (int c = 0; c < 8; c++) { XST(RHO(c, 0), a, b); if (c < 7) { a = cmul(a, E1[0]); b = cmul(b, E1[1]); } }
        W[0] = cmul(W[0], G[0]); W[1] = cmul(W[1], G[1]); a = W[0]; b = W[1];
        XST(RHO(0, 1), a, b); a = cmul(a, E2[0]); b = cmul(b, E2[1]);
        XST(RHO(2, 1), a, b); a = cmul(a, E2[0]); b = cmul(b, E2[1]);
        XST(RHO(3, 1), a, b); a = cmul(a, E2[0]); b = cmul(b, E2[1]);
        XST(RHO(6, 1), a, b); a = cmul(a, E2[0]); b = cmul(b, E2[1]);
        XST(RHO(7, 1), a, b);
#pragma unroll
        for (int h = 0; h < 2; h++) {
            W[0] = cmul(W[0], G[0]); W[1] = cmul(W[1], G[1]); a = W[0]; b = W[1];
            XST(RHO(8, h), a, b); a = cmul(a, E4[0]); b = cmul(b, E4[1]);
            XST(RHO(9, h), a, b); a = cmul(a, E4[0]); b = cmul(b, E4[1]);
            XST(RHO(10, h), a, b);
        }
#pragma unroll
        for (int k = 0; k < 4; k++) {
            W[0] = cmul(W[0], G[0]); W[1] = cmul(W[1], G[1]);
            XST(RHO(11 + 2 * (k >> 1), k & 1), W[0], W[1]); a = cmul(W[0], E8[0]); b = cmul(W[1], E8[1]);
            XST(RHO(12 + 2 * (k >> 1), k & 1), a, b);
        }
    }
    CFENCE();
    const int rr = (r == 5) ? 4 : ((r == 12 || r == 13) ? 7 : r);
    f32x16 acc = {};
#pragma unroll
    for (int s = 0; s < 16; s++) acc = mfma32(*(const LAS u32x4*)(kr + rr * KROW + hh * 16 + s * 32), yf[s], acc);
    if (lane == 0) acc[0] += dsk;
    u32x4 w[4];
    w[0] = u32x4{pkbf(acc[7], acc[6]), pkbf(acc[5], acc[4]), pkbf(acc[3], acc[2]), pkbf(acc[1], acc[0])};
    w[1] = u32x4{pkbf(acc[10], acc[10]), pkbf(acc[9], acc[9]), pkbf(acc[9], acc[8]), pkbf(acc[8], acc[8])};
    { u32 z = pkbf(acc[11], acc[11]); w[2] = u32x4{pkbf(acc[12], acc[12]), pkbf(acc[12], (r & 1) ? acc[12] : acc[11]), z, z}; }
    { u32 z = pkbf(acc[13], acc[13]); w[3] = u32x4{pkbf(acc[14], acc[14]), pkbf(acc[14], (r & 1) ? acc[14] : acc[13]), z, z}; }
    CFENCE();
    char LAS* cp = kr + (63 - lane) * 16;
#pragma unroll
    for (int s = 0; s < 4; s++) *(LAS u32x4*)(cp + (3 - s) * 1024) = w[s];
    if (lane < 8) *(LAS u32x4*)(kr + (lane >> 1) * KCOPY + 4096 + (lane & 1) * 16) = u32x4{0, 0, 0, 0};
    CFENCE();
#pragma unroll
    for (int s = 0; s < 4; s++) {
        u32x2 nx = *(const LAS u32x2*)(cp + (3 - s) * 1024 + 16);
        u32 o0 = __builtin_amdgcn_alignbit(w[s].y, w[s].x, 16), o1 = __builtin_amdgcn_alignbit(w[s].z, w[s].y, 16);
        u32 o2 = __builtin_amdgcn_alignbit(w[s].w, w[s].z, 16), o3 = __builtin_amdgcn_alignbit(nx.x, w[s].w, 16), o4 = __builtin_amdgcn_alignbit(nx.y, nx.x, 16);
        *(LAS u32x4*)(cp + (3 - s) * 1024 + KCOPY) = u32x4{o0, o1, o2, o3};
        *(LAS u32x4*)(cp + (3 - s) * 1024 + 2 * KCOPY) = u32x4{w[s].y, w[s].z, w[s].w, nx.x};
        *(LAS u32x4*)(cp + (3 - s) * 1024 + 3 * KCOPY) = u32x4{o1, o2, o3, o4};
    }
}

DEVI void conv_load(const Params& P, char LAS* ux, int cg) {
    const int tid = threadIdx.x, lane = tid & 63, wave = tid >> 6;
    const char* src = P.ws + WS_UX + (size_t)cg * 32768 + wave * 1024 + (lane & 7) * 128 + ((lane >> 3) ^ (wave & 1)) * 16;
#pragma unroll
    for (int j = 0; j < 8; j++) dma16(src + j * 4096, ux + 4096 + (wave + 4 * j) * 1024);
    *(LAS u32x4*)(ux + tid * 16) = u32x4{0, 0, 0, 0};
}
DEVI void phase_conv(const Params& P, const char LAS* kr, const char LAS* ux, int cg) {
    const int tid = threadIdx.x, lane = tid & 63, c = tid >> 6;
    const int i = lane & 15, hq = lane >> 4;
    const int d = cg * 4 + c;
    f32x4 acc[8];
    {
        const int kx = 2 * i + (hq >> 1), blk = kx >> 3;
        const char LAS* xp = ux + 4096 + blk * 1024 + (4 + (c ^ (blk & 1))) * 128 + (kx & 7) * 16 + (hq & 1) * 8;
#pragma unroll
        for (int g = 0; g < 8; g++) { u32x2 v = *(const LAS u32x2*)(xp + g * 4096); acc[g] = f32x4{bflo(v.x), bfhi(v.x), bflo(v.y), bfhi(v.y)}; }
    }
    const char LAS* ap = kr + ((3 - i) & 3) * KCOPY + (2028 - 256 * 7 + 8 * hq - 4 * (i >> 2)) * 2;
#pragma unroll 1
    for (int q = 0; q < 8; q++) {
        const int kb = 32 - 4 * q - 2 + 2 * i + hq, blk = kb >> 3;
        const char LAS* bp = ux + blk * 1024 + (c ^ (blk & 1)) * 128 + (kb & 7) * 16;
        u32x4 A[8], B[8];
#pragma unroll
        for (int p = 0; p < 8; p++) {
            const char LAS* a = ap - 64 * q + (7 - p) * 512;
            A[p] = cat(*(const LAS u32x2*)a, *(const LAS u32x2*)(a + 8));
            B[p] = *(const LAS u32x4*)(bp + p * 4096);
        }
#pragma unroll
        for (int p = 0; p < 8; p++)
#pragma unroll
            for (int e = 0; e < 8 - p; e++) acc[p + e] = mfma16(A[p], B[e], acc[p + e]);
    }
    char* hp = P.ws + WS_HT + (size_t)(4 * (hq & 1) + (i >> 2)) * 81920 + d * 128 + (((i & 3) ^ ((d >> 1) & 3)) * 32) + (hq >> 1) * 16;
#pragma unroll
    for (int g = 0; g < 4; g++) {
        u32 a0 = pkbf(acc[2 * g][0], acc[2 * g][1]), a1 = pkbf(acc[2 * g][2], acc[2 * g][3]);
        u32 b0 = pkbf(acc[2 * g + 1][0], acc[2 * g + 1][1]), b1 = pkbf(acc[2 * g + 1][2], acc[2 * g + 1][3]);
        auto s0 = __builtin_amdgcn_permlane16_swap(a0, b0, false, false);
        auto s1 = __builtin_amdgcn_permlane16_swap(a1, b1, false, false);
        *(u32x4*)(hp + (size_t)g * 8 * 81920) = u32x4{s0[0], s1[0], s0[1], s1[1]};
    }
}

template <int NP>
DEVI void gemm1_wave(const Params& P, const char LAS* at, float LAS* st, int rt, int pair0, bool do_stats) {
    const int lane = threadIdx.x & 63;
    const int hq = lane >> 4, q4 = (lane & 15) >> 2, p4 = lane & 3;
    const char* wp = P.ws + WS_W1T + (size_t)pair0 * 40960 + lane * 16;
    u32x4 w[20][2 * NP];
#pragma unroll
    for (int ks = 0; ks < 10; ks++)
#pragma unroll
        for (int n = 0; n < 2 * NP; n++) w[ks][n] = *(const u32x4*)(wp + (n >> 1) * 40960 + (ks * 2 + (n & 1)) * 1024);
    if (NP == 2) WAITV(40); else WAITV(20);
    BAR();
#pragma unroll
    for (int ks = 10; ks < 20; ks++)
#pragma unroll
        for (int n = 0; n < 2 * NP; n++) w[ks][n] = *(const u32x4*)(wp + (n >> 1) * 40960 + (ks * 2 + (n & 1)) * 1024);
    __builtin_amdgcn_sched_barrier(0);
    const int sw = (2 * hq + (q4 >> 1)) & 3;
    const char LAS* ab = at + (4 * hq + q4) * 128 + p4 * 8;
    f32x4 acc[2 * NP][4] = {}; f32x4 cs[2 * NP] = {};
    float s1[4] = {}, s2[4] = {};
    const u32x4 ones = {0x3f803f80u, 0x3f803f80u, 0x3f803f80u, 0x3f803f80u};
#pragma unroll
    for (int ks = 0; ks < 20; ks++) {
        u32x4 h[4];
#pragma unroll
        for (int m = 0; m < 4; m++) { const char LAS* a = ab + ((m ^ sw) * 32) + ks * 4096; h[m] = cat(trread(a), trread(a + 2048)); }
#pragma unroll
        for (int n = 0; n < 2 * NP; n++) {
#pragma unroll
            for (int m = 0; m < 4; m++) acc[n][m] = mfma16(w[ks][n], h[m], acc[n][m]);
            cs[n] = mfma16(w[ks][n], ones, cs[n]);
        }
        if (NP == 1) {
#pragma unroll
            for (int m = 0; m < 4; m++)
#pragma unroll
                for (int j = 0; j < 4; j++) { s1[m] = dot2(h[m][j], 0x3f803f80u, s1[m]); s2[m] = dot2(h[m][j], h[m][j], s2[m]); }
        }
    }
    if (NP == 1) {
#pragma unroll
        for (int m = 0; m < 4; m++) {
            s1[m] += __shfl_xor(s1[m], 16); s1[m] += __shfl_xor(s1[m], 32);
            s2[m] += __shfl_xor(s2[m], 16); s2[m] += __shfl_xor(s2[m], 32);
        }
        const float a1 = hq == 0 ? s1[0] : hq == 1 ? s1[1] : hq == 2 ? s1[2] : s1[3];
        const float a2 = hq == 0 ? s2[0] : hq == 1 ? s2[1] : hq == 2 ? s2[2] : s2[3];
        const float mean = a1 * (1.f / D_);
        const float var = fmaxf(a2 * (1.f / D_) - mean * mean, 0.f);
        const float sd = rbf(__builtin_amdgcn_sqrtf(rbf(rbf(var) + 1e-5f)));
        if (do_stats) { st[lane] = rbf(mean); st[64 + lane] = 1.f / sd; }
    }
    WAITL0();
    BAR();
    float mn[4];
#pragma unroll
    for (int m = 0; m < 4; m++) mn[m] = -st[m * 16 + (lane & 15)];
    char* hp = P.ws + WS_HF + (size_t)(rt * 54 + pair0) * 4096 + lane * 16;
#pragma unroll
    for (int p = 0; p < NP; p++)
#pragma unroll
        for (int m = 0; m < 4; m++) {
            u32 o[4];
#pragma unroll
            for (int j = 0; j < 4; j++) {
                const int n = 2 * p + (j >> 1), e = (j & 1) * 2;
                u32 v = pkbf(fmaf(mn[m], cs[n][e], acc[n][m][e]), fmaf(mn[m], cs[n][e + 1], acc[n][m][e + 1]));
                o[j] = __builtin_bit_cast(u32, __builtin_elementwise_max(__builtin_bit_cast(s16x2, v), s16x2{0, 0}));
            }
            *(u32x4*)(hp + p * 4096 + m * 1024) = u32x4{o[0], o[1], o[2], o[3]};
        }
}
DEVI void phase_gemm1(const Params& P, char LAS* sm, int rt, int cj) {
    const int tid = threadIdx.x, wave = __builtin_amdgcn_readfirstlane(tid >> 6);
    const char* src = P.ws + WS_HT + (size_t)rt * 81920 + tid * 16;
#pragma unroll
    for (int j = 0; j < 20; j++) dma16(src + j * 4096, sm + j * 4096 + wave * 1024);
    float LAS* st = (float LAS*)(sm + 81920);
    const int start = cj < 6 ? 7 * cj : 42 + 6 * (cj - 6);
    const int nheavy = cj < 6 ? 3 : 2;
    if (wave < nheavy) gemm1_wave<2>(P, sm, st, rt, start + 2 * wave, false);
    else gemm1_wave<1>(P, sm, st, rt, start + nheavy + wave, wave == 3);
    if (cj == 0 && tid < 64) ((float*)(P.ws + WS_RS))[rt * 64 + tid] = st[64 + tid];
}

DEVI void phase_gemm2(const Params& P, char LAS* sm, int rt, int cj) {
    const int tid = threadIdx.x, lane = tid & 63, wave = __builtin_amdgcn_readfirstlane(tid >> 6);
    const int hq = lane >> 4, q4 = (lane & 15) >> 2, p4 = lane & 3;
    char LAS* hs = sm + 65536;
    {
        const char* src = P.ws + WS_HT + (size_t)rt * 81920 + cj * 10240 + tid * 16;
        u32x4 t0 = *(const u32x4*)src, t1 = *(const u32x4*)(src + 4096), t2 = {};
        if (tid < 128) t2 = *(const u32x4*)(src + 8192);
        *(LAS u32x4*)(hs + tid * 16) = t0; *(LAS u32x4*)(hs + 4096 + tid * 16) = t1;
        if (tid < 128) *(LAS u32x4*)(hs + 8192 + tid * 16) = t2;
    }
    const float rs = ((const float*)(P.ws + WS_RS))[rt * 64 + wave * 16 + (lane & 15)];
    const int p0 = wave < 2 ? 14 * wave : 28 + 13 * (wave - 2), np = wave < 2 ? 14 : 13;
    const char* wp = P.ws + WS_W2T + (size_t)(cj * 54 + p0) * 5120 + lane * 16;
    const char* hp = P.ws + WS_HF + (size_t)(rt * 54 + p0) * 4096 + lane * 16;
    f32x4 acc[4][5] = {};
    constexpr int DEPTH = 4;
    u32x4 w[DEPTH][5], h[DEPTH][4];
#pragma unroll
    for (int k = 0; k < DEPTH; k++) {
#pragma unroll
        for (int m = 0; m < 4; m++) h[k][m] = *(const u32x4*)(hp + k * 4096 + m * 1024);
#pragma unroll
        for (int n = 0; n < 5; n++) w[k][n] = *(const u32x4*)(wp + k * 5120 + n * 1024);
    }
#pragma unroll
    for (int k = 0; k < 14; k++) {
        if (k < 13 || np == 14) {
#pragma unroll
            for (int m = 0; m < 4; m++)
#pragma unroll
                for (int n = 0; n < 5; n++) acc[m][n] = mfma16(w[k % DEPTH][n], h[k % DEPTH][m], acc[m][n]);
        }
        if (k + DEPTH < 14) {
            const int kk = (k + DEPTH == 13 && np == 13) ? 12 : k + DEPTH;
#pragma unroll
            for (int m = 0; m < 4; m++) h[k % DEPTH][m] = *(const u32x4*)(hp + kk * 4096 + m * 1024);
#pragma unroll
            for (int n = 0; n < 5; n++) w[k % DEPTH][n] = *(const u32x4*)(wp + kk * 5120 + n * 1024);
        }
        __builtin_amdgcn_sched_barrier(0);
    }
#pragma unroll
    for (int m = 0; m < 4; m++) {
        if (m != wave) {
            const int sidx = (wave - m - 1) & 3;
#pragma unroll
            for (int n = 0; n < 5; n++) *(LAS f32x4*)(sm + ((m * 3 + sidx) * 5 + n) * 1024 + lane * 16) = acc[m][n];
        }
    }
    f32x4 o[5];
#pragma unroll
    for (int n = 0; n < 5; n++) o[n] = wave == 0 ? acc[0][n] : wave == 1 ? acc[1][n] : wave == 2 ? acc[2][n] : acc[3][n];
    __syncthreads();
#pragma unroll
    for (int s = 0; s < 3; s++)
#pragma unroll
        for (int n = 0; n < 5; n++) o[n] += *(const LAS f32x4*)(sm + ((wave * 3 + s) * 5 + n) * 1024 + lane * 16);
    u32 ob[10];
#pragma unroll
    for (int n = 0; n < 5; n++) {
        const int dl = 20 * hq + 4 * n + q4;
        u32x2 r = trread(hs + dl * 128 + ((wave ^ ((dl >> 1) & 3)) * 32) + p4 * 8);
        ob[2 * n] = pkbf(bflo(r.x) + rbf(o[n][0] * rs), bfhi(r.x) + rbf(o[n][1] * rs));
        ob[2 * n + 1] = pkbf(bflo(r.y) + rbf(o[n][2] * rs), bfhi(r.y) + rbf(o[n][3] * rs));
    }
    char* op = P.out + (size_t)(rt * 64 + wave * 16 + (lane & 15)) * 1280 + cj * 160 + hq * 40;
    typedef u32x4 u32x4u __attribute__((aligned(8)));
    *(u32x4u*)op = u32x4{ob[0], ob[1], ob[2], ob[3]};
    *(u32x4u*)(op + 16) = u32x4{ob[4], ob[5], ob[6], ob[7]};
    *(u32x2*)(op + 32) = u32x2{ob[8], ob[9]};
}

extern __shared__ __attribute__((aligned(16))) char smem[];

__global__ void __launch_bounds__(256) k_prep(Params P) {
    char LAS* sm = (char LAS*)smem;
    const int b = blockIdx.x, wave = threadIdx.x >> 6;
    if (b < 256) phase_ln1(P, sm, b);
    else if (b < 256 + 135) phase_w1tile(P, sm + wave * 5120, (b - 256) * 4 + wave);
    else phase_w2tile(P, sm + wave * 5120, (b - 391) * 4 + wave);
}
__global__ void __launch_bounds__(256, 1) k_conv(Params P) {
    char LAS* sm = (char LAS*)smem;
    const int b = blockIdx.x, wave = threadIdx.x >> 6;
    const int cg = (b & 7) * 20 + (b >> 3);
    char LAS* ux = sm + 4 * KREG;
    conv_load(P, ux, cg);
    phase_kgen(P, sm + wave * KREG, cg * 4 + wave);
    WAITV(0);
    __syncthreads();
    phase_conv(P, sm + wave * KREG, ux, cg);
}
__global__ void __launch_bounds__(256, 1) k_gemm1(Params P) {
    const int b = blockIdx.x;
    phase_gemm1(P, (char LAS*)smem, (b & 7) * 4 + (b >> 6), (b >> 3) & 7);
}
__global__ void __launch_bounds__(256, 1) k_gemm2(Params P) {
    const int b = blockIdx.x;
    phase_gemm2(P, (char LAS*)smem, (b & 7) * 4 + (b >> 6), (b >> 3) & 7);
}

extern "C" void kernel_launch(void* const* d_in, const int* in_sizes, int n_in,
                              void* d_out, int out_size, void* d_ws, size_t ws_size,
                              hipStream_t stream) {
    Params P;
    P.x = (const char*)d_in[0]; P.Are = (const char*)d_in[1]; P.Aim = (const char*)d_in[2]; P.Bre = (const char*)d_in[3];
    P.Bim = (const char*)d_in[4]; P.Cre = (const char*)d_in[5]; P.Cim = (const char*)d_in[6]; P.logdt = (const char*)d_in[7];
    P.Dskip = (const char*)d_in[8]; P.w1 = (const char*)d_in[10]; P.w2 = (const char*)d_in[11];
    P.out = (char*)d_out; P.ws = (char*)d_ws;
    (void)hipFuncSetAttribute((const void*)k_conv, hipFuncAttributeMaxDynamicSharedMemorySize, 160 * 1024);
    (void)hipFuncSetAttribute((const void*)k_gemm1, hipFuncAttributeMaxDynamicSharedMemorySize, 160 * 1024);
    (void)hipFuncSetAttribute((const void*)k_gemm2, hipFuncAttributeMaxDynamicSharedMemorySize, 160 * 1024);
    k_prep<<<256 + 135 + 108, 256, 21504, stream>>>(P);
    k_conv<<<160, 256, 4 * KREG + 36864, stream>>>(P);
    k_gemm1<<<256, 256, 81920 + 512, stream>>>(P);
    k_gemm2<<<256, 256, 65536 + 10240, stream>>>(P);
}
```

```cpp
#include <hip/hip_runtime.h>
#include <stdint.h>

struct Params {
    const void* x; const void* Are; const void* Aim; const void* Bre; const void* Bim; const void* Cre; const void* Cim;
    const void* logdt; const void* Dskip; const void* w1; const void* w2; void* out; void* ws;
};

__global__ void __launch_bounds__(256, 1) mega(Params P) {
    unsigned tid = threadIdx.x;
    asm volatile(
".p2align 12\n"
"s_load_dwordx16 s[4:19], s[0:1], 0x0\n"
"s_load_dwordx8 s[20:27], s[0:1], 0x40\n"
"s_load_dwordx2 s[28:29], s[0:1], 0x60\n"
"s_getpc_b64 s[54:55]\n"
".Lmg_here:\n"
"s_lshr_b32 s45, s2, 3\n"
"v_and_b32 v56, 15, v0\n"
"s_mul_i32 s53, s45, 1280\n"
"v_min_u32 v56, 9, v56\n"
"s_mov_b32 s52, .Lmg_end-.Lmg_here\n"
"v_lshl_add_u32 v56, v56, 7, s53\n"
"s_mov_b32 m0, 145408\n"
"v_min_u32 v56, s52, v56\n"
"global_load_lds_dword v56, s[54:55]\n"
"v_readfirstlane_b32 s30, v0\n"
"v_mov_b32 v126, v0\n"
"s_and_b32 s44, s2, 7\n"
"v_and_b32 v58, 63, v0\n"
"s_lshr_b32 s30, s30, 6\n"
"v_lshlrev_b32 v127, 4, v58\n"
"s_cmp_ge_u32 s45, 20\n"
"s_cbranch_scc1 .Lmg_help0\n"
"s_mul_i32 s46, s44, 20\n"
"s_add_u32 s46, s46, s45\n"
"s_lshl_b32 s52, s46, 2\n"
"s_add_u32 s52, s52, s30\n"
"s_lshl_b32 s53, s52, 8\n"
"v_lshl_add_u32 v59, v58, 2, s53\n"
"v_lshrrev_b32 v56, 1, v0\n"
"v_min_u32 v56, 102, v56\n"
"s_mul_i32 s54, s45, 103\n"
"v_add_u32 v56, s54, v56\n"
"v_min_u32 v56, 2047, v56\n"
"v_mul_u32_u24 v56, 1280, v56\n"
"v_and_b32 v57, 1, v0\n"
"v_mul_u32_u24 v57, 156, v57\n"
"s_mul_i32 s55, s44, 160\n"
"v_add3_u32 v57, v56, v57, s55\n"
"s_lshl_b32 s53, s52, 1\n"
"s_and_b32 s53, s53, -4\n"
"s_waitcnt lgkmcnt(0)\n"
"s_load_dword s96, s[18:19], s53\n"
"global_load_dword v60, v59, s[6:7]\n"
"global_load_dword v61, v59, s[8:9]\n"
"global_load_dword v62, v59, s[10:11]\n"
"global_load_dword v63, v59, s[12:13]\n"
"global_load_dword v64, v59, s[14:15]\n"
"global_load_dword v65, v59, s[16:17]\n"
"s_load_dword s97, s[20:21], s53\n"
"s_load_dword s31, s[28:29], 0\n"
"global_load_lds_dword v57, s[4:5]\n"
"s_mov_b64 exec, 0\n"
"s_cmp_eq_u32 s30, 1\n"
"s_cbranch_scc1 .Lmg_hopS1\n"
"s_cmp_eq_u32 s30, 2\n"
"s_cbranch_scc1 .Lmg_hopS2\n"
"s_cmp_eq_u32 s30, 3\n"
"s_cbranch_scc1 .Lmg_hopS3\n"
"s_mov_b64 exec, -1\n"
"s_branch .Lmg_conv0\n"
".Lmg_help0:\n"
"s_sub_u32 s52, s2, 160\n"
"s_lshl_b32 s96, s52, 2\n"
"s_add_u32 s96, s96, s30\n"
"v_lshrrev_b32 v40, 4, v58\n"
"s_cmp_ge_u32 s30, 2\n"
"s_cbranch_scc1 .Lmg_hw1\n"
"s_lshl_b32 s97, s52, 1\n"
"s_add_u32 s97, s97, s30\n"
"s_mul_i32 s97, s97, 3\n"
"v_and_b32 v43, 15, v58\n"
"v_mul_u32_u24 v3, 1280, v40\n"
"v_lshl_add_u32 v3, v43, 4, v3\n"
"s_waitcnt lgkmcnt(0)\n"
"s_load_dword s31, s[28:29], 0\n"
"s_cmp_ge_u32 s97, 512\n"
"s_cbranch_scc1 .Lmg_hs\n"
"s_mul_i32 s53, s97, 5120\n"
"s_add_u32 s54, s53, 10240\n"
"s_min_u32 s54, s54, 2616320\n"
"v_add_u32 v1, s53, v3\n"
"v_add_u32 v3, s54, v3\n"
"v_add_u32 v2, 5120, v1\n"
"global_load_dwordx4 v[128:131], v1, s[4:5] offset:0\n"
"global_load_dwordx4 v[132:135], v1, s[4:5] offset:256\n"
"global_load_dwordx4 v[136:139], v1, s[4:5] offset:512\n"
"global_load_dwordx4 v[140:143], v1, s[4:5] offset:768\n"
"global_load_dwordx4 v[144:147], v1, s[4:5] offset:1024\n"
"global_load_dwordx4 v[148:151], v2, s[4:5] offset:0\n"
"global_load_dwordx4 v[152:155], v2, s[4:5] offset:256\n"
"global_load_dwordx4 v[156:159], v2, s[4:5] offset:512\n"
"global_load_dwordx4 v[160:163], v2, s[4:5] offset:768\n"
"global_load_dwordx4 v[164:167], v2, s[4:5] offset:1024\n"
"global_load_dwordx4 v[168:171], v3, s[4:5] offset:0\n"
"global_load_dwordx4 v[172:175], v3, s[4:5] offset:256\n"
"global_load_dwordx4 v[176:179], v3, s[4:5] offset:512\n"
"global_load_dwordx4 v[180:183], v3, s[4:5] offset:768\n"
"global_load_dwordx4 v[184:187], v3, s[4:5] offset:1024\n"
"s_branch .Lmg_hs\n"
".Lmg_hw1:\n"
"s_mul_i32 s53, s52, 205\n"
"s_lshr_b32 s53, s53, 11\n"
"s_mul_i32 s54, s53, 10\n"
"s_sub_u32 s54, s52, s54\n"
"s_lshl_b32 s54, s54, 1\n"
"s_add_u32 s54, s54, s30\n"
"s_sub_u32 s54, s54, 2\n"
"v_lshrrev_b32 v1, 3, v58\n"
"v_and_b32 v2, 6, v1\n"
"v_xor_b32 v2, v2, v58\n"
"v_and_b32 v2, 7, v2\n"
"v_mul_u32_u24 v1, 3456, v1\n"
"v_lshl_add_u32 v1, v2, 4, v1\n"
"s_mul_i32 s55, s54, 110592\n"
"s_mul_i32 s97, s53, 384\n"
"s_add_u32 s55, s55, s97\n"
"v_add_u32 v1, s55, v1\n"
"s_mul_i32 m0, s30, 16896\n"
"s_waitcnt lgkmcnt(0)\n"
"s_load_dword s31, s[28:29], 0\n"
"s_cmp_ge_u32 s52, 90\n"
"s_cbranch_scc1 .Lmg_hw2\n"
"s_sleep 24\n"
"global_load_lds_dwordx4 v1, s[22:23] offset:0\n"
"s_add_u32 m0, m0, 896\n"
"s_nop 0\n"
"global_load_lds_dwordx4 v1, s[22:23] offset:128\n"
"s_add_u32 m0, m0, 896\n"
"s_nop 0\n"
"global_load_lds_dwordx4 v1, s[22:23] offset:256\n"
"s_add_u32 m0, m0, 1280\n"
"v_add_u32 v1, 27648, v1\n"
"global_load_lds_dwordx4 v1, s[22:23] offset:0\n"
"s_add_u32 m0, m0, 896\n"
"s_nop 0\n"
"global_load_lds_dwordx4 v1, s[22:23] offset:128\n"
"s_add_u32 m0, m0, 896\n"
"s_nop 0\n"
"global_load_lds_dwordx4 v1, s[22:23] offset:256\n"
"s_add_u32 m0, m0, 1280\n"
"v_add_u32 v1, 27648, v1\n"
"global_load_lds_dwordx4 v1, s[22:23] offset:0\n"
"s_add_u32 m0, m0, 896\n"
"s_nop 0\n"
"global_load_lds_dwordx4 v1, s[22:23] offset:128\n"
"s_add_u32 m0, m0, 896\n"
"s_nop 0\n"
"global_load_lds_dwordx4 v1, s[22:23] offset:256\n"
"s_add_u32 m0, m0, 1280\n"
"v_add_u32 v1, 27648, v1\n"
"global_load_lds_dwordx4 v1, s[22:23] offset:0\n"
"s_add_u32 m0, m0, 896\n"
"s_nop 0\n"
"global_load_lds_dwordx4 v1, s[22:23] offset:128\n"
"s_add_u32 m0, m0, 896\n"
"s_nop 0\n"
"global_load_lds_dwordx4 v1, s[22:23] offset:256\n"
"s_branch .Lmg_hw2\n"
".Lmg_conv0:\n"
"s_mov_b64 exec, -1\n"
"s_mov_b32 s32, s28\n"
"s_and_b32 s33, s29, 0xffff\n"
"s_mov_b32 s34, 31457280\n"
"s_mov_b32 s35, 0x20000\n"
"v_mul_u32_u24 v100, 10240, v126\n"
"s_lshl_b32 s53, s46, 3\n"
"s_mov_b64 s[0:1], s[4:5]\n"
"v_add_u32 v100, s53, v100\n"
"v_add_u32 v101, 5120, v100\n"
"global_load_dwordx2 v[192:193], v100, s[0:1] offset:0\n"
"s_mul_i32 s53, s30, 16896\n"
"v_mul_u32_u24 v119, 4096, v126\n"
"v_cmp_gt_u32 vcc, 8, v126\n"
"v_bfrev_b32 v49, 2\n"
"v_add_u32 v102, s53, v127\n"
"v_cndmask_b32 v119, v49, v119, vcc\n"
"v_min_u32 v103, 368, v127\n"
"s_mov_b32 s51, 0x3f803f80\n"
"v_lshrrev_b32 v48, 1, v127\n"
"v_add_u32 v120, s53, v48\n"
"v_and_b32 v49, 31, v126\n"
"v_bfe_u32 v50, v126, 5, 1\n"
"v_lshlrev_b32 v50, 4, v50\n"
"v_add_u32 v50, s53, v50\n"
"s_movk_i32 s54, 528\n"
"v_mad_u32_u24 v121, v49, s54, v50\n"
"v_cmp_eq_u32 vcc, 5, v49\n"
"v_and_b32 v51, 30, v49\n"
"v_bfe_u32 v104, v126, 3, 2\n"
"v_cndmask_b32 v49, v49, 4, vcc\n"
"v_cmp_eq_u32 vcc, 12, v51\n"
"v_mul_u32_u24 v104, 4160, v104\n"
"v_and_b32 v51, 7, v126\n"
"v_cndmask_b32 v49, v49, 7, vcc\n"
"v_mad_u32_u24 v122, v49, s54, v50\n"
"v_lshl_add_u32 v104, v51, 2, v104\n"
"v_add_u32 v104, s53, v104\n"
"s_mul_i32 s54, s30, 4608\n"
"s_add_u32 s54, s54, 67584\n"
"v_add_u32 v48, s54, v48\n"
"v_mov_b32 v52, 0\n"
"v_mov_b32 v53, 0\n"
"ds_write_b64 v48, v[52:53]\n"
"v_bfe_u32 v40, v126, 4, 2\n"
"v_bfe_u32 v41, v126, 2, 2\n"
"v_and_b32 v42, 3, v126\n"
"v_and_b32 v43, 15, v126\n"
"v_mul_u32_u24 v112, 4160, v41\n"
"v_lshl_add_u32 v112, v42, 3, v112\n"
"v_lshlrev_b32 v49, 4, v40\n"
"v_sub_u32 v112, v112, v49\n"
"s_add_u32 s55, s53, 56\n"
"v_add_u32 v112, s55, v112\n"
"v_lshl_add_u32 v113, v43, 5, v49\n"
"s_add_u32 s54, s54, 32\n"
"v_add_u32 v113, s54, v113\n"
"v_lshlrev_b32 v117, 5, v43\n"
"v_and_b32 v44, 1, v40\n"
"v_lshl_add_u32 v117, v44, 4, v117\n"
"s_lshl_b32 s54, s30, 12\n"
"s_add_u32 s54, s54, 86016\n"
"v_add_u32 v117, s54, v117\n"
"v_and_b32 v43, 1, v40\n"
"v_lshl_add_u32 v49, v43, 2, v41\n"
"s_mov_b32 s54, 81920\n"
"v_mul_lo_u32 v118, v49, s54\n"
"v_lshrrev_b32 v44, 1, v40\n"
"v_lshl_add_u32 v118, v44, 4, v118\n"
"s_lshr_b32 s54, s52, 1\n"
"s_and_b32 s54, s54, 3\n"
"v_xor_b32 v49, s54, v42\n"
"v_lshl_add_u32 v118, v49, 5, v118\n"
"s_lshl_b32 s52, s52, 7\n"
"s_add_u32 s52, s52, 5160960\n"
"v_lshlrev_b32 v114, 4, v126\n"
"v_add_u32 v115, 68096, v114\n"
"v_add_u32 v76, 2112, v120\n"
"v_add_u32 v77, 4224, v120\n"
"v_add_u32 v78, 6336, v120\n"
"v_add_u32 v79, 8448, v120\n"
"v_add_u32 v80, 10560, v120\n"
"v_add_u32 v81, 12672, v120\n"
"v_add_u32 v82, 14784, v120\n"
"s_lshr_b32 s47, s45, 2\n"
"s_lshr_b32 s54, s44, 1\n"
"s_lshl_b32 s54, s54, 3\n"
"s_add_u32 s47, s47, s54\n"
"s_and_b32 s48, s45, 3\n"
"s_and_b32 s54, s44, 1\n"
"s_lshl_b32 s54, s54, 2\n"
"s_add_u32 s48, s48, s54\n"
"s_mul_i32 s55, s48, 3\n"
"s_add_u32 s49, s55, s30\n"
"s_lshl_b32 s50, s48, 2\n"
"s_add_u32 s50, s50, s30\n"
"s_min_u32 s54, s30, 2\n"
"s_add_u32 s54, s54, s55\n"
"s_add_u32 s54, s54, 6\n"
"s_cmp_lt_u32 s48, 6\n"
"s_cselect_b32 s50, s50, s54\n"
"s_cselect_b32 s54, 7, 2\n"
"s_add_u32 s50, s50, 24\n"
"s_mov_b64 s[36:37], s[32:33]\n"
"s_mov_b32 s39, s35\n"
"s_cmp_eq_u32 s54, s30\n"
"s_cselect_b32 s38, 0, s34\n"
"s_lshl_b32 s62, s48, 2\n"
"s_add_u32 s62, s62, s30\n"
"s_mul_i32 s62, s62, 71680\n"
"s_add_u32 s62, s62, 2867200\n"
"s_mul_i32 s60, s49, 40960\n"
"s_add_u32 s60, s60, 655360\n"
"s_mul_i32 s61, s50, 40960\n"
"s_add_u32 s61, s61, 655360\n"
"s_add_u32 s4, s60, 0\n"
"s_add_u32 s5, s60, 4096\n"
"s_add_u32 s6, s60, 8192\n"
"s_add_u32 s7, s60, 12288\n"
"s_add_u32 s8, s60, 16384\n"
"s_add_u32 s9, s60, 20480\n"
"s_add_u32 s10, s60, 24576\n"
"s_add_u32 s11, s60, 28672\n"
"s_add_u32 s12, s60, 32768\n"
"s_add_u32 s13, s60, 36864\n"
"s_add_u32 s14, s61, 0\n"
"s_add_u32 s15, s61, 4096\n"
"s_add_u32 s16, s61, 8192\n"
"s_add_u32 s17, s61, 12288\n"
"s_add_u32 s18, s61, 16384\n"
"s_add_u32 s19, s61, 20480\n"
"s_add_u32 s20, s61, 24576\n"
"s_add_u32 s21, s61, 28672\n"
"s_add_u32 s22, s61, 32768\n"
"s_add_u32 s23, s61, 36864\n"
"s_mul_i32 s54, s47, 54\n"
"s_add_u32 s3, s54, s49\n"
"s_mul_i32 s3, s3, 4352\n"
"s_add_u32 s3, s3, 7782400\n"
"s_add_u32 s60, s54, s50\n"
"s_mul_i32 s60, s60, 4352\n"
"s_add_u32 s60, s60, 7782400\n"
"s_mov_b64 s[56:57], s[32:33]\n"
"s_mov_b32 s59, s35\n"
"s_cmp_eq_u32 s30, 3\n"
"s_cselect_b32 s58, 0, s34\n"
"s_waitcnt lgkmcnt(0)\n"
"s_lshl_b32 s98, s96, 16\n"
"s_and_b32 s54, s96, 0xffff0000\n"
"s_lshl_b32 s55, s97, 16\n"
"s_and_b32 s97, s97, 0xffff0000\n"
"s_add_u32 s31, s31, 1\n"
"s_bitcmp1_b32 s30, 0\n"
"s_cselect_b32 s98, s54, s98\n"
"s_cselect_b32 s97, s97, s55\n"
"v_mov_b32 v3, 0x3fb8aa3b\n"
"v_mul_f32 v3, s98, v3\n"
"v_exp_f32 v3, v3\n"
"v_mov_b32 v52, 0x3f80\n"
"v_mov_b32 v53, 0x3f80\n"
"v_cvt_pk_bf16_f32 v3, v3, v3\n"
"v_and_b32 v3, 0xffff0000, v3\n"
"v_mul_f32 v38, 0x3fb8aa3b, v3\n"
"v_mul_f32 v39, 0.15915494, v3\n"
"s_waitcnt vmcnt(7)\n"
"v_lshlrev_b32 v4, 16, v60\n"
"v_and_b32 v5, 0xffff0000, v60\n"
"s_waitcnt vmcnt(6)\n"
"v_lshlrev_b32 v6, 16, v61\n"
"v_and_b32 v7, 0xffff0000, v61\n"
"v_mul_f32 v4, v38, v4\n"
"v_mul_f32 v5, v38, v5\n"
"v_mul_f32 v6, v39, v6\n"
"v_mul_f32 v7, v39, v7\n"
"v_mul_f32 v48, 0x41000000, v4\n"
"v_mul_f32 v50, 0x41000000, v6\n"
"v_exp_f32 v48, v48\n"
"v_fract_f32 v50, v50\n"
"v_mul_f32 v49, 0x41000000, v5\n"
"v_mul_f32 v51, 0x41000000, v7\n"
"v_exp_f32 v49, v49\n"
"v_fract_f32 v51, v51\n"
"v_cos_f32 v12, v50\n"
"v_sin_f32 v13, v50\n"
"v_cos_f32 v14, v51\n"
"v_sin_f32 v15, v51\n"
"v_pk_mul_f32 v[12:13], v[12:13], v[48:49] op_sel_hi:[1,0]\n"
"v_pk_mul_f32 v[14:15], v[14:15], v[48:49] op_sel:[0,1]\n"
"v_cvt_pk_bf16_f32 v54, v12, v13\n"
"v_cvt_pk_bf16_f32 v55, v14, v15\n"
"ds_write2_b64 v120, v[52:53], v[54:55] offset0:0 offset1:66\n"
"global_load_dwordx2 v[194:195], v100, s[0:1] offset:1280\n"
"v_pk_mul_f32 v[48:49], v[12:13], v[12:13] op_sel_hi:[0,1]\n"
"v_pk_mul_f32 v[50:51], v[14:15], v[14:15] op_sel_hi:[0,1]\n"
"v_pk_fma_f32 v[32:33], v[12:13], v[12:13], v[48:49] op_sel:[1,1,0] op_sel_hi:[1,0,1] neg_lo:[0,1,0]\n"
"v_pk_fma_f32 v[34:35], v[14:15], v[14:15], v[50:51] op_sel:[1,1,0] op_sel_hi:[1,0,1] neg_lo:[0,1,0]\n"
"v_cvt_pk_bf16_f32 v52, v32, v33\n"
"v_cvt_pk_bf16_f32 v53, v34, v35\n"
"v_pk_mul_f32 v[48:49], v[32:33], v[12:13] op_sel_hi:[0,1]\n"
"v_pk_mul_f32 v[50:51], v[34:35], v[14:15] op_sel_hi:[0,1]\n"
"v_pk_fma_f32 v[32:33], v[32:33], v[12:13], v[48:49] op_sel:[1,1,0] op_sel_hi:[1,0,1] neg_lo:[0,1,0]\n"
"v_pk_fma_f32 v[34:35], v[34:35], v[14:15], v[50:51] op_sel:[1,1,0] op_sel_hi:[1,0,1] neg_lo:[0,1,0]\n"
"v_cvt_pk_bf16_f32 v54, v32, v33\n"
"v_cvt_pk_bf16_f32 v55, v34, v35\n"
"ds_write2_b64 v120, v[52:53], v[54:55] offset0:132 offset1:198\n"
"v_pk_mul_f32 v[48:49], v[32:33], v[12:13] op_sel_hi:[0,1]\n"
"v_pk_mul_f32 v[50:51], v[34:35], v[14:15] op_sel_hi:[0,1]\n"
"v_pk_fma_f32 v[32:33], v[32:33], v[12:13], v[48:49] op_sel:[1,1,0] op_sel_hi:[1,0,1] neg_lo:[0,1,0]\n"
"v_pk_fma_f32 v[34:35], v[34:35], v[14:15], v[50:51] op_sel:[1,1,0] op_sel_hi:[1,0,1] neg_lo:[0,1,0]\n"
"v_cvt_pk_bf16_f32 v52, v32, v33\n"
"v_cvt_pk_bf16_f32 v53, v34, v35\n"
"v_pk_mul_f32 v[48:49], v[32:33], v[12:13] op_sel_hi:[0,1]\n"
"v_pk_mul_f32 v[50:51], v[34:35], v[14:15] op_sel_hi:[0,1]\n"
"v_pk_fma_f32 v[32:33], v[32:33], v[12:13], v[48:49] op_sel:[1,1,0] op_sel_hi:[1,0,1] neg_lo:[0,1,0]\n"
"v_pk_fma_f32 v[34:35], v[34:35], v[14:15], v[50:51] op_sel:[1,1,0] op_sel_hi:[1,0,1] neg_lo:[0,1,0]\n"
"v_cvt_pk_bf16_f32 v54, v32, v33\n"
"v_cvt_pk_bf16_f32 v55, v34, v35\n"
"ds_write2_b64 v76, v[52:53], v[54:55] offset0:0 offset1:66\n"
"v_pk_mul_f32 v[48:49], v[32:33], v[12:13] op_sel_hi:[0,1]\n"
"v_pk_mul_f32 v[50:51], v[34:35], v[14:15] op_sel_hi:[0,1]\n"
"v_pk_fma_f32 v[32:33], v[32:33], v[12:13], v[48:49] op_sel:[1,1,0] op_sel_hi:[1,0,1] neg_lo:[0,1,0]\n"
"v_pk_fma_f32 v[34:35], v[34:35], v[14:15], v[50:51] op_sel:[1,1,0] op_sel_hi:[1,0,1] neg_lo:[0,1,0]\n"
"v_cvt_pk_bf16_f32 v52, v32, v33\n"
"v_cvt_pk_bf16_f32 v53, v34, v35\n"
"v_pk_mul_f32 v[48:49], v[32:33], v[12:13] op_sel_hi:[0,1]\n"
"v_pk_mul_f32 v[50:51], v[34:35], v[14:15] op_sel_hi:[0,1]\n"
"v_pk_fma_f32 v[32:33], v[32:33], v[12:13], v[48:49] op_sel:[1,1,0] op_sel_hi:[1,0,1] neg_lo:[0,1,0]\n"
"v_pk_fma_f32 v[34:35], v[34:35], v[14:15], v[50:51] op_sel:[1,1,0] op_sel_hi:[1,0,1] neg_lo:[0,1,0]\n"
"v_cvt_pk_bf16_f32 v54, v32, v33\n"
"v_cvt_pk_bf16_f32 v55, v34, v35\n"
"ds_write2_b64 v76, v[52:53], v[54:55] offset0:132 offset1:198\n"
"v_pk_mul_f32 v[48:49], v[32:33], v[12:13] op_sel_hi:[0,1]\n"
"v_pk_mul_f32 v[50:51], v[34:35], v[14:15] op_sel_hi:[0,1]\n"
"v_pk_fma_f32 v[32:33], v[32:33], v[12:13], v[48:49] op_sel:[1,1,0] op_sel_hi:[1,0,1] neg_lo:[0,1,0]\n"
"v_pk_fma_f32 v[34:35], v[34:35], v[14:15], v[50:51] op_sel:[1,1,0] op_sel_hi:[1,0,1] neg_lo:[0,1,0]\n"
"v_cvt_pk_bf16_f32 v52, v32, v33\n"
"v_cvt_pk_bf16_f32 v53, v34, v35\n"
"global_load_dwordx2 v[196:197], v100, s[0:1] offset:2560\n"
"v_pk_mul_f32 v[48:49], v[32:33], v[12:13] op_sel_hi:[0,1]\n"
"v_pk_mul_f32 v[50:51], v[34:35], v[14:15] op_sel_hi:[0,1]\n"
"v_pk_fma_f32 v[32:33], v[32:33], v[12:13], v[48:49] op_sel:[1,1,0] op_sel_hi:[1,0,1] neg_lo:[0,1,0]\n"
"v_pk_fma_f32 v[34:35], v[34:35], v[14:15], v[50:51] op_sel:[1,1,0] op_sel_hi:[1,0,1] neg_lo:[0,1,0]\n"
"v_cvt_pk_bf16_f32 v54, v32, v33\n"
"v_cvt_pk_bf16_f32 v55, v34, v35\n"
"ds_write2_b64 v77, v[52:53], v[54:55] offset0:0 offset1:66\n"
"v_pk_mul_f32 v[48:49], v[32:33], v[12:13] op_sel_hi:[0,1]\n"
"v_pk_mul_f32 v[50:51], v[34:35], v[14:15] op_sel_hi:[0,1]\n"
"v_pk_fma_f32 v[32:33], v[32:33], v[12:13], v[48:49] op_sel:[1,1,0] op_sel_hi:[1,0,1] neg_lo:[0,1,0]\n"
"v_pk_fma_f32 v[34:35], v[34:35], v[14:15], v[50:51] op_sel:[1,1,0] op_sel_hi:[1,0,1] neg_lo:[0,1,0]\n"
"v_cvt_pk_bf16_f32 v52, v32, v33\n"
"v_cvt_pk_bf16_f32 v53, v34, v35\n"
"v_pk_mul_f32 v[48:49], v[32:33], v[12:13] op_sel_hi:[0,1]\n"
"v_pk_mul_f32 v[50:51], v[34:35], v[14:15] op_sel_hi:[0,1]\n"
"v_pk_fma_f32 v[32:33], v[32:33], v[12:13], v[48:49] op_sel:[1,1,0] op_sel_hi:[1,0,1] neg_lo:[0,1,0]\n"
"v_pk_fma_f32 v[34:35], v[34:35], v[14:15], v[50:51] op_sel:[1,1,0] op_sel_hi:[1,0,1] neg_lo:[0,1,0]\n"
"v_cvt_pk_bf16_f32 v54, v32, v33\n"
"v_cvt_pk_bf16_f32 v55, v34, v35\n"
"ds_write2_b64 v77, v[52:53], v[54:55] offset0:132 offset1:198\n"
"v_pk_mul_f32 v[48:49], v[32:33], v[12:13] op_sel_hi:[0,1]\n"
"v_pk_mul_f32 v[50:51], v[34:35], v[14:15] op_sel_hi:[0,1]\n"
"v_pk_fma_f32 v[32:33], v[32:33], v[12:13], v[48:49] op_sel:[1,1,0] op_sel_hi:[1,0,1] neg_lo:[0,1,0]\n"
"v_pk_fma_f32 v[34:35], v[34:35], v[14:15], v[50:51] op_sel:[1,1,0] op_sel_hi:[1,0,1] neg_lo:[0,1,0]\n"
"v_cvt_pk_bf16_f32 v52, v32, v33\n"
"v_cvt_pk_bf16_f32 v53, v34, v35\n"
"v_pk_mul_f32 v[48:49], v[32:33], v[12:13] op_sel_hi:[0,1]\n"
"v_pk_mul_f32 v[50:51], v[34:35], v[14:15] op_sel_hi:[0,1]\n"
"v_pk_fma_f32 v[32:33], v[32:33], v[12:13], v[48:49] op_sel:[1,1,0] op_sel_hi:[1,0,1] neg_lo:[0,1,0]\n"
"v_pk_fma_f32 v[34:35], v[34:35], v[14:15], v[50:51] op_sel:[1,1,0] op_sel_hi:[1,0,1] neg_lo:[0,1,0]\n"
"v_cvt_pk_bf16_f32 v54, v32, v33\n"
"v_cvt_pk_bf16_f32 v55, v34, v35\n"
"ds_write2_b64 v78, v[52:53], v[54:55] offset0:0 offset1:66\n"
"v_pk_mul_f32 v[48:49], v[32:33], v[12:13] op_sel_hi:[0,1]\n"
"v_pk_mul_f32 v[50:51], v[34:35], v[14:15] op_sel_hi:[0,1]\n"
"v_pk_fma_f32 v[32:33], v[32:33], v[12:13], v[48:49] op_sel:[1,1,0] op_sel_hi:[1,0,1] neg_lo:[0,1,0]\n"
"v_pk_fma_f32 v[34:35], v[34:35], v[14:15], v[50:51] op_sel:[1,1,0] op_sel_hi:[1,0,1] neg_lo:[0,1,0]\n"
"v_cvt_pk_bf16_f32 v52, v32, v33\n"
"v_cvt_pk_bf16_f32 v53, v34, v35\n"
"v_pk_mul_f32 v[48:49], v[32:33], v[12:13] op_sel_hi:[0,1]\n"
"v_pk_mul_f32 v[50:51], v[34:35], v[14:15] op_sel_hi:[0,1]\n"
"v_pk_fma_f32 v[32:33], v[32:33], v[12:13], v[48:49] op_sel:[1,1,0] op_sel_hi:[1,0,1] neg_lo:[0,1,0]\n"
"v_pk_fma_f32 v[34:35], v[34:35], v[14:15], v[50:51] op_sel:[1,1,0] op_sel_hi:[1,0,1] neg_lo:[0,1,0]\n"
"v_cvt_pk_bf16_f32 v54, v32, v33\n"
"v_cvt_pk_bf16_f32 v55, v34, v35\n"
"ds_write2_b64 v78, v[52:53], v[54:55] offset0:132 offset1:198\n"
"global_load_dwordx2 v[198:199], v100, s[0:1] offset:3840\n"
"v_pk_mul_f32 v[48:49], v[32:33], v[12:13] op_sel_hi:[0,1]\n"
"v_pk_mul_f32 v[50:51], v[34:35], v[14:15] op_sel_hi:[0,1]\n"
"v_pk_fma_f32 v[32:33], v[32:33], v[12:13], v[48:49] op_sel:[1,1,0] op_sel_hi:[1,0,1] neg_lo:[0,1,0]\n"
"v_pk_fma_f32 v[34:35], v[34:35], v[14:15], v[50:51] op_sel:[1,1,0] op_sel_hi:[1,0,1] neg_lo:[0,1,0]\n"
"v_cvt_pk_bf16_f32 v52, v32, v33\n"
"v_cvt_pk_bf16_f32 v53, v34, v35\n"
"v_pk_mul_f32 v[48:49], v[32:33], v[12:13] op_sel_hi:[0,1]\n"
"v_pk_mul_f32 v[50:51], v[34:35], v[14:15] op_sel_hi:[0,1]\n"
"v_pk_fma_f32 v[32:33], v[32:33], v[12:13], v[48:49] op_sel:[1,1,0] op_sel_hi:[1,0,1] neg_lo:[0,1,0]\n"
"v_pk_fma_f32 v[34:35], v[34:35], v[14:15], v[50:51] op_sel:[1,1,0] op_sel_hi:[1,0,1] neg_lo:[0,1,0]\n"
"v_cvt_pk_bf16_f32 v54, v32, v33\n"
"v_cvt_pk_bf16_f32 v55, v34, v35\n"
"ds_write2_b64 v79, v[52:53], v[54:55] offset0:0 offset1:66\n"
"v_pk_mul_f32 v[48:49], v[32:33], v[12:13] op_sel_hi:[0,1]\n"
"v_pk_mul_f32 v[50:51], v[34:35], v[14:15] op_sel_hi:[0,1]\n"
"v_pk_fma_f32 v[32:33], v[32:33], v[12:13], v[48:49] op_sel:[1,1,0] op_sel_hi:[1,0,1] neg_lo:[0,1,0]\n"
"v_pk_fma_f32 v[34:35], v[34:35], v[14:15], v[50:51] op_sel:[1,1,0] op_sel_hi:[1,0,1] neg_lo:[0,1,0]\n"
"v_cvt_pk_bf16_f32 v52, v32, v33\n"
"v_cvt_pk_bf16_f32 v53, v34, v35\n"
"v_pk_mul_f32 v[48:49], v[32:33], v[12:13] op_sel_hi:[0,1]\n"
"v_pk_mul_f32 v[50:51], v[34:35], v[14:15] op_sel_hi:[0,1]\n"
"v_pk_fma_f32 v[32:33], v[32:33], v[12:13], v[48:49] op_sel:[1,1,0] op_sel_hi:[1,0,1] neg_lo:[0,1,0]\n"
"v_pk_fma_f32 v[34:35], v[34:35], v[14:15], v[50:51] op_sel:[1,1,0] op_sel_hi:[1,0,1] neg_lo:[0,1,0]\n"
"v_cvt_pk_bf16_f32 v54, v32, v33\n"
"v_cvt_pk_bf16_f32 v55, v34, v35\n"
"ds_write2_b64 v79, v[52:53], v[54:55] offset0:132 offset1:198\n"
"v_pk_mul_f32 v[48:49], v[32:33], v[12:13] op_sel_hi:[0,1]\n"
"v_pk_mul_f32 v[50:51], v[34:35], v[14:15] op_sel_hi:[0,1]\n"
"v_pk_fma_f32 v[32:33], v[32:33], v[12:13], v[48:49] op_sel:[1,1,0] op_sel_hi:[1,0,1] neg_lo:[0,1,0]\n"
"v_pk_fma_f32 v[34:35], v[34:35], v[14:15], v[50:51] op_sel:[1,1,0] op_sel_hi:[1,0,1] neg_lo:[0,1,0]\n"
"v_cvt_pk_bf16_f32 v52, v32, v33\n"
"v_cvt_pk_bf16_f32 v53, v34, v35\n"
"v_pk_mul_f32 v[48:49], v[32:33], v[12:13] op_sel_hi:[0,1]\n"
"v_pk_mul_f32 v[50:51], v[34:35], v[14:15] op_sel_hi:[0,1]\n"
"v_pk_fma_f32 v[32:33], v[32:33], v[12:13], v[48:49] op_sel:[1,1,0] op_sel_hi:[1,0,1] neg_lo:[0,1,0]\n"
"v_pk_fma_f32 v[34:35], v[34:35], v[14:15], v[50:51] op_sel:[1,1,0] op_sel_hi:[1,0,1] neg_lo:[0,1,0]\n"
"v_cvt_pk_bf16_f32 v54, v32, v33\n"
"v_cvt_pk_bf16_f32 v55, v34, v35\n"
"ds_write2_b64 v80, v[52:53], v[54:55] offset0:0 offset1:66\n"
"v_pk_mul_f32 v[48:49], v[32:33], v[12:13] op_sel_hi:[0,1]\n"
"v_pk_mul_f32 v[50:51], v[34:35], v[14:15] op_sel_hi:[0,1]\n"
"v_pk_fma_f32 v[32:33], v[32:33], v[12:13], v[48:49] op_sel:[1,1,0] op_sel_hi:[1,0,1] neg_lo:[0,1,0]\n"
"v_pk_fma_f32 v[34:35], v[34:35], v[14:15], v[50:51] op_sel:[1,1,0] op_sel_hi:[1,0,1] neg_lo:[0,1,0]\n"
"v_cvt_pk_bf16_f32 v52, v32, v33\n"
"v_cvt_pk_bf16_f32 v53, v34, v35\n"
"global_load_dwordx2 v[200:201], v101, s[0:1] offset:0\n"
"v_pk_mul_f32 v[48:49], v[32:33], v[12:13] op_sel_hi:[0,1]\n"
"v_pk_mul_f32 v[50:51], v[34:35], v[14:15] op_sel_hi:[0,1]\n"
"v_pk_fma_f32 v[32:33], v[32:33], v[12:13], v[48:49] op_sel:[1,1,0] op_sel_hi:[1,0,1] neg_lo:[0,1,0]\n"
"v_pk_fma_f32 v[34:35], v[34:35], v[14:15], v[50:51] op_sel:[1,1,0] op_sel_hi:[1,0,1] neg_lo:[0,1,0]\n"
"v_cvt_pk_bf16_f32 v54, v32, v33\n"
"v_cvt_pk_bf16_f32 v55, v34, v35\n"
"ds_write2_b64 v80, v[52:53], v[54:55] offset0:132 offset1:198\n"
"v_pk_mul_f32 v[48:49], v[32:33], v[12:13] op_sel_hi:[0,1]\n"
"v_pk_mul_f32 v[50:51], v[34:35], v[14:15] op_sel_hi:[0,1]\n"
"v_pk_fma_f32 v[32:33], v[32:33], v[12:13], v[48:49] op_sel:[1,1,0] op_sel_hi:[1,0,1] neg_lo:[0,1,0]\n"
"v_pk_fma_f32 v[34:35], v[34:35], v[14:15], v[50:51] op_sel:[1,1,0] op_sel_hi:[1,0,1] neg_lo:[0,1,0]\n"
"v_cvt_pk_bf16_f32 v52, v32, v33\n"
"v_cvt_pk_bf16_f32 v53, v34, v35\n"
"v_pk_mul_f32 v[48:49], v[32:33], v[12:13] op_sel_hi:[0,1]\n"
"v_pk_mul_f32 v[50:51], v[34:35], v[14:15] op_sel_hi:[0,1]\n"
"v_pk_fma_f32 v[32:33], v[32:33], v[12:13], v[48:49] op_sel:[1,1,0] op_sel_hi:[1,0,1] neg_lo:[0,1,0]\n"
"v_pk_fma_f32 v[34:35], v[34:35], v[14:15], v[50:51] op_sel:[1,1,0] op_sel_hi:[1,0,1] neg_lo:[0,1,0]\n"
"v_cvt_pk_bf16_f32 v54, v32, v33\n"
"v_cvt_pk_bf16_f32 v55, v34, v35\n"
"ds_write2_b64 v81, v[52:53], v[54:55] offset0:0 offset1:66\n"
"v_pk_mul_f32 v[48:49], v[32:33], v[12:13] op_sel_hi:[0,1]\n"
"v_pk_mul_f32 v[50:51], v[34:35], v[14:15] op_sel_hi:[0,1]\n"
"v_pk_fma_f32 v[32:33], v[32:33], v[12:13], v[48:49] op_sel:[1,1,0] op_sel_hi:[1,0,1] neg_lo:[0,1,0]\n"
"v_pk_fma_f32 v[34:35], v[34:35], v[14:15], v[50:51] op_sel:[1,1,0] op_sel_hi:[1,0,1] neg_lo:[0,1,0]\n"
"v_cvt_pk_bf16_f32 v52, v32, v33\n"
"v_cvt_pk_bf16_f32 v53, v34, v35\n"
"v_pk_mul_f32 v[48:49], v[32:33], v[12:13] op_sel_hi:[0,1]\n"
"v_pk_mul_f32 v[50:51], v[34:35], v[14:15] op_sel_hi:[0,1]\n"
"v_pk_fma_f32 v[32:33], v[32:33], v[12:13], v[48:49] op_sel:[1,1,0] op_sel_hi:[1,0,1] neg_lo:[0,1,0]\n"
"v_pk_fma_f32 v[34:35], v[34:35], v[14:15], v[50:51] op_sel:[1,1,0] op_sel_hi:[1,0,1] neg_lo:[0,1,0]\n"
"v_cvt_pk_bf16_f32 v54, v32, v33\n"
"v_cvt_pk_bf16_f32 v55, v34, v35\n"
"ds_write2_b64 v81, v[52:53], v[54:55] offset0:132 offset1:198\n"
"v_pk_mul_f32 v[48:49], v[32:33], v[12:13] op_sel_hi:[0,1]\n"
"v_pk_mul_f32 v[50:51], v[34:35], v[14:15] op_sel_hi:[0,1]\n"
"v_pk_fma_f32 v[32:33], v[32:33], v[12:13], v[48:49] op_sel:[1,1,0] op_sel_hi:[1,0,1] neg_lo:[0,1,0]\n"
"v_pk_fma_f32 v[34:35], v[34:35], v[14:15], v[50:51] op_sel:[1,1,0] op_sel_hi:[1,0,1] neg_lo:[0,1,0]\n"
"v_cvt_pk_bf16_f32 v52, v32, v33\n"
"v_cvt_pk_bf16_f32 v53, v34, v35\n"
"v_pk_mul_f32 v[48:49], v[32:33], v[12:13] op_sel_hi:[0,1]\n"
"v_pk_mul_f32 v[50:51], v[34:35], v[14:15] op_sel_hi:[0,1]\n"
"v_pk_fma_f32 v[32:33], v[32:33], v[12:13], v[48:49] op_sel:[1,1,0] op_sel_hi:[1,0,1] neg_lo:[0,1,0]\n"
"v_pk_fma_f32 v[34:35], v[34:35], v[14:15], v[50:51] op_sel:[1,1,0] op_sel_hi:[1,0,1] neg_lo:[0,1,0]\n"
"v_cvt_pk_bf16_f32 v54, v32, v33\n"
"v_cvt_pk_bf16_f32 v55, v34, v35\n"
"ds_write2_b64 v82, v[52:53], v[54:55] offset0:0 offset1:66\n"
"global_load_dwordx2 v[202:203], v101, s[0:1] offset:1280\n"
"v_pk_mul_f32 v[48:49], v[32:33], v[12:13] op_sel_hi:[0,1]\n"
"v_pk_mul_f32 v[50:51], v[34:35], v[14:15] op_sel_hi:[0,1]\n"
"v_pk_fma_f32 v[32:33], v[32:33], v[12:13], v[48:49] op_sel:[1,1,0] op_sel_hi:[1,0,1] neg_lo:[0,1,0]\n"
"v_pk_fma_f32 v[34:35], v[34:35], v[14:15], v[50:51] op_sel:[1,1,0] op_sel_hi:[1,0,1] neg_lo:[0,1,0]\n"
"v_cvt_pk_bf16_f32 v52, v32, v33\n"
"v_cvt_pk_bf16_f32 v53, v34, v35\n"
"v_pk_mul_f32 v[48:49], v[32:33], v[12:13] op_sel_hi:[0,1]\n"
"v_pk_mul_f32 v[50:51], v[34:35], v[14:15] op_sel_hi:[0,1]\n"
"v_pk_fma_f32 v[32:33], v[32:33], v[12:13], v[48:49] op_sel:[1,1,0] op_sel_hi:[1,0,1] neg_lo:[0,1,0]\n"
"v_pk_fma_f32 v[34:35], v[34:35], v[14:15], v[50:51] op_sel:[1,1,0] op_sel_hi:[1,0,1] neg_lo:[0,1,0]\n"
"v_cvt_pk_bf16_f32 v54, v32, v33\n"
"v_cvt_pk_bf16_f32 v55, v34, v35\n"
"ds_write2_b64 v82, v[52:53], v[54:55] offset0:132 offset1:198\n"
"ds_read_b128 v[128:131], v121 offset:0\n"
"ds_read_b128 v[132:135], v121 offset:32\n"
"ds_read_b128 v[136:139], v121 offset:64\n"
"ds_read_b128 v[140:143], v121 offset:96\n"
"ds_read_b128 v[144:147], v121 offset:128\n"
"ds_read_b128 v[148:151], v121 offset:160\n"
"ds_read_b128 v[152:155], v121 offset:192\n"
"ds_read_b128 v[156:159], v121 offset:224\n"
"ds_read_b128 v[160:163], v121 offset:256\n"
"ds_read_b128 v[164:167], v121 offset:288\n"
"ds_read_b128 v[168:171], v121 offset:320\n"
"ds_read_b128 v[172:175], v121 offset:352\n"
"ds_read_b128 v[176:179], v121 offset:384\n"
"ds_read_b128 v[180:183], v121 offset:416\n"
"ds_read_b128 v[184:187], v121 offset:448\n"
"ds_read_b128 v[188:191], v121 offset:480\n"
"v_pk_mul_f32 v[48:49], v[32:33], v[12:13] op_sel_hi:[0,1]\n"
"v_pk_mul_f32 v[50:51], v[34:35], v[14:15] op_sel_hi:[0,1]\n"
"v_pk_fma_f32 v[28:29], v[32:33], v[12:13], v[48:49] op_sel:[1,1,0] op_sel_hi:[1,0,1] neg_lo:[0,1,0]\n"
"v_pk_fma_f32 v[30:31], v[34:35], v[14:15], v[50:51] op_sel:[1,1,0] op_sel_hi:[1,0,1] neg_lo:[0,1,0]\n"
"s_waitcnt vmcnt(10)\n"
"v_lshlrev_b32 v36, 16, v62\n"
"s_waitcnt vmcnt(9)\n"
"v_lshlrev_b32 v37, 16, v63\n"
"v_and_b32 v38, 0xffff0000, v62\n"
"v_and_b32 v39, 0xffff0000, v63\n"
"s_waitcnt vmcnt(8)\n"
"v_lshlrev_b32 v8, 16, v64\n"
"s_waitcnt vmcnt(7)\n"
"v_lshlrev_b32 v9, 16, v65\n"
"v_and_b32 v10, 0xffff0000, v64\n"
"v_and_b32 v11, 0xffff0000, v65\n"
"v_pk_mul_f32 v[36:37], v[36:37], v[2:3] op_sel:[0,1]\n"
"v_pk_mul_f32 v[38:39], v[38:39], v[2:3] op_sel:[0,1]\n"
"v_pk_mul_f32 v[48:49], v[8:9], v[36:37] op_sel_hi:[0,1]\n"
"v_pk_mul_f32 v[50:51], v[10:11], v[38:39] op_sel_hi:[0,1]\n"
"v_pk_fma_f32 v[8:9], v[8:9], v[36:37], v[48:49] op_sel:[1,1,0] op_sel_hi:[1,0,1] neg_lo:[0,1,0]\n"
"v_pk_fma_f32 v[10:11], v[10:11], v[38:39], v[50:51] op_sel:[1,1,0] op_sel_hi:[1,0,1] neg_lo:[0,1,0]\n"
"v_exp_f32 v48, v4\n"
"v_fract_f32 v50, v6\n"
"v_exp_f32 v49, v5\n"
"v_fract_f32 v51, v7\n"
"v_cos_f32 v16, v50\n"
"v_sin_f32 v17, v50\n"
"v_cos_f32 v18, v51\n"
"v_sin_f32 v19, v51\n"
"v_pk_mul_f32 v[16:17], v[16:17], v[48:49] op_sel_hi:[1,0]\n"
"v_pk_mul_f32 v[18:19], v[18:19], v[48:49] op_sel:[0,1]\n"
"v_pk_mul_f32 v[48:49], v[16:17], v[16:17] op_sel_hi:[0,1]\n"
"v_pk_mul_f32 v[50:51], v[18:19], v[18:19] op_sel_hi:[0,1]\n"
"v_pk_fma_f32 v[20:21], v[16:17], v[16:17], v[48:49] op_sel:[1,1,0] op_sel_hi:[1,0,1] neg_lo:[0,1,0]\n"
"v_pk_fma_f32 v[22:23], v[18:19], v[18:19], v[50:51] op_sel:[1,1,0] op_sel_hi:[1,0,1] neg_lo:[0,1,0]\n"
"v_cvt_pk_bf16_f32 v52, v8, -v9\n"
"v_cvt_pk_bf16_f32 v53, v10, -v11\n"
"v_pk_mul_f32 v[48:49], v[20:21], v[20:21] op_sel_hi:[0,1]\n"
"v_pk_mul_f32 v[50:51], v[22:23], v[22:23] op_sel_hi:[0,1]\n"
"v_pk_fma_f32 v[24:25], v[20:21], v[20:21], v[48:49] op_sel:[1,1,0] op_sel_hi:[1,0,1] neg_lo:[0,1,0]\n"
"v_pk_fma_f32 v[26:27], v[22:23], v[22:23], v[50:51] op_sel:[1,1,0] op_sel_hi:[1,0,1] neg_lo:[0,1,0]\n"
"v_pk_mul_f32 v[48:49], v[8:9], v[16:17] op_sel_hi:[0,1]\n"
"v_pk_mul_f32 v[50:51], v[10:11], v[18:19] op_sel_hi:[0,1]\n"
"v_pk_fma_f32 v[36:37], v[8:9], v[16:17], v[48:49] op_sel:[1,1,0] op_sel_hi:[1,0,1] neg_lo:[0,1,0]\n"
".Lmg_hopS1:\n"
"s_cbranch_execz .Lmg_conv0\n"
"v_pk_fma_f32 v[38:39], v[10:11], v[18:19], v[50:51] op_sel:[1,1,0] op_sel_hi:[1,0,1] neg_lo:[0,1,0]\n"
"v_cvt_pk_bf16_f32 v54, v36, -v37\n"
"v_cvt_pk_bf16_f32 v55, v38, -v39\n"
"ds_write2_b64 v120, v[52:53], v[54:55] offset0:0 offset1:66\n"
"v_pk_mul_f32 v[48:49], v[36:37], v[16:17] op_sel_hi:[0,1]\n"
"v_pk_mul_f32 v[50:51], v[38:39], v[18:19] op_sel_hi:[0,1]\n"
"v_pk_fma_f32 v[36:37], v[36:37], v[16:17], v[48:49] op_sel:[1,1,0] op_sel_hi:[1,0,1] neg_lo:[0,1,0]\n"
"v_pk_fma_f32 v[38:39], v[38:39], v[18:19], v[50:51] op_sel:[1,1,0] op_sel_hi:[1,0,1] neg_lo:[0,1,0]\n"
"v_cvt_pk_bf16_f32 v52, v36, -v37\n"
"v_cvt_pk_bf16_f32 v53, v38, -v39\n"
"v_pk_mul_f32 v[48:49], v[36:37], v[16:17] op_sel_hi:[0,1]\n"
"v_pk_mul_f32 v[50:51], v[38:39], v[18:19] op_sel_hi:[0,1]\n"
"v_pk_fma_f32 v[36:37], v[36:37], v[16:17], v[48:49] op_sel:[1,1,0] op_sel_hi:[1,0,1] neg_lo:[0,1,0]\n"
"v_pk_fma_f32 v[38:39], v[38:39], v[18:19], v[50:51] op_sel:[1,1,0] op_sel_hi:[1,0,1] neg_lo:[0,1,0]\n"
"v_cvt_pk_bf16_f32 v54, v36, -v37\n"
"v_cvt_pk_bf16_f32 v55, v38, -v39\n"
"ds_write2_b64 v120, v[52:53], v[54:55] offset0:132 offset1:198\n"
"v_pk_mul_f32 v[48:49], v[36:37], v[16:17] op_sel_hi:[0,1]\n"
"v_pk_mul_f32 v[50:51], v[38:39], v[18:19] op_sel_hi:[0,1]\n"
"v_pk_fma_f32 v[36:37], v[36:37], v[16:17], v[48:49] op_sel:[1,1,0] op_sel_hi:[1,0,1] neg_lo:[0,1,0]\n"
"v_pk_fma_f32 v[38:39], v[38:39], v[18:19], v[50:51] op_sel:[1,1,0] op_sel_hi:[1,0,1] neg_lo:[0,1,0]\n"
"v_cvt_pk_bf16_f32 v52, v36, -v37\n"
"v_cvt_pk_bf16_f32 v53, v38, -v39\n"
"global_load_dwordx2 v[204:205], v101, s[0:1] offset:2560\n"
"v_pk_mul_f32 v[48:49], v[36:37], v[16:17] op_sel_hi:[0,1]\n"
"v_pk_mul_f32 v[50:51], v[38:39], v[18:19] op_sel_hi:[0,1]\n"
"v_pk_fma_f32 v[36:37], v[36:37], v[16:17], v[48:49] op_sel:[1,1,0] op_sel_hi:[1,0,1] neg_lo:[0,1,0]\n"
"v_pk_fma_f32 v[38:39], v[38:39], v[18:19], v[50:51] op_sel:[1,1,0] op_sel_hi:[1,0,1] neg_lo:[0,1,0]\n"
"v_cvt_pk_bf16_f32 v54, v36, -v37\n"
"v_cvt_pk_bf16_f32 v55, v38, -v39\n"
"ds_write2_b64 v77, v[52:53], v[54:55] offset0:0 offset1:66\n"
"v_pk_mul_f32 v[48:49], v[36:37], v[16:17] op_sel_hi:[0,1]\n"
"v_pk_mul_f32 v[50:51], v[38:39], v[18:19] op_sel_hi:[0,1]\n"
"v_pk_fma_f32 v[36:37], v[36:37], v[16:17], v[48:49] op_sel:[1,1,0] op_sel_hi:[1,0,1] neg_lo:[0,1,0]\n"
"v_pk_fma_f32 v[38:39], v[38:39], v[18:19], v[50:51] op_sel:[1,1,0] op_sel_hi:[1,0,1] neg_lo:[0,1,0]\n"
"v_cvt_pk_bf16_f32 v52, v36, -v37\n"
"v_cvt_pk_bf16_f32 v53, v38, -v39\n"
"v_pk_mul_f32 v[48:49], v[36:37], v[16:17] op_sel_hi:[0,1]\n"
"v_pk_mul_f32 v[50:51], v[38:39], v[18:19] op_sel_hi:[0,1]\n"
"v_pk_fma_f32 v[36:37], v[36:37], v[16:17], v[48:49] op_sel:[1,1,0] op_sel_hi:[1,0,1] neg_lo:[0,1,0]\n"
"v_pk_fma_f32 v[38:39], v[38:39], v[18:19], v[50:51] op_sel:[1,1,0] op_sel_hi:[1,0,1] neg_lo:[0,1,0]\n"
"v_cvt_pk_bf16_f32 v54, v36, -v37\n"
"v_cvt_pk_bf16_f32 v55, v38, -v39\n"
"ds_write2_b64 v77, v[52:53], v[54:55] offset0:132 offset1:198\n"
"v_pk_mul_f32 v[48:49], v[8:9], v[28:29] op_sel_hi:[0,1]\n"
"v_pk_mul_f32 v[50:51], v[10:11], v[30:31] op_sel_hi:[0,1]\n"
"v_pk_fma_f32 v[8:9], v[8:9], v[28:29], v[48:49] op_sel:[1,1,0] op_sel_hi:[1,0,1] neg_lo:[0,1,0]\n"
"v_pk_fma_f32 v[10:11], v[10:11], v[30:31], v[50:51] op_sel:[1,1,0] op_sel_hi:[1,0,1] neg_lo:[0,1,0]\n"
"v_cvt_pk_bf16_f32 v52, v8, -v9\n"
"v_cvt_pk_bf16_f32 v53, v10, -v11\n"
"v_pk_mul_f32 v[48:49], v[8:9], v[20:21] op_sel_hi:[0,1]\n"
"v_pk_mul_f32 v[50:51], v[10:11], v[22:23] op_sel_hi:[0,1]\n"
"v_pk_fma_f32 v[36:37], v[8:9], v[20:21], v[48:49] op_sel:[1,1,0] op_sel_hi:[1,0,1] neg_lo:[0,1,0]\n"
"v_pk_fma_f32 v[38:39], v[10:11], v[22:23], v[50:51] op_sel:[1,1,0] op_sel_hi:[1,0,1] neg_lo:[0,1,0]\n"
"v_cvt_pk_bf16_f32 v54, v36, -v37\n"
"v_cvt_pk_bf16_f32 v55, v38, -v39\n"
"ds_write2_b64 v76, v[52:53], v[54:55] offset0:0 offset1:132\n"
"v_pk_mul_f32 v[48:49], v[36:37], v[20:21] op_sel_hi:[0,1]\n"
"v_pk_mul_f32 v[50:51], v[38:39], v[22:23] op_sel_hi:[0,1]\n"
"v_pk_fma_f32 v[36:37], v[36:37], v[20:21], v[48:49] op_sel:[1,1,0] op_sel_hi:[1,0,1] neg_lo:[0,1,0]\n"
"v_pk_fma_f32 v[38:39], v[38:39], v[22:23], v[50:51] op_sel:[1,1,0] op_sel_hi:[1,0,1] neg_lo:[0,1,0]\n"
"v_cvt_pk_bf16_f32 v54, v36, -v37\n"
"v_cvt_pk_bf16_f32 v55, v38, -v39\n"
"ds_write_b64 v120, v[54:55] offset:3696\n"
"v_pk_mul_f32 v[48:49], v[36:37], v[20:21] op_sel_hi:[0,1]\n"
"v_pk_mul_f32 v[50:51], v[38:39], v[22:23] op_sel_hi:[0,1]\n"
"v_pk_fma_f32 v[36:37], v[36:37], v[20:21], v[48:49] op_sel:[1,1,0] op_sel_hi:[1,0,1] neg_lo:[0,1,0]\n"
"v_pk_fma_f32 v[38:39], v[38:39], v[22:23], v[50:51] op_sel:[1,1,0] op_sel_hi:[1,0,1] neg_lo:[0,1,0]\n"
"v_cvt_pk_bf16_f32 v52, v36, -v37\n"
"v_cvt_pk_bf16_f32 v53, v38, -v39\n"
"global_load_dwordx2 v[206:207], v101, s[0:1] offset:3840\n"
"v_pk_mul_f32 v[48:49], v[36:37], v[20:21] op_sel_hi:[0,1]\n"
"v_pk_mul_f32 v[50:51], v[38:39], v[22:23] op_sel_hi:[0,1]\n"
"v_pk_fma_f32 v[36:37], v[36:37], v[20:21], v[48:49] op_sel:[1,1,0] op_sel_hi:[1,0,1] neg_lo:[0,1,0]\n"
"v_pk_fma_f32 v[38:39], v[38:39], v[22:23], v[50:51] op_sel:[1,1,0] op_sel_hi:[1,0,1] neg_lo:[0,1,0]\n"
"v_cvt_pk_bf16_f32 v54, v36, -v37\n"
"v_cvt_pk_bf16_f32 v55, v38, -v39\n"
"ds_write2_b64 v78, v[52:53], v[54:55] offset0:132 offset1:198\n"
"v_pk_mul_f32 v[48:49], v[8:9], v[28:29] op_sel_hi:[0,1]\n"
"v_pk_mul_f32 v[50:51], v[10:11], v[30:31] op_sel_hi:[0,1]\n"
"v_pk_fma_f32 v[8:9], v[8:9], v[28:29], v[48:49] op_sel:[1,1,0] op_sel_hi:[1,0,1] neg_lo:[0,1,0]\n"
"v_pk_fma_f32 v[10:11], v[10:11], v[30:31], v[50:51] op_sel:[1,1,0] op_sel_hi:[1,0,1] neg_lo:[0,1,0]\n"
"v_cvt_pk_bf16_f32 v52, v8, -v9\n"
"v_cvt_pk_bf16_f32 v53, v10, -v11\n"
"v_pk_mul_f32 v[48:49], v[8:9], v[24:25] op_sel_hi:[0,1]\n"
"v_pk_mul_f32 v[50:51], v[10:11], v[26:27] op_sel_hi:[0,1]\n"
"v_pk_fma_f32 v[36:37], v[8:9], v[24:25], v[48:49] op_sel:[1,1,0] op_sel_hi:[1,0,1] neg_lo:[0,1,0]\n"
"v_pk_fma_f32 v[38:39], v[10:11], v[26:27], v[50:51] op_sel:[1,1,0] op_sel_hi:[1,0,1] neg_lo:[0,1,0]\n"
"v_cvt_pk_bf16_f32 v54, v36, -v37\n"
"v_cvt_pk_bf16_f32 v55, v38, -v39\n"
"ds_write2_b64 v79, v[52:53], v[54:55] offset0:0 offset1:66\n"
"v_pk_mul_f32 v[48:49], v[36:37], v[24:25] op_sel_hi:[0,1]\n"
"v_pk_mul_f32 v[50:51], v[38:39], v[26:27] op_sel_hi:[0,1]\n"
"v_pk_fma_f32 v[36:37], v[36:37], v[24:25], v[48:49] op_sel:[1,1,0] op_sel_hi:[1,0,1] neg_lo:[0,1,0]\n"
"v_pk_fma_f32 v[38:39], v[38:39], v[26:27], v[50:51] op_sel:[1,1,0] op_sel_hi:[1,0,1] neg_lo:[0,1,0]\n"
"v_cvt_pk_bf16_f32 v52, v36, -v37\n"
"v_cvt_pk_bf16_f32 v53, v38, -v39\n"
"v_pk_mul_f32 v[48:49], v[8:9], v[28:29] op_sel_hi:[0,1]\n"
"v_pk_mul_f32 v[50:51], v[10:11], v[30:31] op_sel_hi:[0,1]\n"
"v_pk_fma_f32 v[8:9], v[8:9], v[28:29], v[48:49] op_sel:[1,1,0] op_sel_hi:[1,0,1] neg_lo:[0,1,0]\n"
"v_pk_fma_f32 v[10:11], v[10:11], v[30:31], v[50:51] op_sel:[1,1,0] op_sel_hi:[1,0,1] neg_lo:[0,1,0]\n"
"v_cvt_pk_bf16_f32 v68, v8, -v9\n"
"v_cvt_pk_bf16_f32 v69, v10, -v11\n"
"v_pk_mul_f32 v[48:49], v[8:9], v[24:25] op_sel_hi:[0,1]\n"
"v_pk_mul_f32 v[50:51], v[10:11], v[26:27] op_sel_hi:[0,1]\n"
"v_pk_fma_f32 v[36:37], v[8:9], v[24:25], v[48:49] op_sel:[1,1,0] op_sel_hi:[1,0,1] neg_lo:[0,1,0]\n"
"v_pk_fma_f32 v[38:39], v[10:11], v[26:27], v[50:51] op_sel:[1,1,0] op_sel_hi:[1,0,1] neg_lo:[0,1,0]\n"
"v_cvt_pk_bf16_f32 v54, v36, -v37\n"
"v_cvt_pk_bf16_f32 v55, v38, -v39\n"
"ds_write2_b64 v80, v[68:69], v[54:55] offset0:0 offset1:66\n"
"v_pk_mul_f32 v[48:49], v[36:37], v[24:25] op_sel_hi:[0,1]\n"
"v_pk_mul_f32 v[50:51], v[38:39], v[26:27] op_sel_hi:[0,1]\n"
"v_pk_fma_f32 v[36:37], v[36:37], v[24:25], v[48:49] op_sel:[1,1,0] op_sel_hi:[1,0,1] neg_lo:[0,1,0]\n"
"v_pk_fma_f32 v[38:39], v[38:39], v[26:27], v[50:51] op_sel:[1,1,0] op_sel_hi:[1,0,1] neg_lo:[0,1,0]\n"
"v_cvt_pk_bf16_f32 v68, v36, -v37\n"
"v_cvt_pk_bf16_f32 v69, v38, -v39\n"
"v_pk_mul_f32 v[48:49], v[8:9], v[28:29] op_sel_hi:[0,1]\n"
"v_pk_mul_f32 v[50:51], v[10:11], v[30:31] op_sel_hi:[0,1]\n"
"v_pk_fma_f32 v[8:9], v[8:9], v[28:29], v[48:49] op_sel:[1,1,0] op_sel_hi:[1,0,1] neg_lo:[0,1,0]\n"
"v_pk_fma_f32 v[10:11], v[10:11], v[30:31], v[50:51] op_sel:[1,1,0] op_sel_hi:[1,0,1] neg_lo:[0,1,0]\n"
"v_cvt_pk_bf16_f32 v54, v8, -v9\n"
"v_cvt_pk_bf16_f32 v55, v10, -v11\n"
"ds_write2_b64 v79, v[52:53], v[54:55] offset0:132 offset1:198\n"
"v_pk_mul_f32 v[48:49], v[8:9], v[12:13] op_sel_hi:[0,1]\n"
"v_pk_mul_f32 v[50:51], v[10:11], v[14:15] op_sel_hi:[0,1]\n"
"v_pk_fma_f32 v[36:37], v[8:9], v[12:13], v[48:49] op_sel:[1,1,0] op_sel_hi:[1,0,1] neg_lo:[0,1,0]\n"
"v_pk_fma_f32 v[38:39], v[10:11], v[14:15], v[50:51] op_sel:[1,1,0] op_sel_hi:[1,0,1] neg_lo:[0,1,0]\n"
"v_cvt_pk_bf16_f32 v54, v36, -v37\n"
"v_cvt_pk_bf16_f32 v55, v38, -v39\n"
"ds_write_b64 v120, v[54:55] offset:12672\n"
"v_pk_mul_f32 v[48:49], v[8:9], v[28:29] op_sel_hi:[0,1]\n"
"v_pk_mul_f32 v[50:51], v[10:11], v[30:31] op_sel_hi:[0,1]\n"
"v_pk_fma_f32 v[8:9], v[8:9], v[28:29], v[48:49] op_sel:[1,1,0] op_sel_hi:[1,0,1] neg_lo:[0,1,0]\n"
"v_pk_fma_f32 v[10:11], v[10:11], v[30:31], v[50:51] op_sel:[1,1,0] op_sel_hi:[1,0,1] neg_lo:[0,1,0]\n"
"v_cvt_pk_bf16_f32 v54, v8, -v9\n"
"v_cvt_pk_bf16_f32 v55, v10, -v11\n"
"ds_write2_b64 v80, v[68:69], v[54:55] offset0:132 offset1:198\n"
"v_pk_mul_f32 v[48:49], v[8:9], v[12:13] op_sel_hi:[0,1]\n"
"v_pk_mul_f32 v[50:51], v[10:11], v[14:15] op_sel_hi:[0,1]\n"
"v_pk_fma_f32 v[36:37], v[8:9], v[12:13], v[48:49] op_sel:[1,1,0] op_sel_hi:[1,0,1] neg_lo:[0,1,0]\n"
"v_pk_fma_f32 v[38:39], v[10:11], v[14:15], v[50:51] op_sel:[1,1,0] op_sel_hi:[1,0,1] neg_lo:[0,1,0]\n"
"v_cvt_pk_bf16_f32 v54, v36, -v37\n"
"v_cvt_pk_bf16_f32 v55, v38, -v39\n"
"ds_write_b64 v120, v[54:55] offset:14784\n"
"v_pk_mul_f32 v[48:49], v[8:9], v[28:29] op_sel_hi:[0,1]\n"
"v_pk_mul_f32 v[50:51], v[10:11], v[30:31] op_sel_hi:[0,1]\n"
"v_pk_fma_f32 v[8:9], v[8:9], v[28:29], v[48:49] op_sel:[1,1,0] op_sel_hi:[1,0,1] neg_lo:[0,1,0]\n"
"v_pk_fma_f32 v[10:11], v[10:11], v[30:31], v[50:51] op_sel:[1,1,0] op_sel_hi:[1,0,1] neg_lo:[0,1,0]\n"
"v_cvt_pk_bf16_f32 v52, v8, -v9\n"
"v_cvt_pk_bf16_f32 v53, v10, -v11\n"
"v_pk_mul_f32 v[48:49], v[8:9], v[12:13] op_sel_hi:[0,1]\n"
"v_pk_mul_f32 v[50:51], v[10:11], v[14:15] op_sel_hi:[0,1]\n"
"v_pk_fma_f32 v[36:37], v[8:9], v[12:13], v[48:49] op_sel:[1,1,0] op_sel_hi:[1,0,1] neg_lo:[0,1,0]\n"
"v_pk_fma_f32 v[38:39], v[10:11], v[14:15], v[50:51] op_sel:[1,1,0] op_sel_hi:[1,0,1] neg_lo:[0,1,0]\n"
"v_cvt_pk_bf16_f32 v54, v36, -v37\n"
"v_cvt_pk_bf16_f32 v55, v38, -v39\n"
"ds_write2_b64 v81, v[52:53], v[54:55] offset0:66 offset1:132\n"
"v_pk_mul_f32 v[48:49], v[8:9], v[28:29] op_sel_hi:[0,1]\n"
"v_pk_mul_f32 v[50:51], v[10:11], v[30:31] op_sel_hi:[0,1]\n"
"v_pk_fma_f32 v[8:9], v[8:9], v[28:29], v[48:49] op_sel:[1,1,0] op_sel_hi:[1,0,1] neg_lo:[0,1,0]\n"
"v_pk_fma_f32 v[10:11], v[10:11], v[30:31], v[50:51] op_sel:[1,1,0] op_sel_hi:[1,0,1] neg_lo:[0,1,0]\n"
"v_cvt_pk_bf16_f32 v52, v8, -v9\n"
"v_cvt_pk_bf16_f32 v53, v10, -v11\n"
"v_pk_mul_f32 v[48:49], v[8:9], v[12:13] op_sel_hi:[0,1]\n"
"v_pk_mul_f32 v[50:51], v[10:11], v[14:15] op_sel_hi:[0,1]\n"
"v_pk_fma_f32 v[36:37], v[8:9], v[12:13], v[48:49] op_sel:[1,1,0] op_sel_hi:[1,0,1] neg_lo:[0,1,0]\n"
"v_pk_fma_f32 v[38:39], v[10:11], v[14:15], v[50:51] op_sel:[1,1,0] op_sel_hi:[1,0,1] neg_lo:[0,1,0]\n"
"v_cvt_pk_bf16_f32 v54, v36, -v37\n"
"v_cvt_pk_bf16_f32 v55, v38, -v39\n"
"ds_write2_b64 v82, v[52:53], v[54:55] offset0:66 offset1:132\n"
"ds_read_b128 v[68:71], v122 offset:0\n"
"ds_read_b128 v[72:75], v122 offset:32\n"
"ds_read_b128 v[76:79], v122 offset:64\n"
"ds_read_b128 v[80:83], v122 offset:96\n"
"ds_read_b128 v[84:87], v122 offset:128\n"
"ds_read_b128 v[88:91], v122 offset:160\n"
"ds_read_b128 v[92:95], v122 offset:192\n"
"ds_read_b128 v[96:99], v122 offset:224\n"
"s_lshl_b32 s98, s44, 14\n"
"s_add_u32 s98, s98, 524288\n"
"s_add_u32 s99, s98, 8192\n"
"v_add_u32 v116, 4096, v114\n"
"s_lshl_b32 s54, s44, 11\n"
"s_add_u32 s55, s54, 163840\n"
"s_add_u32 s54, s54, 131072\n"
"s_mov_b32 s0, 0x05040100\n"
"s_mov_b32 s1, 0x07060302\n"
"v_cmp_eq_u32 vcc, 0, v127\n"
"v_mov_b32 v20, s97\n"
"v_and_b32 v21, 16, v127\n"
"v_cndmask_b32 v20, 0, v20, vcc\n"
"v_cmp_ne_u32 vcc, 0, v21\n"
"s_waitcnt lgkmcnt(7)\n"
"v_mfma_f32_32x32x16_bf16 v[4:19], v[68:71], v[128:131], 0\n"
"ds_read_b128 v[68:71], v122 offset:256\n"
"v_mov_b32 v22, 0\n"
"s_waitcnt vmcnt(7)\n"
"v_lshlrev_b32 v224, 16, v192\n"
"s_waitcnt lgkmcnt(7)\n"
"v_mfma_f32_32x32x16_bf16 v[4:19], v[72:75], v[132:135], v[4:19]\n"
"ds_read_b128 v[72:75], v122 offset:288\n"
"v_and_b32 v225, 0xffff0000, v192\n"
"v_lshlrev_b32 v226, 16, v193\n"
"v_and_b32 v227, 0xffff0000, v193\n"
"s_waitcnt lgkmcnt(7)\n"
"v_mfma_f32_32x32x16_bf16 v[4:19], v[76:79], v[136:139], v[4:19]\n"
"ds_read_b128 v[76:79], v122 offset:320\n"
"s_waitcnt vmcnt(6)\n"
"v_lshlrev_b32 v228, 16, v194\n"
"v_and_b32 v229, 0xffff0000, v194\n"
"s_waitcnt lgkmcnt(7)\n"
"v_mfma_f32_32x32x16_bf16 v[4:19], v[80:83], v[140:143], v[4:19]\n"
"ds_read_b128 v[80:83], v122 offset:352\n"
"v_lshlrev_b32 v230, 16, v195\n"
"v_and_b32 v231, 0xffff0000, v195\n"
"s_waitcnt vmcnt(5)\n"
"v_lshlrev_b32 v232, 16, v196\n"
"s_waitcnt lgkmcnt(7)\n"
"v_mfma_f32_32x32x16_bf16 v[4:19], v[84:87], v[144:147], v[4:19]\n"
"ds_read_b128 v[84:87], v122 offset:384\n"
"v_and_b32 v233, 0xffff0000, v196\n"
"v_lshlrev_b32 v234, 16, v197\n"
"v_and_b32 v235, 0xffff0000, v197\n"
"s_waitcnt lgkmcnt(7)\n"
"v_mfma_f32_32x32x16_bf16 v[4:19], v[88:91], v[148:151], v[4:19]\n"
"ds_read_b128 v[88:91], v122 offset:416\n"
"s_waitcnt vmcnt(4)\n"
"v_lshlrev_b32 v236, 16, v198\n"
"v_and_b32 v237, 0xffff0000, v198\n"
"s_waitcnt lgkmcnt(7)\n"
"v_mfma_f32_32x32x16_bf16 v[4:19], v[92:95], v[152:155], v[4:19]\n"
"ds_read_b128 v[92:95], v122 offset:448\n"
"v_lshlrev_b32 v238, 16, v199\n"
"v_and_b32 v239, 0xffff0000, v199\n"
"s_waitcnt lgkmcnt(7)\n"
"v_mfma_f32_32x32x16_bf16 v[4:19], v[96:99], v[156:159], v[4:19]\n"
"ds_read_b128 v[96:99], v122 offset:480\n"
"s_waitcnt lgkmcnt(7)\n"
"v_mfma_f32_32x32x16_bf16 v[4:19], v[68:71], v[160:163], v[4:19]\n"
"s_waitcnt lgkmcnt(6)\n"
"v_mfma_f32_32x32x16_bf16 v[4:19], v[72:75], v[164:167], v[4:19]\n"
"s_waitcnt lgkmcnt(5)\n"
"v_mfma_f32_32x32x16_bf16 v[4:19], v[76:79], v[168:171], v[4:19]\n"
"s_waitcnt lgkmcnt(4)\n"
"v_mfma_f32_32x32x16_bf16 v[4:19], v[80:83], v[172:175], v[4:19]\n"
"s_waitcnt lgkmcnt(3)\n"
"v_mfma_f32_32x32x16_bf16 v[4:19], v[84:87], v[176:179], v[4:19]\n"
"s_waitcnt lgkmcnt(2)\n"
"v_mfma_f32_32x32x16_bf16 v[4:19], v[88:91], v[180:183], v[4:19]\n"
"s_waitcnt lgkmcnt(1)\n"
"v_mfma_f32_32x32x16_bf16 v[4:19], v[92:95], v[184:187], v[4:19]\n"
"s_waitcnt lgkmcnt(0)\n"
"v_mfma_f32_32x32x16_bf16 v[4:19], v[96:99], v[188:191], v[4:19]\n"
"ds_write_b32 v104, v22\n"
"v_perm_b32 v64, v194, v192, s0\n"
"v_perm_b32 v68, v194, v192, s1\n"
"v_perm_b32 v72, v195, v193, s0\n"
"v_perm_b32 v76, v195, v193, s1\n"
"v_perm_b32 v65, v198, v196, s0\n"
"v_perm_b32 v69, v198, v196, s1\n"
"v_perm_b32 v73, v199, v197, s0\n"
"v_perm_b32 v77, v199, v197, s1\n"
"s_nop 2\n"
"v_add_f32 v4, v4, v20\n"
"v_cndmask_b32 v20, v15, v16, vcc\n"
"v_cndmask_b32 v21, v17, v18, vcc\n"
"v_cvt_pk_bf16_f32 v26, v4, v5\n"
"v_cvt_pk_bf16_f32 v27, v6, v7\n"
"v_cvt_pk_bf16_f32 v28, v8, v9\n"
"v_cvt_pk_bf16_f32 v29, v10, v11\n"
"ds_write_b128 v102, v[26:29] offset:32\n"
"v_cvt_pk_bf16_f32 v34, v12, v12\n"
"v_cvt_pk_bf16_f32 v35, v12, v13\n"
"v_cvt_pk_bf16_f32 v36, v13, v13\n"
"v_cvt_pk_bf16_f32 v37, v14, v14\n"
"ds_write_b128 v102, v[34:37] offset:1056\n"
"v_cvt_pk_bf16_f32 v42, v15, v15\n"
"v_cvt_pk_bf16_f32 v43, v15, v15\n"
"v_cvt_pk_bf16_f32 v44, v20, v16\n"
"v_cvt_pk_bf16_f32 v45, v16, v16\n"
"ds_write_b128 v102, v[42:45] offset:2080\n"
"v_cvt_pk_bf16_f32 v50, v17, v17\n"
"v_cvt_pk_bf16_f32 v51, v17, v17\n"
"v_cvt_pk_bf16_f32 v52, v21, v18\n"
"v_cvt_pk_bf16_f32 v53, v18, v18\n"
"ds_write_b128 v102, v[50:53] offset:3104\n"
"ds_read_b64 v[24:25], v102 offset:24\n"
"ds_read_b64 v[32:33], v102 offset:1048\n"
"ds_read_b64 v[40:41], v102 offset:2072\n"
"ds_read_b64 v[48:49], v102 offset:3096\n"
"buffer_load_dwordx4 v[208:211], v114, s[32:35], s98 offen sc1\n"
"buffer_load_dwordx4 v[212:215], v116, s[32:35], s98 offen sc1\n"
"buffer_load_dwordx4 v[216:219], v114, s[32:35], s99 offen sc1\n"
"buffer_load_dwordx4 v[220:223], v116, s[32:35], s99 offen sc1\n"
"s_waitcnt lgkmcnt(3)\n"
"v_alignbit_b32 v128, v25, v24, 16\n"
"v_alignbit_b32 v129, v26, v25, 16\n"
"v_alignbit_b32 v130, v27, v26, 16\n"
"v_alignbit_b32 v131, v28, v27, 16\n"
"s_waitcnt vmcnt(7)\n"
"v_lshlrev_b32 v240, 16, v200\n"
"ds_write_b128 v102, v[128:131] offset:12512\n"
"v_alignbit_b32 v132, v26, v25, 16\n"
"v_alignbit_b32 v133, v27, v26, 16\n"
"v_alignbit_b32 v134, v28, v27, 16\n"
"v_alignbit_b32 v135, v29, v28, 16\n"
"v_and_b32 v241, 0xffff0000, v200\n"
"ds_write_b128 v102, v[132:135] offset:4192\n"
"v_mov_b32 v136, v25\n"
"v_mov_b32 v137, v26\n"
"v_mov_b32 v138, v27\n"
"v_mov_b32 v139, v28\n"
"v_lshlrev_b32 v242, 16, v201\n"
"ds_write_b128 v102, v[136:139] offset:8352\n"
"s_waitcnt lgkmcnt(5)\n"
"v_alignbit_b32 v140, v33, v32, 16\n"
"v_alignbit_b32 v141, v34, v33, 16\n"
"v_alignbit_b32 v142, v35, v34, 16\n"
"v_alignbit_b32 v143, v36, v35, 16\n"
"v_and_b32 v243, 0xffff0000, v201\n"
"ds_write_b128 v102, v[140:143] offset:13536\n"
"v_alignbit_b32 v144, v34, v33, 16\n"
"v_alignbit_b32 v145, v35, v34, 16\n"
"v_alignbit_b32 v146, v36, v35, 16\n"
"v_alignbit_b32 v147, v37, v36, 16\n"
"s_waitcnt vmcnt(6)\n"
"v_lshlrev_b32 v244, 16, v202\n"
"ds_write_b128 v102, v[144:147] offset:5216\n"
"v_mov_b32 v148, v33\n"
"v_mov_b32 v149, v34\n"
"v_mov_b32 v150, v35\n"
"v_mov_b32 v151, v36\n"
"v_and_b32 v245, 0xffff0000, v202\n"
"ds_write_b128 v102, v[148:151] offset:9376\n"
"s_waitcnt lgkmcnt(7)\n"
"v_alignbit_b32 v128, v41, v40, 16\n"
"v_alignbit_b32 v129, v42, v41, 16\n"
"v_alignbit_b32 v130, v43, v42, 16\n"
"v_alignbit_b32 v131, v44, v43, 16\n"
"v_lshlrev_b32 v246, 16, v203\n"
"ds_write_b128 v102, v[128:131] offset:14560\n"
"v_alignbit_b32 v132, v42, v41, 16\n"
"v_alignbit_b32 v133, v43, v42, 16\n"
"v_alignbit_b32 v134, v44, v43, 16\n"
"v_alignbit_b32 v135, v45, v44, 16\n"
"v_and_b32 v247, 0xffff0000, v203\n"
"ds_write_b128 v102, v[132:135] offset:6240\n"
"v_mov_b32 v136, v41\n"
"v_mov_b32 v137, v42\n"
"v_mov_b32 v138, v43\n"
"v_mov_b32 v139, v44\n"
"v_perm_b32 v66, v202, v200, s0\n"
"ds_write_b128 v102, v[136:139] offset:10400\n"
"s_waitcnt lgkmcnt(9)\n"
"v_alignbit_b32 v140, v49, v48, 16\n"
"v_alignbit_b32 v141, v50, v49, 16\n"
"v_alignbit_b32 v142, v51, v50, 16\n"
"v_alignbit_b32 v143, v52, v51, 16\n"
"v_perm_b32 v70, v202, v200, s1\n"
"ds_write_b128 v102, v[140:143] offset:15584\n"
"v_alignbit_b32 v144, v50, v49, 16\n"
"v_alignbit_b32 v145, v51, v50, 16\n"
"v_alignbit_b32 v146, v52, v51, 16\n"
"v_alignbit_b32 v147, v53, v52, 16\n"
"v_perm_b32 v74, v203, v201, s0\n"
"ds_write_b128 v102, v[144:147] offset:7264\n"
"v_mov_b32 v148, v49\n"
"v_mov_b32 v149, v50\n"
"v_mov_b32 v150, v51\n"
"v_mov_b32 v151, v52\n"
"v_perm_b32 v78, v203, v201, s1\n"
"ds_write_b128 v102, v[148:151] offset:11424\n"
"ds_read_b64_tr_b16 v[32:33], v112 offset:8\n"
"ds_read_b64_tr_b16 v[34:35], v112 offset:0\n"
"ds_read_b64_tr_b16 v[36:37], v112 offset:520\n"
"ds_read_b64_tr_b16 v[38:39], v112 offset:512\n"
"ds_read_b64_tr_b16 v[40:41], v112 offset:1032\n"
"ds_read_b64_tr_b16 v[42:43], v112 offset:1024\n"
"ds_read_b64_tr_b16 v[44:45], v112 offset:1544\n"
"ds_read_b64_tr_b16 v[46:47], v112 offset:1536\n"
"ds_read_b64_tr_b16 v[48:49], v112 offset:2056\n"
"ds_read_b64_tr_b16 v[50:51], v112 offset:2048\n"
"ds_read_b64_tr_b16 v[52:53], v112 offset:2568\n"
"ds_read_b64_tr_b16 v[54:55], v112 offset:2560\n"
"ds_read_b64_tr_b16 v[56:57], v112 offset:3080\n"
"ds_read_b64_tr_b16 v[58:59], v112 offset:3072\n"
"ds_read_b64_tr_b16 v[60:61], v112 offset:3592\n"
"ds_read_b64_tr_b16 v[62:63], v112 offset:3584\n"
"s_waitcnt vmcnt(5)\n"
"v_lshlrev_b32 v248, 16, v204\n"
"v_and_b32 v249, 0xffff0000, v204\n"
"v_lshlrev_b32 v250, 16, v205\n"
"v_and_b32 v251, 0xffff0000, v205\n"
"s_waitcnt vmcnt(4)\n"
"v_lshlrev_b32 v252, 16, v206\n"
"v_and_b32 v253, 0xffff0000, v206\n"
"v_lshlrev_b32 v254, 16, v207\n"
"v_and_b32 v255, 0xffff0000, v207\n"
"v_perm_b32 v67, v206, v204, s0\n"
"v_perm_b32 v71, v206, v204, s1\n"
"v_perm_b32 v75, v207, v205, s0\n"
"v_perm_b32 v79, v207, v205, s1\n"
"s_waitcnt vmcnt(3)\n"
"v_xor_b32 v124, s31, v209\n"
"v_bitop3_b32 v124, v124, v211, s31 bitop3:0xf6\n"
"s_waitcnt vmcnt(2)\n"
"v_bitop3_b32 v124, v124, v213, s31 bitop3:0xf6\n"
"v_bitop3_b32 v124, v124, v215, s31 bitop3:0xf6\n"
"s_waitcnt vmcnt(1)\n"
"v_bitop3_b32 v124, v124, v217, s31 bitop3:0xf6\n"
"v_bitop3_b32 v124, v124, v219, s31 bitop3:0xf6\n"
"s_waitcnt vmcnt(0)\n"
"v_bitop3_b32 v124, v124, v221, s31 bitop3:0xf6\n"
"v_bitop3_b32 v124, v124, v223, s31 bitop3:0xf6\n"
"v_cmp_ne_u32 vcc, 0, v124\n"
"s_cbranch_vccnz .Lmg_gslow\n"
".Lmg_gret:\n"
"v_and_b32 v209, 0xffff0000, v208\n"
"v_lshlrev_b32 v208, 16, v208\n"
"v_and_b32 v211, 0xffff0000, v210\n"
"v_lshlrev_b32 v210, 16, v210\n"
"v_and_b32 v213, 0xffff0000, v212\n"
"v_lshlrev_b32 v212, 16, v212\n"
"v_and_b32 v215, 0xffff0000, v214\n"
"v_lshlrev_b32 v214, 16, v214\n"
"v_and_b32 v217, 0xffff0000, v216\n"
"v_lshlrev_b32 v216, 16, v216\n"
"v_and_b32 v219, 0xffff0000, v218\n"
"v_lshlrev_b32 v218, 16, v218\n"
"v_and_b32 v221, 0xffff0000, v220\n"
"v_lshlrev_b32 v220, 16, v220\n"
"v_and_b32 v223, 0xffff0000, v222\n"
"v_lshlrev_b32 v222, 16, v222\n"
"v_rcp_f32 v209, v209\n"
"v_rcp_f32 v211, v211\n"
"v_rcp_f32 v213, v213\n"
"v_rcp_f32 v215, v215\n"
"v_rcp_f32 v217, v217\n"
"v_rcp_f32 v219, v219\n"
"v_rcp_f32 v221, v221\n"
"v_rcp_f32 v223, v223\n"
"v_mul_f32 v208, v208, v209\n"
"v_mul_f32 v210, v210, v211\n"
"v_mul_f32 v212, v212, v213\n"
"v_mul_f32 v214, v214, v215\n"
"v_mul_f32 v216, v216, v217\n"
"v_mul_f32 v218, v218, v219\n"
"v_mul_f32 v220, v220, v221\n"
"v_mul_f32 v222, v222, v223\n"
"v_pk_fma_f32 v[224:225], v[224:225], v[208:209], v[208:209] op_sel:[0,1,0] op_sel_hi:[1,1,0] neg_lo:[0,0,1] neg_hi:[0,0,1]\n"
"v_pk_fma_f32 v[228:229], v[228:229], v[210:211], v[210:211] op_sel:[0,1,0] op_sel_hi:[1,1,0] neg_lo:[0,0,1] neg_hi:[0,0,1]\n"
"v_pk_fma_f32 v[232:233], v[232:233], v[212:213], v[212:213] op_sel:[0,1,0] op_sel_hi:[1,1,0] neg_lo:[0,0,1] neg_hi:[0,0,1]\n"
"v_pk_fma_f32 v[236:237], v[236:237], v[214:215], v[214:215] op_sel:[0,1,0] op_sel_hi:[1,1,0] neg_lo:[0,0,1] neg_hi:[0,0,1]\n"
"v_pk_fma_f32 v[240:241], v[240:241], v[216:217], v[216:217] op_sel:[0,1,0] op_sel_hi:[1,1,0] neg_lo:[0,0,1] neg_hi:[0,0,1]\n"
"v_pk_fma_f32 v[244:245], v[244:245], v[218:219], v[218:219] op_sel:[0,1,0] op_sel_hi:[1,1,0] neg_lo:[0,0,1] neg_hi:[0,0,1]\n"
"v_pk_fma_f32 v[248:249], v[248:249], v[220:221], v[220:221] op_sel:[0,1,0] op_sel_hi:[1,1,0] neg_lo:[0,0,1] neg_hi:[0,0,1]\n"
"v_pk_fma_f32 v[252:253], v[252:253], v[222:223], v[222:223] op_sel:[0,1,0] op_sel_hi:[1,1,0] neg_lo:[0,0,1] neg_hi:[0,0,1]\n"
"v_cvt_pk_bf16_f32 v192, v224, v228\n"
"v_cvt_pk_bf16_f32 v193, v232, v236\n"
"v_cvt_pk_bf16_f32 v194, v240, v244\n"
"v_cvt_pk_bf16_f32 v195, v248, v252\n"
"ds_write_b128 v115, v[192:195] offset:0\n"
"v_cvt_pk_bf16_f32 v196, v225, v229\n"
"v_cvt_pk_bf16_f32 v197, v233, v237\n"
"v_cvt_pk_bf16_f32 v198, v241, v245\n"
"v_cvt_pk_bf16_f32 v199, v249, v253\n"
"ds_write_b128 v115, v[196:199] offset:4608\n"
"v_pk_fma_f32 v[226:227], v[226:227], v[208:209], v[208:209] op_sel:[0,1,0] op_sel_hi:[1,1,0] neg_lo:[0,0,1] neg_hi:[0,0,1]\n"
"v_pk_fma_f32 v[230:231], v[230:231], v[210:211], v[210:211] op_sel:[0,1,0] op_sel_hi:[1,1,0] neg_lo:[0,0,1] neg_hi:[0,0,1]\n"
"v_pk_fma_f32 v[234:235], v[234:235], v[212:213], v[212:213] op_sel:[0,1,0] op_sel_hi:[1,1,0] neg_lo:[0,0,1] neg_hi:[0,0,1]\n"
"v_pk_fma_f32 v[238:239], v[238:239], v[214:215], v[214:215] op_sel:[0,1,0] op_sel_hi:[1,1,0] neg_lo:[0,0,1] neg_hi:[0,0,1]\n"
"v_pk_fma_f32 v[242:243], v[242:243], v[216:217], v[216:217] op_sel:[0,1,0] op_sel_hi:[1,1,0] neg_lo:[0,0,1] neg_hi:[0,0,1]\n"
"v_pk_fma_f32 v[246:247], v[246:247], v[218:219], v[218:219] op_sel:[0,1,0] op_sel_hi:[1,1,0] neg_lo:[0,0,1] neg_hi:[0,0,1]\n"
"v_pk_fma_f32 v[250:251], v[250:251], v[220:221], v[220:221] op_sel:[0,1,0] op_sel_hi:[1,1,0] neg_lo:[0,0,1] neg_hi:[0,0,1]\n"
"v_pk_fma_f32 v[254:255], v[254:255], v[222:223], v[222:223] op_sel:[0,1,0] op_sel_hi:[1,1,0] neg_lo:[0,0,1] neg_hi:[0,0,1]\n"
"v_cvt_pk_bf16_f32 v200, v226, v230\n"
"v_cvt_pk_bf16_f32 v201, v234, v238\n"
"v_cvt_pk_bf16_f32 v202, v242, v246\n"
"v_cvt_pk_bf16_f32 v203, v250, v254\n"
"ds_write_b128 v115, v[200:203] offset:9216\n"
"v_cvt_pk_bf16_f32 v204, v227, v231\n"
"v_cvt_pk_bf16_f32 v205, v235, v239\n"
"v_cvt_pk_bf16_f32 v206, v243, v247\n"
"v_cvt_pk_bf16_f32 v207, v251, v255\n"
"ds_write_b128 v115, v[204:207] offset:13824\n"
"v_min_u32 v116, 496, v127\n"
"v_add_u32 v116, 1024, v116\n"
"buffer_load_dword v124, v127, s[32:35], s54 offen sc1\n"
"buffer_load_dword v125, v116, s[32:35], s54 offen sc1\n"
"s_waitcnt lgkmcnt(0)\n"
"s_barrier\n"
"ds_read_b128 v[80:83], v113 offset:448\n"
"ds_read_b128 v[84:87], v113 offset:960\n"
"ds_read_b128 v[88:91], v113 offset:1472\n"
"ds_read_b128 v[92:95], v113 offset:1984\n"
"s_waitcnt lgkmcnt(3)\n"
"v_mfma_f32_16x16x32_bf16 v[0:3], v[32:35], v[80:83], 0\n"
"ds_read_b128 v[96:99], v113 offset:2496\n"
"s_waitcnt lgkmcnt(3)\n"
"v_mfma_f32_16x16x32_bf16 v[4:7], v[32:35], v[84:87], 0\n"
"ds_read_b128 v[100:103], v113 offset:3008\n"
"v_mfma_f32_16x16x32_bf16 v[4:7], v[36:39], v[80:83], v[4:7]\n"
"ds_read_b128 v[104:107], v113 offset:3520\n"
"s_waitcnt lgkmcnt(4)\n"
"v_mfma_f32_16x16x32_bf16 v[8:11], v[32:35], v[88:91], 0\n"
"ds_read_b128 v[108:111], v113 offset:4032\n"
"v_mfma_f32_16x16x32_bf16 v[8:11], v[36:39], v[84:87], v[8:11]\n"
"v_mfma_f32_16x16x32_bf16 v[8:11], v[40:43], v[80:83], v[8:11]\n"
"s_waitcnt lgkmcnt(4)\n"
"v_mfma_f32_16x16x32_bf16 v[12:15], v[32:35], v[92:95], 0\n"
"ds_read_b64_tr_b16 v[128:129], v112 offset:72\n"
"v_mfma_f32_16x16x32_bf16 v[12:15], v[36:39], v[88:91], v[12:15]\n"
"ds_read_b64_tr_b16 v[130:131], v112 offset:64\n"
"v_mfma_f32_16x16x32_bf16 v[12:15], v[40:43], v[84:87], v[12:15]\n"
"ds_read_b128 v[160:163], v113 offset:384\n"
"v_mfma_f32_16x16x32_bf16 v[12:15], v[44:47], v[80:83], v[12:15]\n"
"ds_read_b64_tr_b16 v[132:133], v112 offset:584\n"
"s_waitcnt lgkmcnt(7)\n"
"v_mfma_f32_16x16x32_bf16 v[16:19], v[32:35], v[96:99], 0\n"
"ds_read_b64_tr_b16 v[134:135], v112 offset:576\n"
"v_mfma_f32_16x16x32_bf16 v[16:19], v[36:39], v[92:95], v[16:19]\n"
"ds_read_b128 v[164:167], v113 offset:896\n"
"v_mfma_f32_16x16x32_bf16 v[16:19], v[40:43], v[88:91], v[16:19]\n"
"ds_read_b64_tr_b16 v[136:137], v112 offset:1096\n"
"v_mfma_f32_16x16x32_bf16 v[16:19], v[44:47], v[84:87], v[16:19]\n"
"ds_read_b64_tr_b16 v[138:139], v112 offset:1088\n"
"v_mfma_f32_16x16x32_bf16 v[16:19], v[48:51], v[80:83], v[16:19]\n"
"ds_read_b128 v[168:171], v113 offset:1408\n"
"s_waitcnt lgkmcnt(11)\n"
"v_mfma_f32_16x16x32_bf16 v[20:23], v[32:35], v[100:103], 0\n"
"ds_read_b64_tr_b16 v[140:141], v112 offset:1608\n"
"v_mfma_f32_16x16x32_bf16 v[20:23], v[36:39], v[96:99], v[20:23]\n"
"ds_read_b64_tr_b16 v[142:143], v112 offset:1600\n"
"v_mfma_f32_16x16x32_bf16 v[20:23], v[40:43], v[92:95], v[20:23]\n"
"ds_read_b128 v[172:175], v113 offset:1920\n"
"v_mfma_f32_16x16x32_bf16 v[20:23], v[44:47], v[88:91], v[20:23]\n"
"ds_read_b64_tr_b16 v[144:145], v112 offset:2120\n"
"s_waitcnt vmcnt(1)\n"
"v_cmp_ne_u32 vcc, s31, v124\n"
"s_cbranch_vccnz .Lmg_slow1\n"
".Lmg_ret1:\n"
"v_mfma_f32_16x16x32_bf16 v[20:23], v[48:51], v[84:87], v[20:23]\n"
"ds_read_b64_tr_b16 v[146:147], v112 offset:2112\n"
"s_waitcnt vmcnt(0)\n"
"v_cmp_ne_u32 vcc, s31, v125\n"
"s_cbranch_vccnz .Lmg_slow2\n"
".Lmg_ret2:\n"
"v_mfma_f32_16x16x32_bf16 v[20:23], v[52:55], v[80:83], v[20:23]\n"
"ds_read_b128 v[176:179], v113 offset:2432\n"
"s_waitcnt lgkmcnt(14)\n"
"v_mfma_f32_16x16x32_bf16 v[24:27], v[32:35], v[104:107], 0\n"
"ds_read_b64_tr_b16 v[148:149], v112 offset:2632\n"
"v_mfma_f32_16x16x32_bf16 v[24:27], v[36:39], v[100:103], v[24:27]\n"
"ds_read_b64_tr_b16 v[150:151], v112 offset:2624\n"
"v_mfma_f32_16x16x32_bf16 v[24:27], v[40:43], v[96:99], v[24:27]\n"
"buffer_load_dwordx4 v[192:195], v127, s[36:39], s14 offen offset:0 sc1\n"
"v_mfma_f32_16x16x32_bf16 v[24:27], v[44:47], v[92:95], v[24:27]\n"
"ds_read_b128 v[180:183], v113 offset:2944\n"
"v_mfma_f32_16x16x32_bf16 v[24:27], v[48:51], v[88:91], v[24:27]\n"
"ds_read_b64_tr_b16 v[152:153], v112 offset:3144\n"
"v_mfma_f32_16x16x32_bf16 v[24:27], v[52:55], v[84:87], v[24:27]\n"
"ds_read_b64_tr_b16 v[154:155], v112 offset:3136\n"
"v_mfma_f32_16x16x32_bf16 v[24:27], v[56:59], v[80:83], v[24:27]\n"
"buffer_load_dwordx4 v[196:199], v127, s[36:39], s14 offen offset:1024 sc1\n"
"v_mfma_f32_16x16x32_bf16 v[28:31], v[32:35], v[108:111], 0\n"
"ds_read_b128 v[184:187], v113 offset:3456\n"
"v_mfma_f32_16x16x32_bf16 v[28:31], v[36:39], v[104:107], v[28:31]\n"
"ds_read_b64_tr_b16 v[156:157], v112 offset:3656\n"
"v_mfma_f32_16x16x32_bf16 v[28:31], v[40:43], v[100:103], v[28:31]\n"
"ds_read_b64_tr_b16 v[158:159], v112 offset:3648\n"
"v_mfma_f32_16x16x32_bf16 v[28:31], v[44:47], v[96:99], v[28:31]\n"
"buffer_load_dwordx4 v[200:203], v127, s[36:39], s14 offen offset:2048 sc1\n"
"v_mfma_f32_16x16x32_bf16 v[28:31], v[48:51], v[92:95], v[28:31]\n"
".Lmg_hopS2:\n"
"s_cbranch_execz .Lmg_conv0\n"
"ds_read_b128 v[188:191], v113 offset:3968\n"
"v_mfma_f32_16x16x32_bf16 v[28:31], v[52:55], v[88:91], v[28:31]\n"
"v_mfma_f32_16x16x32_bf16 v[28:31], v[56:59], v[84:87], v[28:31]\n"
"v_mfma_f32_16x16x32_bf16 v[28:31], v[60:63], v[80:83], v[28:31]\n"
"buffer_load_dwordx4 v[204:207], v127, s[36:39], s14 offen offset:3072 sc1\n"
"s_waitcnt lgkmcnt(14)\n"
"v_mfma_f32_16x16x32_bf16 v[0:3], v[128:131], v[160:163], v[0:3]\n"
"ds_read_b64_tr_b16 v[32:33], v112 offset:136\n"
"v_mfma_f32_16x16x32_bf16 v[4:7], v[128:131], v[164:167], v[4:7]\n"
"ds_read_b64_tr_b16 v[34:35], v112 offset:128\n"
"v_mfma_f32_16x16x32_bf16 v[4:7], v[132:135], v[160:163], v[4:7]\n"
"ds_read_b128 v[80:83], v113 offset:320\n"
"v_mfma_f32_16x16x32_bf16 v[8:11], v[128:131], v[168:171], v[8:11]\n"
"buffer_load_dwordx4 v[208:211], v127, s[36:39], s15 offen offset:0 sc1\n"
"v_mfma_f32_16x16x32_bf16 v[8:11], v[132:135], v[164:167], v[8:11]\n"
"ds_read_b64_tr_b16 v[36:37], v112 offset:648\n"
"v_mfma_f32_16x16x32_bf16 v[8:11], v[136:139], v[160:163], v[8:11]\n"
"ds_read_b64_tr_b16 v[38:39], v112 offset:640\n"
"s_waitcnt lgkmcnt(14)\n"
"v_mfma_f32_16x16x32_bf16 v[12:15], v[128:131], v[172:175], v[12:15]\n"
"ds_read_b128 v[84:87], v113 offset:832\n"
"v_mfma_f32_16x16x32_bf16 v[12:15], v[132:135], v[168:171], v[12:15]\n"
"buffer_load_dwordx4 v[212:215], v127, s[36:39], s15 offen offset:1024 sc1\n"
"v_mfma_f32_16x16x32_bf16 v[12:15], v[136:139], v[164:167], v[12:15]\n"
"ds_read_b64_tr_b16 v[40:41], v112 offset:1160\n"
"v_mfma_f32_16x16x32_bf16 v[12:15], v[140:143], v[160:163], v[12:15]\n"
"ds_read_b64_tr_b16 v[42:43], v112 offset:1152\n"
"v_mfma_f32_16x16x32_bf16 v[16:19], v[128:131], v[176:179], v[16:19]\n"
"ds_read_b128 v[88:91], v113 offset:1344\n"
"v_mfma_f32_16x16x32_bf16 v[16:19], v[132:135], v[172:175], v[16:19]\n"
"buffer_load_dwordx4 v[216:219], v127, s[36:39], s15 offen offset:2048 sc1\n"
"v_mfma_f32_16x16x32_bf16 v[16:19], v[136:139], v[168:171], v[16:19]\n"
"ds_read_b64_tr_b16 v[44:45], v112 offset:1672\n"
"v_mfma_f32_16x16x32_bf16 v[16:19], v[140:143], v[164:167], v[16:19]\n"
"ds_read_b64_tr_b16 v[46:47], v112 offset:1664\n"
"v_mfma_f32_16x16x32_bf16 v[16:19], v[144:147], v[160:163], v[16:19]\n"
"ds_read_b128 v[92:95], v113 offset:1856\n"
"s_waitcnt lgkmcnt(14)\n"
"v_mfma_f32_16x16x32_bf16 v[20:23], v[128:131], v[180:183], v[20:23]\n"
"buffer_load_dwordx4 v[220:223], v127, s[36:39], s15 offen offset:3072 sc1\n"
"v_mfma_f32_16x16x32_bf16 v[20:23], v[132:135], v[176:179], v[20:23]\n"
"ds_read_b64_tr_b16 v[48:49], v112 offset:2184\n"
"v_mfma_f32_16x16x32_bf16 v[20:23], v[136:139], v[172:175], v[20:23]\n"
"ds_read_b64_tr_b16 v[50:51], v112 offset:2176\n"
"v_mfma_f32_16x16x32_bf16 v[20:23], v[140:143], v[168:171], v[20:23]\n"
"ds_read_b128 v[96:99], v113 offset:2368\n"
"v_mfma_f32_16x16x32_bf16 v[20:23], v[144:147], v[164:167], v[20:23]\n"
"buffer_load_dwordx4 v[224:227], v127, s[36:39], s16 offen offset:0 sc1\n"
"v_mfma_f32_16x16x32_bf16 v[20:23], v[148:151], v[160:163], v[20:23]\n"
"ds_read_b64_tr_b16 v[52:53], v112 offset:2696\n"
"v_mfma_f32_16x16x32_bf16 v[24:27], v[128:131], v[184:187], v[24:27]\n"
"ds_read_b64_tr_b16 v[54:55], v112 offset:2688\n"
"v_mfma_f32_16x16x32_bf16 v[24:27], v[132:135], v[180:183], v[24:27]\n"
"ds_read_b128 v[100:103], v113 offset:2880\n"
"v_mfma_f32_16x16x32_bf16 v[24:27], v[136:139], v[176:179], v[24:27]\n"
"buffer_load_dwordx4 v[228:231], v127, s[36:39], s16 offen offset:1024 sc1\n"
"v_mfma_f32_16x16x32_bf16 v[24:27], v[140:143], v[172:175], v[24:27]\n"
"ds_read_b64_tr_b16 v[56:57], v112 offset:3208\n"
"v_mfma_f32_16x16x32_bf16 v[24:27], v[144:147], v[168:171], v[24:27]\n"
"ds_read_b64_tr_b16 v[58:59], v112 offset:3200\n"
"v_mfma_f32_16x16x32_bf16 v[24:27], v[148:151], v[164:167], v[24:27]\n"
"ds_read_b128 v[104:107], v113 offset:3392\n"
"v_mfma_f32_16x16x32_bf16 v[24:27], v[152:155], v[160:163], v[24:27]\n"
"buffer_load_dwordx4 v[232:235], v127, s[36:39], s16 offen offset:2048 sc1\n"
"s_waitcnt lgkmcnt(14)\n"
"v_mfma_f32_16x16x32_bf16 v[28:31], v[128:131], v[188:191], v[28:31]\n"
"ds_read_b64_tr_b16 v[60:61], v112 offset:3720\n"
"v_mfma_f32_16x16x32_bf16 v[28:31], v[132:135], v[184:187], v[28:31]\n"
"ds_read_b64_tr_b16 v[62:63], v112 offset:3712\n"
"v_mfma_f32_16x16x32_bf16 v[28:31], v[136:139], v[180:183], v[28:31]\n"
"ds_read_b128 v[108:111], v113 offset:3904\n"
"v_mfma_f32_16x16x32_bf16 v[28:31], v[140:143], v[176:179], v[28:31]\n"
"buffer_load_dwordx4 v[236:239], v127, s[36:39], s16 offen offset:3072 sc1\n"
"v_mfma_f32_16x16x32_bf16 v[28:31], v[144:147], v[172:175], v[28:31]\n"
"ds_write_b128 v115, v[64:67] offset:17920\n"
"v_mfma_f32_16x16x32_bf16 v[28:31], v[148:151], v[168:171], v[28:31]\n"
"s_cmp_eq_u32 s45, 0\n"
"s_cselect_b32 s53, 8, 0\n"
"v_mfma_f32_16x16x32_bf16 v[28:31], v[152:155], v[164:167], v[28:31]\n"
"ds_write_b128 v115, v[68:71] offset:22016\n"
"v_mfma_f32_16x16x32_bf16 v[28:31], v[156:159], v[160:163], v[28:31]\n"
"buffer_load_dwordx4 v[240:243], v127, s[36:39], s17 offen offset:0 sc1\n"
"s_waitcnt lgkmcnt(0)\n"
"v_mfma_f32_16x16x32_bf16 v[0:3], v[32:35], v[80:83], v[0:3]\n"
"ds_read_b64_tr_b16 v[128:129], v112 offset:200\n"
"v_mfma_f32_16x16x32_bf16 v[4:7], v[32:35], v[84:87], v[4:7]\n"
"ds_read_b64_tr_b16 v[130:131], v112 offset:192\n"
"v_mfma_f32_16x16x32_bf16 v[4:7], v[36:39], v[80:83], v[4:7]\n"
"ds_read_b128 v[160:163], v113 offset:256\n"
"v_mfma_f32_16x16x32_bf16 v[8:11], v[32:35], v[88:91], v[8:11]\n"
"buffer_load_dwordx4 v[244:247], v127, s[36:39], s17 offen offset:1024 sc1\n"
"v_mfma_f32_16x16x32_bf16 v[8:11], v[36:39], v[84:87], v[8:11]\n"
"ds_read_b64_tr_b16 v[132:133], v112 offset:712\n"
"v_mfma_f32_16x16x32_bf16 v[8:11], v[40:43], v[80:83], v[8:11]\n"
"ds_read_b64_tr_b16 v[134:135], v112 offset:704\n"
"v_mfma_f32_16x16x32_bf16 v[12:15], v[32:35], v[92:95], v[12:15]\n"
"ds_read_b128 v[164:167], v113 offset:768\n"
"v_mfma_f32_16x16x32_bf16 v[12:15], v[36:39], v[88:91], v[12:15]\n"
"buffer_load_dwordx4 v[248:251], v127, s[36:39], s17 offen offset:2048 sc1\n"
"v_mfma_f32_16x16x32_bf16 v[12:15], v[40:43], v[84:87], v[12:15]\n"
"ds_read_b64_tr_b16 v[136:137], v112 offset:1224\n"
"v_mfma_f32_16x16x32_bf16 v[12:15], v[44:47], v[80:83], v[12:15]\n"
"ds_read_b64_tr_b16 v[138:139], v112 offset:1216\n"
"v_mfma_f32_16x16x32_bf16 v[16:19], v[32:35], v[96:99], v[16:19]\n"
"ds_read_b128 v[168:171], v113 offset:1280\n"
"v_mfma_f32_16x16x32_bf16 v[16:19], v[36:39], v[92:95], v[16:19]\n"
"buffer_load_dwordx4 v[252:255], v127, s[36:39], s17 offen offset:3072 sc1\n"
"v_mfma_f32_16x16x32_bf16 v[16:19], v[40:43], v[88:91], v[16:19]\n"
"ds_read_b64_tr_b16 v[140:141], v112 offset:1736\n"
"v_mfma_f32_16x16x32_bf16 v[16:19], v[44:47], v[84:87], v[16:19]\n"
"ds_read_b64_tr_b16 v[142:143], v112 offset:1728\n"
"v_mfma_f32_16x16x32_bf16 v[16:19], v[48:51], v[80:83], v[16:19]\n"
"ds_read_b128 v[172:175], v113 offset:1792\n"
"v_mfma_f32_16x16x32_bf16 v[20:23], v[32:35], v[100:103], v[20:23]\n"
"buffer_load_dwordx4 a[160:163], v127, s[36:39], s18 offen offset:0 sc1\n"
"v_mfma_f32_16x16x32_bf16 v[20:23], v[36:39], v[96:99], v[20:23]\n"
"ds_read_b64_tr_b16 v[144:145], v112 offset:2248\n"
"v_mfma_f32_16x16x32_bf16 v[20:23], v[40:43], v[92:95], v[20:23]\n"
"ds_read_b64_tr_b16 v[146:147], v112 offset:2240\n"
"v_mfma_f32_16x16x32_bf16 v[20:23], v[44:47], v[88:91], v[20:23]\n"
"ds_read_b128 v[176:179], v113 offset:2304\n"
"v_mfma_f32_16x16x32_bf16 v[20:23], v[48:51], v[84:87], v[20:23]\n"
"buffer_load_dwordx4 a[164:167], v127, s[36:39], s18 offen offset:1024 sc1\n"
"v_mfma_f32_16x16x32_bf16 v[20:23], v[52:55], v[80:83], v[20:23]\n"
"ds_read_b64_tr_b16 v[148:149], v112 offset:2760\n"
"v_mfma_f32_16x16x32_bf16 v[24:27], v[32:35], v[104:107], v[24:27]\n"
"ds_read_b64_tr_b16 v[150:151], v112 offset:2752\n"
"v_mfma_f32_16x16x32_bf16 v[24:27], v[36:39], v[100:103], v[24:27]\n"
"ds_read_b128 v[180:183], v113 offset:2816\n"
"v_mfma_f32_16x16x32_bf16 v[24:27], v[40:43], v[96:99], v[24:27]\n"
"buffer_load_dwordx4 a[168:171], v127, s[36:39], s18 offen offset:2048 sc1\n"
"v_mfma_f32_16x16x32_bf16 v[24:27], v[44:47], v[92:95], v[24:27]\n"
"ds_read_b64_tr_b16 v[152:153], v112 offset:3272\n"
"v_mfma_f32_16x16x32_bf16 v[24:27], v[48:51], v[88:91], v[24:27]\n"
"ds_read_b64_tr_b16 v[154:155], v112 offset:3264\n"
"v_mfma_f32_16x16x32_bf16 v[24:27], v[52:55], v[84:87], v[24:27]\n"
"ds_read_b128 v[184:187], v113 offset:3328\n"
"v_mfma_f32_16x16x32_bf16 v[24:27], v[56:59], v[80:83], v[24:27]\n"
"buffer_load_dwordx4 a[172:175], v127, s[36:39], s18 offen offset:3072 sc1\n"
"v_mfma_f32_16x16x32_bf16 v[28:31], v[32:35], v[108:111], v[28:31]\n"
"ds_read_b64_tr_b16 v[156:157], v112 offset:3784\n"
"v_mfma_f32_16x16x32_bf16 v[28:31], v[36:39], v[104:107], v[28:31]\n"
"ds_read_b64_tr_b16 v[158:159], v112 offset:3776\n"
"v_mfma_f32_16x16x32_bf16 v[28:31], v[40:43], v[100:103], v[28:31]\n"
"ds_read_b128 v[188:191], v113 offset:3840\n"
"v_mfma_f32_16x16x32_bf16 v[28:31], v[44:47], v[96:99], v[28:31]\n"
"buffer_load_dwordx4 a[176:179], v127, s[36:39], s19 offen offset:0 sc1\n"
"v_mfma_f32_16x16x32_bf16 v[28:31], v[48:51], v[92:95], v[28:31]\n"
"ds_write_b128 v115, v[72:75] offset:26112\n"
"v_mfma_f32_16x16x32_bf16 v[28:31], v[52:55], v[88:91], v[28:31]\n"
"v_cmp_gt_u32 vcc, s53, v126\n"
"v_lshlrev_b32 v114, 10, v126\n"
"v_mfma_f32_16x16x32_bf16 v[28:31], v[56:59], v[84:87], v[28:31]\n"
"ds_write_b128 v115, v[76:79] offset:30208\n"
"v_mfma_f32_16x16x32_bf16 v[28:31], v[60:63], v[80:83], v[28:31]\n"
"buffer_load_dwordx4 a[180:183], v127, s[36:39], s19 offen offset:1024 sc1\n"
"s_waitcnt lgkmcnt(0)\n"
"s_barrier\n"
"v_mfma_f32_16x16x32_bf16 v[0:3], v[128:131], v[160:163], v[0:3]\n"
"ds_read_b64_tr_b16 v[32:33], v112 offset:264\n"
"v_bfrev_b32 v120, 2\n"
"v_mfma_f32_16x16x32_bf16 v[4:7], v[128:131], v[164:167], v[4:7]\n"
"ds_read_b64_tr_b16 v[34:35], v112 offset:256\n"
"v_cndmask_b32 v114, v120, v114, vcc\n"
"v_mfma_f32_16x16x32_bf16 v[4:7], v[132:135], v[160:163], v[4:7]\n"
"ds_read_b128 v[80:83], v113 offset:192\n"
"s_lshl_b32 s53, s44, 7\n"
"v_mfma_f32_16x16x32_bf16 v[8:11], v[128:131], v[168:171], v[8:11]\n"
"buffer_load_dwordx4 a[184:187], v127, s[36:39], s19 offen offset:2048 sc1\n"
"v_mfma_f32_16x16x32_bf16 v[8:11], v[132:135], v[164:167], v[8:11]\n"
"ds_read_b64_tr_b16 v[36:37], v112 offset:776\n"
"s_add_u32 s53, s53, 327680\n"
"v_mfma_f32_16x16x32_bf16 v[8:11], v[136:139], v[160:163], v[8:11]\n"
"ds_read_b64_tr_b16 v[38:39], v112 offset:768\n"
"v_mfma_f32_16x16x32_bf16 v[12:15], v[128:131], v[172:175], v[12:15]\n"
"ds_read_b128 v[84:87], v113 offset:704\n"
"v_mfma_f32_16x16x32_bf16 v[12:15], v[132:135], v[168:171], v[12:15]\n"
"buffer_load_dwordx4 a[188:191], v127, s[36:39], s19 offen offset:3072 sc1\n"
"v_mfma_f32_16x16x32_bf16 v[12:15], v[136:139], v[164:167], v[12:15]\n"
"ds_read_b64_tr_b16 v[40:41], v112 offset:1288\n"
"v_mfma_f32_16x16x32_bf16 v[12:15], v[140:143], v[160:163], v[12:15]\n"
"ds_read_b64_tr_b16 v[42:43], v112 offset:1280\n"
"v_mfma_f32_16x16x32_bf16 v[16:19], v[128:131], v[176:179], v[16:19]\n"
"ds_read_b128 v[88:91], v113 offset:1216\n"
"v_mfma_f32_16x16x32_bf16 v[16:19], v[132:135], v[172:175], v[16:19]\n"
"buffer_load_dwordx4 a[192:195], v127, s[36:39], s20 offen offset:0 sc1\n"
"v_mfma_f32_16x16x32_bf16 v[16:19], v[136:139], v[168:171], v[16:19]\n"
"ds_read_b64_tr_b16 v[44:45], v112 offset:1800\n"
"v_mfma_f32_16x16x32_bf16 v[16:19], v[140:143], v[164:167], v[16:19]\n"
"ds_read_b64_tr_b16 v[46:47], v112 offset:1792\n"
"v_mfma_f32_16x16x32_bf16 v[16:19], v[144:147], v[160:163], v[16:19]\n"
"ds_read_b128 v[92:95], v113 offset:1728\n"
"v_mfma_f32_16x16x32_bf16 v[20:23], v[128:131], v[180:183], v[20:23]\n"
"buffer_load_dwordx4 a[196:199], v127, s[36:39], s20 offen offset:1024 sc1\n"
"v_mfma_f32_16x16x32_bf16 v[20:23], v[132:135], v[176:179], v[20:23]\n"
"ds_read_b64_tr_b16 v[48:49], v112 offset:2312\n"
"v_mfma_f32_16x16x32_bf16 v[20:23], v[136:139], v[172:175], v[20:23]\n"
"ds_read_b64_tr_b16 v[50:51], v112 offset:2304\n"
"v_mfma_f32_16x16x32_bf16 v[20:23], v[140:143], v[168:171], v[20:23]\n"
"ds_read_b128 v[96:99], v113 offset:2240\n"
"v_mfma_f32_16x16x32_bf16 v[20:23], v[144:147], v[164:167], v[20:23]\n"
"buffer_load_dwordx4 a[200:203], v127, s[36:39], s20 offen offset:2048 sc1\n"
"v_mfma_f32_16x16x32_bf16 v[20:23], v[148:151], v[160:163], v[20:23]\n"
"ds_read_b64_tr_b16 v[52:53], v112 offset:2824\n"
"v_mfma_f32_16x16x32_bf16 v[24:27], v[128:131], v[184:187], v[24:27]\n"
"ds_read_b64_tr_b16 v[54:55], v112 offset:2816\n"
"v_mfma_f32_16x16x32_bf16 v[24:27], v[132:135], v[180:183], v[24:27]\n"
"ds_read_b128 v[100:103], v113 offset:2752\n"
"v_mfma_f32_16x16x32_bf16 v[24:27], v[136:139], v[176:179], v[24:27]\n"
"buffer_load_dwordx4 a[204:207], v127, s[36:39], s20 offen offset:3072 sc1\n"
"v_mfma_f32_16x16x32_bf16 v[24:27], v[140:143], v[172:175], v[24:27]\n"
"ds_read_b64_tr_b16 v[56:57], v112 offset:3336\n"
"v_mfma_f32_16x16x32_bf16 v[24:27], v[144:147], v[168:171], v[24:27]\n"
"ds_read_b64_tr_b16 v[58:59], v112 offset:3328\n"
"v_mfma_f32_16x16x32_bf16 v[24:27], v[148:151], v[164:167], v[24:27]\n"
"ds_read_b128 v[104:107], v113 offset:3264\n"
"v_mfma_f32_16x16x32_bf16 v[24:27], v[152:155], v[160:163], v[24:27]\n"
"buffer_load_dwordx4 a[208:211], v127, s[36:39], s21 offen offset:0 sc1\n"
"v_mfma_f32_16x16x32_bf16 v[28:31], v[128:131], v[188:191], v[28:31]\n"
"ds_read_b64_tr_b16 v[60:61], v112 offset:3848\n"
"v_mfma_f32_16x16x32_bf16 v[28:31], v[132:135], v[184:187], v[28:31]\n"
"ds_read_b64_tr_b16 v[62:63], v112 offset:3840\n"
"v_mfma_f32_16x16x32_bf16 v[28:31], v[136:139], v[180:183], v[28:31]\n"
"ds_read_b128 v[108:111], v113 offset:3776\n"
"v_mfma_f32_16x16x32_bf16 v[28:31], v[140:143], v[176:179], v[28:31]\n"
"buffer_load_dwordx4 a[212:215], v127, s[36:39], s21 offen offset:1024 sc1\n"
"v_mfma_f32_16x16x32_bf16 v[28:31], v[144:147], v[172:175], v[28:31]\n"
"v_bfe_u32 v68, v126, 4, 2\n"
"v_and_b32 v69, 15, v126\n"
"v_mfma_f32_16x16x32_bf16 v[28:31], v[148:151], v[168:171], v[28:31]\n"
"v_lshlrev_b32 v68, 3, v68\n"
"v_sub_u32 v68, v69, v68\n"
"v_mfma_f32_16x16x32_bf16 v[28:31], v[152:155], v[164:167], v[28:31]\n"
"v_lshlrev_b32 v69, 4, v68\n"
"v_mov_b32 v70, 0x3f80\n"
"v_mfma_f32_16x16x32_bf16 v[28:31], v[156:159], v[160:163], v[28:31]\n"
"buffer_load_dwordx4 a[216:219], v127, s[36:39], s21 offen offset:2048 sc1\n"
"s_waitcnt lgkmcnt(0)\n"
"v_mfma_f32_16x16x32_bf16 v[0:3], v[32:35], v[80:83], v[0:3]\n"
"ds_read_b64_tr_b16 v[128:129], v112 offset:328\n"
"v_mfma_f32_16x16x32_bf16 v[4:7], v[32:35], v[84:87], v[4:7]\n"
"ds_read_b64_tr_b16 v[130:131], v112 offset:320\n"
"v_mfma_f32_16x16x32_bf16 v[4:7], v[36:39], v[80:83], v[4:7]\n"
"ds_read_b128 v[160:163], v113 offset:128\n"
"v_mfma_f32_16x16x32_bf16 v[8:11], v[32:35], v[88:91], v[8:11]\n"
"buffer_load_dwordx4 a[220:223], v127, s[36:39], s21 offen offset:3072 sc1\n"
"v_mfma_f32_16x16x32_bf16 v[8:11], v[36:39], v[84:87], v[8:11]\n"
"ds_read_b64_tr_b16 v[132:133], v112 offset:840\n"
"v_mfma_f32_16x16x32_bf16 v[8:11], v[40:43], v[80:83], v[8:11]\n"
"ds_read_b64_tr_b16 v[134:135], v112 offset:832\n"
"v_mfma_f32_16x16x32_bf16 v[12:15], v[32:35], v[92:95], v[12:15]\n"
"ds_read_b128 v[164:167], v113 offset:640\n"
"v_mfma_f32_16x16x32_bf16 v[12:15], v[36:39], v[88:91], v[12:15]\n"
"buffer_load_dwordx4 a[224:227], v127, s[36:39], s22 offen offset:0 sc1\n"
"v_mfma_f32_16x16x32_bf16 v[12:15], v[40:43], v[84:87], v[12:15]\n"
"ds_read_b64_tr_b16 v[136:137], v112 offset:1352\n"
"v_mfma_f32_16x16x32_bf16 v[12:15], v[44:47], v[80:83], v[12:15]\n"
"ds_read_b64_tr_b16 v[138:139], v112 offset:1344\n"
"v_mfma_f32_16x16x32_bf16 v[16:19], v[32:35], v[96:99], v[16:19]\n"
"ds_read_b128 v[168:171], v113 offset:1152\n"
"v_mfma_f32_16x16x32_bf16 v[16:19], v[36:39], v[92:95], v[16:19]\n"
"buffer_load_dwordx4 a[228:231], v127, s[36:39], s22 offen offset:1024 sc1\n"
"v_mfma_f32_16x16x32_bf16 v[16:19], v[40:43], v[88:91], v[16:19]\n"
"ds_read_b64_tr_b16 v[140:141], v112 offset:1864\n"
"v_mfma_f32_16x16x32_bf16 v[16:19], v[44:47], v[84:87], v[16:19]\n"
"ds_read_b64_tr_b16 v[142:143], v112 offset:1856\n"
"v_mfma_f32_16x16x32_bf16 v[16:19], v[48:51], v[80:83], v[16:19]\n"
"ds_read_b128 v[172:175], v113 offset:1664\n"
"v_mfma_f32_16x16x32_bf16 v[20:23], v[32:35], v[100:103], v[20:23]\n"
"buffer_load_dwordx4 a[232:235], v127, s[36:39], s22 offen offset:2048 sc1\n"
"v_mfma_f32_16x16x32_bf16 v[20:23], v[36:39], v[96:99], v[20:23]\n"
"ds_read_b64_tr_b16 v[144:145], v112 offset:2376\n"
"v_mfma_f32_16x16x32_bf16 v[20:23], v[40:43], v[92:95], v[20:23]\n"
"ds_read_b64_tr_b16 v[146:147], v112 offset:2368\n"
"v_mfma_f32_16x16x32_bf16 v[20:23], v[44:47], v[88:91], v[20:23]\n"
"ds_read_b128 v[176:179], v113 offset:2176\n"
"v_mfma_f32_16x16x32_bf16 v[20:23], v[48:51], v[84:87], v[20:23]\n"
"buffer_load_dwordx4 a[236:239], v127, s[36:39], s22 offen offset:3072 sc1\n"
"v_mfma_f32_16x16x32_bf16 v[20:23], v[52:55], v[80:83], v[20:23]\n"
"ds_read_b64_tr_b16 v[148:149], v112 offset:2888\n"
"v_mfma_f32_16x16x32_bf16 v[24:27], v[32:35], v[104:107], v[24:27]\n"
"ds_read_b64_tr_b16 v[150:151], v112 offset:2880\n"
"v_mfma_f32_16x16x32_bf16 v[24:27], v[36:39], v[100:103], v[24:27]\n"
"ds_read_b128 v[180:183], v113 offset:2688\n"
"v_mfma_f32_16x16x32_bf16 v[24:27], v[40:43], v[96:99], v[24:27]\n"
"buffer_load_dwordx4 a[240:243], v127, s[36:39], s23 offen offset:0 sc1\n"
"v_mfma_f32_16x16x32_bf16 v[24:27], v[44:47], v[92:95], v[24:27]\n"
"ds_read_b64_tr_b16 v[152:153], v112 offset:3400\n"
"v_mfma_f32_16x16x32_bf16 v[24:27], v[48:51], v[88:91], v[24:27]\n"
"ds_read_b64_tr_b16 v[154:155], v112 offset:3392\n"
"v_mfma_f32_16x16x32_bf16 v[24:27], v[52:55], v[84:87], v[24:27]\n"
"ds_read_b128 v[184:187], v113 offset:3200\n"
"v_mfma_f32_16x16x32_bf16 v[24:27], v[56:59], v[80:83], v[24:27]\n"
"buffer_load_dwordx4 a[244:247], v127, s[36:39], s23 offen offset:1024 sc1\n"
"v_mfma_f32_16x16x32_bf16 v[28:31], v[32:35], v[108:111], v[28:31]\n"
"ds_read_b64_tr_b16 v[156:157], v112 offset:3912\n"
"v_mfma_f32_16x16x32_bf16 v[28:31], v[36:39], v[104:107], v[28:31]\n"
"ds_read_b64_tr_b16 v[158:159], v112 offset:3904\n"
"v_mfma_f32_16x16x32_bf16 v[28:31], v[40:43], v[100:103], v[28:31]\n"
"ds_read_b128 v[188:191], v113 offset:3712\n"
"v_mfma_f32_16x16x32_bf16 v[28:31], v[44:47], v[96:99], v[28:31]\n"
"buffer_load_dwordx4 a[248:251], v127, s[36:39], s23 offen offset:2048 sc1\n"
"v_mfma_f32_16x16x32_bf16 v[28:31], v[48:51], v[92:95], v[28:31]\n"
"v_lshlrev_b32 v70, v69, v70\n"
"v_ashrrev_i32 v68, 1, v68\n"
"v_mfma_f32_16x16x32_bf16 v[28:31], v[52:55], v[88:91], v[28:31]\n"
"v_cmp_eq_u32 vcc, 0, v68\n"
"v_cmp_eq_u32 s[0:1], 1, v68\n"
"v_mfma_f32_16x16x32_bf16 v[28:31], v[56:59], v[84:87], v[28:31]\n"
"v_cndmask_b32 v76, 0, v70, vcc\n"
"v_cndmask_b32 v77, 0, v70, s[0:1]\n"
"v_mfma_f32_16x16x32_bf16 v[28:31], v[60:63], v[80:83], v[28:31]\n"
"buffer_load_dwordx4 a[252:255], v127, s[36:39], s23 offen offset:3072 sc1\n"
"s_waitcnt lgkmcnt(0)\n"
"v_mfma_f32_16x16x32_bf16 v[0:3], v[128:131], v[160:163], v[0:3]\n"
"ds_read_b64_tr_b16 v[32:33], v112 offset:392\n"
"v_mfma_f32_16x16x32_bf16 v[4:7], v[128:131], v[164:167], v[4:7]\n"
"ds_read_b64_tr_b16 v[34:35], v112 offset:384\n"
"v_mfma_f32_16x16x32_bf16 v[4:7], v[132:135], v[160:163], v[4:7]\n"
"ds_read_b128 v[80:83], v113 offset:64\n"
"v_mfma_f32_16x16x32_bf16 v[8:11], v[128:131], v[168:171], v[8:11]\n"
"buffer_load_dwordx4 a[0:3], v127, s[56:59], s4 offen offset:0 sc1\n"
"v_mfma_f32_16x16x32_bf16 v[8:11], v[132:135], v[164:167], v[8:11]\n"
"ds_read_b64_tr_b16 v[36:37], v112 offset:904\n"
"v_mfma_f32_16x16x32_bf16 v[8:11], v[136:139], v[160:163], v[8:11]\n"
"ds_read_b64_tr_b16 v[38:39], v112 offset:896\n"
"v_mfma_f32_16x16x32_bf16 v[12:15], v[128:131], v[172:175], v[12:15]\n"
"ds_read_b128 v[84:87], v113 offset:576\n"
"v_mfma_f32_16x16x32_bf16 v[12:15], v[132:135], v[168:171], v[12:15]\n"
"buffer_load_dwordx4 a[4:7], v127, s[56:59], s4 offen offset:1024 sc1\n"
"v_mfma_f32_16x16x32_bf16 v[12:15], v[136:139], v[164:167], v[12:15]\n"
"ds_read_b64_tr_b16 v[40:41], v112 offset:1416\n"
"v_mfma_f32_16x16x32_bf16 v[12:15], v[140:143], v[160:163], v[12:15]\n"
"ds_read_b64_tr_b16 v[42:43], v112 offset:1408\n"
"v_mfma_f32_16x16x32_bf16 v[16:19], v[128:131], v[176:179], v[16:19]\n"
"ds_read_b128 v[88:91], v113 offset:1088\n"
"v_mfma_f32_16x16x32_bf16 v[16:19], v[132:135], v[172:175], v[16:19]\n"
"buffer_load_dwordx4 a[8:11], v127, s[56:59], s4 offen offset:2048 sc1\n"
"v_mfma_f32_16x16x32_bf16 v[16:19], v[136:139], v[168:171], v[16:19]\n"
"ds_read_b64_tr_b16 v[44:45], v112 offset:1928\n"
"v_mfma_f32_16x16x32_bf16 v[16:19], v[140:143], v[164:167], v[16:19]\n"
"ds_read_b64_tr_b16 v[46:47], v112 offset:1920\n"
"v_mfma_f32_16x16x32_bf16 v[16:19], v[144:147], v[160:163], v[16:19]\n"
"ds_read_b128 v[92:95], v113 offset:1600\n"
"v_mfma_f32_16x16x32_bf16 v[20:23], v[128:131], v[180:183], v[20:23]\n"
"buffer_load_dwordx4 a[12:15], v127, s[56:59], s4 offen offset:3072 sc1\n"
"v_mfma_f32_16x16x32_bf16 v[20:23], v[132:135], v[176:179], v[20:23]\n"
"ds_read_b64_tr_b16 v[48:49], v112 offset:2440\n"
"v_mfma_f32_16x16x32_bf16 v[20:23], v[136:139], v[172:175], v[20:23]\n"
"ds_read_b64_tr_b16 v[50:51], v112 offset:2432\n"
"v_mfma_f32_16x16x32_bf16 v[20:23], v[140:143], v[168:171], v[20:23]\n"
"ds_read_b128 v[96:99], v113 offset:2112\n"
"v_mfma_f32_16x16x32_bf16 v[20:23], v[144:147], v[164:167], v[20:23]\n"
"buffer_load_dwordx4 a[16:19], v127, s[56:59], s5 offen offset:0 sc1\n"
"v_mfma_f32_16x16x32_bf16 v[20:23], v[148:151], v[160:163], v[20:23]\n"
"ds_read_b64_tr_b16 v[52:53], v112 offset:2952\n"
"v_mfma_f32_16x16x32_bf16 v[24:27], v[128:131], v[184:187], v[24:27]\n"
"ds_read_b64_tr_b16 v[54:55], v112 offset:2944\n"
"v_mfma_f32_16x16x32_bf16 v[24:27], v[132:135], v[180:183], v[24:27]\n"
"ds_read_b128 v[100:103], v113 offset:2624\n"
"v_mfma_f32_16x16x32_bf16 v[24:27], v[136:139], v[176:179], v[24:27]\n"
"buffer_load_dwordx4 a[20:23], v127, s[56:59], s5 offen offset:1024 sc1\n"
"v_mfma_f32_16x16x32_bf16 v[24:27], v[140:143], v[172:175], v[24:27]\n"
"ds_read_b64_tr_b16 v[56:57], v112 offset:3464\n"
"v_mfma_f32_16x16x32_bf16 v[24:27], v[144:147], v[168:171], v[24:27]\n"
"ds_read_b64_tr_b16 v[58:59], v112 offset:3456\n"
"v_mfma_f32_16x16x32_bf16 v[24:27], v[148:151], v[164:167], v[24:27]\n"
"ds_read_b128 v[104:107], v113 offset:3136\n"
"v_mfma_f32_16x16x32_bf16 v[24:27], v[152:155], v[160:163], v[24:27]\n"
"buffer_load_dwordx4 a[24:27], v127, s[56:59], s5 offen offset:2048 sc1\n"
"v_mfma_f32_16x16x32_bf16 v[28:31], v[128:131], v[188:191], v[28:31]\n"
"ds_read_b64_tr_b16 v[60:61], v112 offset:3976\n"
"v_mfma_f32_16x16x32_bf16 v[28:31], v[132:135], v[184:187], v[28:31]\n"
"ds_read_b64_tr_b16 v[62:63], v112 offset:3968\n"
"v_mfma_f32_16x16x32_bf16 v[28:31], v[136:139], v[180:183], v[28:31]\n"
"ds_read_b128 v[108:111], v113 offset:3648\n"
"v_mfma_f32_16x16x32_bf16 v[28:31], v[140:143], v[176:179], v[28:31]\n"
"buffer_load_dwordx4 a[28:31], v127, s[56:59], s5 offen offset:3072 sc1\n"
"v_mfma_f32_16x16x32_bf16 v[28:31], v[144:147], v[172:175], v[28:31]\n"
"v_cmp_eq_u32 vcc, 2, v68\n"
"v_cmp_eq_u32 s[0:1], 3, v68\n"
"v_mfma_f32_16x16x32_bf16 v[28:31], v[148:151], v[168:171], v[28:31]\n"
"v_cndmask_b32 v78, 0, v70, vcc\n"
"v_cndmask_b32 v79, 0, v70, s[0:1]\n"
"v_mfma_f32_16x16x32_bf16 v[28:31], v[152:155], v[164:167], v[28:31]\n"
"v_mov_b32 v121, s31\n"
"s_memrealtime s[96:97]\n"
"v_mfma_f32_16x16x32_bf16 v[28:31], v[156:159], v[160:163], v[28:31]\n"
"buffer_load_dwordx4 a[32:35], v127, s[56:59], s6 offen offset:0 sc1\n"
"s_waitcnt lgkmcnt(0)\n"
"v_mfma_f32_16x16x32_bf16 v[0:3], v[32:35], v[80:83], v[0:3]\n"
"ds_read_b64_tr_b16 v[128:129], v112 offset:456\n"
"v_mov_b32 v120, s96\n"
"v_mfma_f32_16x16x32_bf16 v[4:7], v[32:35], v[84:87], v[4:7]\n"
"ds_read_b64_tr_b16 v[130:131], v112 offset:448\n"
"buffer_store_dwordx2 v[120:121], v114, s[32:35], s53 offen sc1\n"
"v_mfma_f32_16x16x32_bf16 v[4:7], v[36:39], v[80:83], v[4:7]\n"
"ds_read_b128 v[160:163], v113 offset:0\n"
"v_mfma_f32_16x16x32_bf16 v[8:11], v[32:35], v[88:91], v[8:11]\n"
"buffer_load_dwordx4 a[36:39], v127, s[56:59], s6 offen offset:1024 sc1\n"
"v_mfma_f32_16x16x32_bf16 v[8:11], v[36:39], v[84:87], v[8:11]\n"
"ds_read_b64_tr_b16 v[132:133], v112 offset:968\n"
"v_mfma_f32_16x16x32_bf16 v[8:11], v[40:43], v[80:83], v[8:11]\n"
"ds_read_b64_tr_b16 v[134:135], v112 offset:960\n"
"v_mfma_f32_16x16x32_bf16 v[12:15], v[32:35], v[92:95], v[12:15]\n"
"ds_read_b128 v[164:167], v113 offset:512\n"
"v_mfma_f32_16x16x32_bf16 v[12:15], v[36:39], v[88:91], v[12:15]\n"
"buffer_load_dwordx4 a[40:43], v127, s[56:59], s6 offen offset:2048 sc1\n"
"v_mfma_f32_16x16x32_bf16 v[12:15], v[40:43], v[84:87], v[12:15]\n"
"ds_read_b64_tr_b16 v[136:137], v112 offset:1480\n"
"v_mfma_f32_16x16x32_bf16 v[12:15], v[44:47], v[80:83], v[12:15]\n"
"ds_read_b64_tr_b16 v[138:139], v112 offset:1472\n"
"v_mfma_f32_16x16x32_bf16 v[16:19], v[32:35], v[96:99], v[16:19]\n"
"ds_read_b128 v[168:171], v113 offset:1024\n"
"v_mfma_f32_16x16x32_bf16 v[16:19], v[36:39], v[92:95], v[16:19]\n"
"buffer_load_dwordx4 a[44:47], v127, s[56:59], s6 offen offset:3072 sc1\n"
"v_mfma_f32_16x16x32_bf16 v[16:19], v[40:43], v[88:91], v[16:19]\n"
"ds_read_b64_tr_b16 v[140:141], v112 offset:1992\n"
"v_mfma_f32_16x16x32_bf16 v[16:19], v[44:47], v[84:87], v[16:19]\n"
"ds_read_b64_tr_b16 v[142:143], v112 offset:1984\n"
"v_mfma_f32_16x16x32_bf16 v[16:19], v[48:51], v[80:83], v[16:19]\n"
"ds_read_b128 v[172:175], v113 offset:1536\n"
"v_mfma_f32_16x16x32_bf16 v[20:23], v[32:35], v[100:103], v[20:23]\n"
"buffer_load_dwordx4 a[48:51], v127, s[56:59], s7 offen offset:0 sc1\n"
"v_mfma_f32_16x16x32_bf16 v[20:23], v[36:39], v[96:99], v[20:23]\n"
"ds_read_b64_tr_b16 v[144:145], v112 offset:2504\n"
"v_mfma_f32_16x16x32_bf16 v[20:23], v[40:43], v[92:95], v[20:23]\n"
"ds_read_b64_tr_b16 v[146:147], v112 offset:2496\n"
"v_mfma_f32_16x16x32_bf16 v[20:23], v[44:47], v[88:91], v[20:23]\n"
"ds_read_b128 v[176:179], v113 offset:2048\n"
"v_mfma_f32_16x16x32_bf16 v[20:23], v[48:51], v[84:87], v[20:23]\n"
"buffer_load_dwordx4 a[52:55], v127, s[56:59], s7 offen offset:1024 sc1\n"
"v_mfma_f32_16x16x32_bf16 v[20:23], v[52:55], v[80:83], v[20:23]\n"
"ds_read_b64_tr_b16 v[148:149], v112 offset:3016\n"
"v_mfma_f32_16x16x32_bf16 v[24:27], v[32:35], v[104:107], v[24:27]\n"
"ds_read_b64_tr_b16 v[150:151], v112 offset:3008\n"
"v_mfma_f32_16x16x32_bf16 v[24:27], v[36:39], v[100:103], v[24:27]\n"
"ds_read_b128 v[180:183], v113 offset:2560\n"
"v_mfma_f32_16x16x32_bf16 v[24:27], v[40:43], v[96:99], v[24:27]\n"
"buffer_load_dwordx4 a[56:59], v127, s[56:59], s7 offen offset:2048 sc1\n"
"v_mfma_f32_16x16x32_bf16 v[24:27], v[44:47], v[92:95], v[24:27]\n"
"ds_read_b64_tr_b16 v[152:153], v112 offset:3528\n"
"v_mfma_f32_16x16x32_bf16 v[24:27], v[48:51], v[88:91], v[24:27]\n"
"ds_read_b64_tr_b16 v[154:155], v112 offset:3520\n"
"v_mfma_f32_16x16x32_bf16 v[24:27], v[52:55], v[84:87], v[24:27]\n"
"ds_read_b128 v[184:187], v113 offset:3072\n"
"v_mfma_f32_16x16x32_bf16 v[24:27], v[56:59], v[80:83], v[24:27]\n"
"buffer_load_dwordx4 a[60:63], v127, s[56:59], s7 offen offset:3072 sc1\n"
"v_mfma_f32_16x16x32_bf16 v[28:31], v[32:35], v[108:111], v[28:31]\n"
"ds_read_b64_tr_b16 v[156:157], v112 offset:4040\n"
"v_mfma_f32_16x16x32_bf16 v[28:31], v[36:39], v[104:107], v[28:31]\n"
"ds_read_b64_tr_b16 v[158:159], v112 offset:4032\n"
"v_mfma_f32_16x16x32_bf16 v[28:31], v[40:43], v[100:103], v[28:31]\n"
"ds_read_b128 v[188:191], v113 offset:3584\n"
"v_mfma_f32_16x16x32_bf16 v[28:31], v[44:47], v[96:99], v[28:31]\n"
"buffer_load_dwordx4 a[64:67], v127, s[56:59], s8 offen offset:0 sc1\n"
"v_mfma_f32_16x16x32_bf16 v[28:31], v[48:51], v[92:95], v[28:31]\n"
"v_min_u32 v116, 368, v127\n"
"s_mul_i32 s54, s30, 384\n"
"v_mfma_f32_16x16x32_bf16 v[28:31], v[52:55], v[88:91], v[28:31]\n"
"s_add_u32 s55, s55, s54\n"
"v_mfma_f32_16x16x32_bf16 v[28:31], v[56:59], v[84:87], v[28:31]\n"
"buffer_load_dword v124, v116, s[32:35], s55 offen sc1\n"
"v_mfma_f32_16x16x32_bf16 v[28:31], v[60:63], v[80:83], v[28:31]\n"
"buffer_load_dwordx4 a[68:71], v127, s[56:59], s8 offen offset:1024 sc1\n"
"s_waitcnt lgkmcnt(0)\n"
"v_mfma_f32_16x16x32_bf16 v[0:3], v[128:131], v[160:163], v[0:3]\n"
"ds_read_b128 v[80:83], v117 offset:0\n"
"v_mfma_f32_16x16x32_bf16 v[4:7], v[128:131], v[164:167], v[4:7]\n"
"ds_read_b128 v[84:87], v117 offset:512\n"
"v_mfma_f32_16x16x32_bf16 v[4:7], v[132:135], v[160:163], v[4:7]\n"
"ds_read_b128 v[88:91], v117 offset:1024\n"
"v_mfma_f32_16x16x32_bf16 v[8:11], v[128:131], v[168:171], v[8:11]\n"
"buffer_load_dwordx4 a[72:75], v127, s[56:59], s8 offen offset:2048 sc1\n"
"v_mfma_f32_16x16x32_bf16 v[8:11], v[132:135], v[164:167], v[8:11]\n"
"ds_read_b128 v[92:95], v117 offset:1536\n"
"v_mfma_f32_16x16x32_bf16 v[8:11], v[136:139], v[160:163], v[8:11]\n"
"ds_read_b128 v[96:99], v117 offset:2048\n"
"s_waitcnt lgkmcnt(4)\n"
"v_mfma_f32_16x16x32_bf16 v[0:3], v[76:79], v[80:83], v[0:3]\n"
"s_waitcnt lgkmcnt(3)\n"
"v_mfma_f32_16x16x32_bf16 v[4:7], v[76:79], v[84:87], v[4:7]\n"
"v_mfma_f32_16x16x32_bf16 v[12:15], v[128:131], v[172:175], v[12:15]\n"
"ds_read_b128 v[100:103], v117 offset:2560\n"
"v_mfma_f32_16x16x32_bf16 v[12:15], v[132:135], v[168:171], v[12:15]\n"
"buffer_load_dwordx4 a[76:79], v127, s[56:59], s8 offen offset:3072 sc1\n"
"v_mfma_f32_16x16x32_bf16 v[12:15], v[136:139], v[164:167], v[12:15]\n"
"ds_read_b128 v[104:107], v117 offset:3072\n"
"v_mfma_f32_16x16x32_bf16 v[12:15], v[140:143], v[160:163], v[12:15]\n"
"ds_read_b128 v[108:111], v117 offset:3584\n"
"s_waitcnt lgkmcnt(4)\n"
"v_mfma_f32_16x16x32_bf16 v[12:15], v[76:79], v[92:95], v[12:15]\n"
"v_mfma_f32_16x16x32_bf16 v[8:11], v[76:79], v[88:91], v[8:11]\n"
"v_mfma_f32_16x16x32_bf16 v[16:19], v[128:131], v[176:179], v[16:19]\n"
"v_mfma_f32_16x16x32_bf16 v[16:19], v[132:135], v[172:175], v[16:19]\n"
"buffer_load_dwordx4 a[80:83], v127, s[56:59], s9 offen offset:0 sc1\n"
"v_mfma_f32_16x16x32_bf16 v[16:19], v[136:139], v[168:171], v[16:19]\n"
"v_cvt_pk_bf16_f32 v64, v0, v1\n"
"v_cvt_pk_bf16_f32 v66, v4, v5\n"
"v_mfma_f32_16x16x32_bf16 v[16:19], v[140:143], v[164:167], v[16:19]\n"
"v_cvt_pk_bf16_f32 v65, v2, v3\n"
"v_cvt_pk_bf16_f32 v67, v6, v7\n"
"v_mfma_f32_16x16x32_bf16 v[16:19], v[144:147], v[160:163], v[16:19]\n"
"v_permlane16_swap_b32 v64, v66\n"
"s_waitcnt lgkmcnt(3)\n"
"v_mfma_f32_16x16x32_bf16 v[16:19], v[76:79], v[96:99], v[16:19]\n"
"v_mfma_f32_16x16x32_bf16 v[20:23], v[128:131], v[180:183], v[20:23]\n"
"v_permlane16_swap_b32 v65, v67\n"
"v_mfma_f32_16x16x32_bf16 v[20:23], v[132:135], v[176:179], v[20:23]\n"
"buffer_store_dwordx4 v[64:67], v118, s[32:35], s52 offen sc1\n"
"v_mfma_f32_16x16x32_bf16 v[20:23], v[136:139], v[172:175], v[20:23]\n"
"v_mfma_f32_16x16x32_bf16 v[20:23], v[140:143], v[168:171], v[20:23]\n"
"buffer_load_dwordx4 a[84:87], v127, s[56:59], s9 offen offset:1024 sc1\n"
"v_mfma_f32_16x16x32_bf16 v[20:23], v[144:147], v[164:167], v[20:23]\n"
"v_mfma_f32_16x16x32_bf16 v[20:23], v[148:151], v[160:163], v[20:23]\n"
"v_cvt_pk_bf16_f32 v68, v8, v9\n"
"v_cvt_pk_bf16_f32 v70, v12, v13\n"
"s_waitcnt lgkmcnt(2)\n"
"v_mfma_f32_16x16x32_bf16 v[20:23], v[76:79], v[100:103], v[20:23]\n"
"v_mfma_f32_16x16x32_bf16 v[24:27], v[128:131], v[184:187], v[24:27]\n"
"v_cvt_pk_bf16_f32 v69, v10, v11\n"
"v_cvt_pk_bf16_f32 v71, v14, v15\n"
"v_mfma_f32_16x16x32_bf16 v[24:27], v[132:135], v[180:183], v[24:27]\n"
"v_permlane16_swap_b32 v68, v70\n"
"s_add_u32 s52, s52, 655360\n"
"v_mfma_f32_16x16x32_bf16 v[24:27], v[136:139], v[176:179], v[24:27]\n"
"v_permlane16_swap_b32 v69, v71\n"
"v_mfma_f32_16x16x32_bf16 v[24:27], v[140:143], v[172:175], v[24:27]\n"
"buffer_store_dwordx4 v[68:71], v118, s[32:35], s52 offen sc1\n"
"v_mfma_f32_16x16x32_bf16 v[24:27], v[144:147], v[168:171], v[24:27]\n"
"v_mfma_f32_16x16x32_bf16 v[24:27], v[148:151], v[164:167], v[24:27]\n"
"buffer_load_dwordx4 a[88:91], v127, s[56:59], s9 offen offset:2048 sc1\n"
".Lmg_hopS3:\n"
"s_cbranch_execz .Lmg_conv0\n"
"v_mfma_f32_16x16x32_bf16 v[24:27], v[152:155], v[160:163], v[24:27]\n"
"s_waitcnt lgkmcnt(1)\n"
"v_mfma_f32_16x16x32_bf16 v[24:27], v[76:79], v[104:107], v[24:27]\n"
"s_waitcnt lgkmcnt(0)\n"
"v_mfma_f32_16x16x32_bf16 v[28:31], v[76:79], v[108:111], v[28:31]\n"
"v_mfma_f32_16x16x32_bf16 v[28:31], v[128:131], v[188:191], v[28:31]\n"
"v_mfma_f32_16x16x32_bf16 v[28:31], v[132:135], v[184:187], v[28:31]\n"
"v_cvt_pk_bf16_f32 v72, v16, v17\n"
"v_cvt_pk_bf16_f32 v74, v20, v21\n"
"v_mfma_f32_16x16x32_bf16 v[28:31], v[136:139], v[180:183], v[28:31]\n"
"v_cvt_pk_bf16_f32 v73, v18, v19\n"
"v_cvt_pk_bf16_f32 v75, v22, v23\n"
"v_mfma_f32_16x16x32_bf16 v[28:31], v[140:143], v[176:179], v[28:31]\n"
"v_permlane16_swap_b32 v72, v74\n"
"s_add_u32 s52, s52, 655360\n"
"v_mfma_f32_16x16x32_bf16 v[28:31], v[144:147], v[172:175], v[28:31]\n"
"v_permlane16_swap_b32 v73, v75\n"
"v_mfma_f32_16x16x32_bf16 v[28:31], v[148:151], v[168:171], v[28:31]\n"
"buffer_store_dwordx4 v[72:75], v118, s[32:35], s52 offen sc1\n"
"v_mfma_f32_16x16x32_bf16 v[28:31], v[152:155], v[164:167], v[28:31]\n"
"v_mfma_f32_16x16x32_bf16 v[28:31], v[156:159], v[160:163], v[28:31]\n"
"v_cvt_pk_bf16_f32 v76, v24, v25\n"
"s_nop 6\n"
"v_cvt_pk_bf16_f32 v78, v28, v29\n"
"v_cvt_pk_bf16_f32 v77, v26, v27\n"
"v_cvt_pk_bf16_f32 v79, v30, v31\n"
"v_permlane16_swap_b32 v76, v78\n"
"s_add_u32 s52, s52, 655360\n"
"v_permlane16_swap_b32 v77, v79\n"
"buffer_store_dwordx4 v[76:79], v118, s[32:35], s52 offen sc1\n"
"s_waitcnt vmcnt(10)\n"
"v_cmp_ne_u32 vcc, s31, v124\n"
"s_cbranch_vccnz .Lmg_slow3\n"
".Lmg_ret3:\n"
"s_mov_b32 m0, 145408\n"
"v_mov_b32 v120, s51\n"
"v_mov_b32 v121, s51\n"
"v_mov_b32 v122, s51\n"
"v_mov_b32 v123, s51\n"
"s_lshr_b32 s52, s46, 3\n"
"s_mul_i32 s52, s52, 0\n"
"s_lshl_b32 s55, s46, 4\n"
"s_add_u32 s52, s52, s55\n"
"s_add_u32 s52, s52, 16777216\n"
"s_nop 0\n"
"s_cmp_eq_u32 s30, 3\n"
"s_cselect_b64 vcc, -1, 0\n"
"s_cbranch_vccnz 2\n"
"buffer_load_dwordx4 a[92:95], v127, s[56:59], s9 offen offset:3072 sc1\n"
"v_mfma_f32_16x16x32_bf16 v[72:75], v[192:195], v[120:123], 0\n"
"s_add_u32 s14, s62, 0\n"
"s_add_u32 s15, s62, 4096\n"
"v_mfma_f32_16x16x32_bf16 v[76:79], v[196:199], v[120:123], 0\n"
"s_add_u32 s16, s62, 8192\n"
"s_add_u32 s17, s62, 12288\n"
"s_cbranch_vccnz 2\n"
"buffer_load_dwordx4 a[96:99], v127, s[56:59], s10 offen offset:0 sc1\n"
"v_mfma_f32_16x16x32_bf16 v[72:75], v[200:203], v[120:123], v[72:75]\n"
"s_add_u32 s18, s62, 16384\n"
"s_add_u32 s19, s62, 20480\n"
"v_mfma_f32_16x16x32_bf16 v[76:79], v[204:207], v[120:123], v[76:79]\n"
"s_add_u32 s20, s62, 24576\n"
"s_add_u32 s21, s62, 28672\n"
"s_cbranch_vccnz 2\n"
"buffer_load_dwordx4 a[100:103], v127, s[56:59], s10 offen offset:1024 sc1\n"
"v_mfma_f32_16x16x32_bf16 v[72:75], v[208:211], v[120:123], v[72:75]\n"
"s_mov_b64 s[40:41], s[32:33]\n"
"s_mov_b32 s43, s35\n"
"v_mfma_f32_16x16x32_bf16 v[76:79], v[212:215], v[120:123], v[76:79]\n"
"s_cmp_lt_u32 s30, 2\n"
"s_cselect_b32 s42, s34, 0\n"
"s_cbranch_vccnz 2\n"
"buffer_load_dwordx4 a[104:107], v127, s[56:59], s10 offen offset:2048 sc1\n"
"v_mfma_f32_16x16x32_bf16 v[72:75], v[216:219], v[120:123], v[72:75]\n"
"s_mul_i32 s54, s47, 54\n"
"s_mul_i32 s53, s30, 6\n"
"v_mfma_f32_16x16x32_bf16 v[76:79], v[220:223], v[120:123], v[76:79]\n"
"s_add_u32 s53, s53, s54\n"
"s_mul_i32 s53, s53, 4352\n"
"s_cbranch_vccnz 2\n"
"buffer_load_dwordx4 a[108:111], v127, s[56:59], s10 offen offset:3072 sc1\n"
"v_mfma_f32_16x16x32_bf16 v[72:75], v[224:227], v[120:123], v[72:75]\n"
"s_add_u32 s53, s53, 7782400\n"
"s_add_u32 s64, s53, 0\n"
"v_mfma_f32_16x16x32_bf16 v[76:79], v[228:231], v[120:123], v[76:79]\n"
"s_add_u32 s65, s53, 4352\n"
"s_add_u32 s66, s53, 8704\n"
"s_cbranch_vccnz 2\n"
"buffer_load_dwordx4 a[112:115], v127, s[56:59], s11 offen offset:0 sc1\n"
"v_mfma_f32_16x16x32_bf16 v[72:75], v[232:235], v[120:123], v[72:75]\n"
"s_add_u32 s67, s53, 13056\n"
"s_add_u32 s68, s53, 17408\n"
"v_mfma_f32_16x16x32_bf16 v[76:79], v[236:239], v[120:123], v[76:79]\n"
"s_add_u32 s69, s53, 21760\n"
"s_mul_i32 s53, s30, 7\n"
"s_cbranch_vccnz 2\n"
"buffer_load_dwordx4 a[116:119], v127, s[56:59], s11 offen offset:1024 sc1\n"
"v_mfma_f32_16x16x32_bf16 v[72:75], v[240:243], v[120:123], v[72:75]\n"
"s_min_u32 s55, s30, 1\n"
"s_add_u32 s53, s53, s55\n"
"v_mfma_f32_16x16x32_bf16 v[76:79], v[244:247], v[120:123], v[76:79]\n"
"s_add_u32 s53, s53, s54\n"
"s_add_u32 s53, s53, 24\n"
"s_cbranch_vccnz 2\n"
"buffer_load_dwordx4 a[120:123], v127, s[56:59], s11 offen offset:2048 sc1\n"
"v_mfma_f32_16x16x32_bf16 v[72:75], v[248:251], v[120:123], v[72:75]\n"
"s_mul_i32 s53, s53, 4352\n"
"s_add_u32 s53, s53, 7782400\n"
"v_mfma_f32_16x16x32_bf16 v[76:79], v[252:255], v[120:123], v[76:79]\n"
"s_add_u32 s70, s53, 0\n"
"s_add_u32 s71, s53, 4352\n"
"s_cbranch_vccnz 2\n"
"buffer_load_dwordx4 a[124:127], v127, s[56:59], s11 offen offset:3072 sc1\n"
"v_mfma_f32_16x16x32_bf16 v[72:75], a[160:163], v[120:123], v[72:75]\n"
"s_add_u32 s72, s53, 8704\n"
"s_add_u32 s73, s53, 13056\n"
"v_mfma_f32_16x16x32_bf16 v[76:79], a[164:167], v[120:123], v[76:79]\n"
"s_add_u32 s74, s53, 17408\n"
"s_add_u32 s75, s53, 21760\n"
"s_cbranch_vccnz 2\n"
"buffer_load_dwordx4 a[128:131], v127, s[56:59], s12 offen offset:0 sc1\n"
"v_mfma_f32_16x16x32_bf16 v[72:75], a[168:171], v[120:123], v[72:75]\n"
"s_add_u32 s76, s53, 26112\n"
"s_add_u32 s77, s53, 30464\n"
"v_mfma_f32_16x16x32_bf16 v[76:79], a[172:175], v[120:123], v[76:79]\n"
"v_and_b32 v113, 63, v126\n"
"v_min_u32 v113, 39, v113\n"
"s_cbranch_vccnz 2\n"
"buffer_load_dwordx4 a[132:135], v127, s[56:59], s12 offen offset:1024 sc1\n"
"v_mfma_f32_16x16x32_bf16 v[72:75], a[176:179], v[120:123], v[72:75]\n"
"v_lshrrev_b32 v114, 3, v113\n"
"v_and_b32 v113, 7, v113\n"
"v_mfma_f32_16x16x32_bf16 v[76:79], a[180:183], v[120:123], v[76:79]\n"
"v_mul_u32_u24 v114, 512, v114\n"
"v_lshl_add_u32 v113, v113, 4, v114\n"
"s_cbranch_vccnz 2\n"
"buffer_load_dwordx4 a[136:139], v127, s[56:59], s12 offen offset:2048 sc1\n"
"v_mfma_f32_16x16x32_bf16 v[72:75], a[184:187], v[120:123], v[72:75]\n"
"s_mul_i32 s98, s44, 4096\n"
"s_mul_i32 s55, s30, 128\n"
"v_mfma_f32_16x16x32_bf16 v[76:79], a[188:191], v[120:123], v[76:79]\n"
"s_add_u32 s98, s98, s55\n"
"s_add_u32 s98, s98, 16777216\n"
"s_cbranch_vccnz 2\n"
"buffer_load_dwordx4 a[140:143], v127, s[56:59], s12 offen offset:3072 sc1\n"
"v_mfma_f32_16x16x32_bf16 v[72:75], a[192:195], v[120:123], v[72:75]\n"
"v_bfe_u32 v32, v126, 4, 2\n"
"v_bfe_u32 v33, v126, 2, 2\n"
"v_mfma_f32_16x16x32_bf16 v[76:79], a[196:199], v[120:123], v[76:79]\n"
"v_and_b32 v34, 3, v126\n"
"v_lshl_add_u32 v35, v32, 2, v33\n"
"s_cbranch_vccnz 2\n"
"buffer_load_dwordx4 a[144:147], v127, s[56:59], s13 offen offset:0 sc1\n"
"v_mfma_f32_16x16x32_bf16 v[72:75], a[200:203], v[120:123], v[72:75]\n"
"v_bfe_u32 v36, v35, 1, 2\n"
"v_lshlrev_b32 v35, 7, v35\n"
"v_mfma_f32_16x16x32_bf16 v[76:79], a[204:207], v[120:123], v[76:79]\n"
"v_lshl_add_u32 v35, v34, 3, v35\n"
"v_lshl_add_u32 v117, v36, 5, v35\n"
"s_cbranch_vccz 1\n"
"s_waitcnt vmcnt(0)\n"
"v_mov_b32 v112, s31\n"
"s_waitcnt vmcnt(14)\n"
"s_barrier\n"
"buffer_store_dword v112, v119, s[32:35], s52 offen sc1\n"
"v_and_b32 v37, 7, v126\n"
"v_lshlrev_b32 v37, 7, v37\n"
"s_lshl_b32 s97, s44, 10\n"
"s_add_u32 s97, s97, 327680\n"
"buffer_load_dwordx2 v[38:39], v37, s[32:35], s97 offen sc1\n"
"v_mfma_f32_16x16x32_bf16 v[72:75], a[208:211], v[120:123], v[72:75]\n"
"v_xor_b32 v118, 32, v117\n"
"v_xor_b32 v116, 64, v117\n"
"v_mfma_f32_16x16x32_bf16 v[76:79], a[212:215], v[120:123], v[76:79]\n"
"v_xor_b32 v124, 96, v117\n"
"v_and_b32 v125, 15, v126\n"
"s_cbranch_vccnz 2\n"
"buffer_load_dwordx4 a[148:151], v127, s[56:59], s13 offen offset:1024 sc1\n"
"v_mfma_f32_16x16x32_bf16 v[72:75], a[216:219], v[120:123], v[72:75]\n"
"v_lshlrev_b32 v125, 2, v125\n"
"v_add_u32 v125, 81920, v125\n"
"v_mfma_f32_16x16x32_bf16 v[76:79], a[220:223], v[120:123], v[76:79]\n"
"s_mul_i32 s99, s47, 81920\n"
"s_lshl_b32 s55, s30, 12\n"
"s_cbranch_vccnz 2\n"
"buffer_load_dwordx4 a[152:155], v127, s[56:59], s13 offen offset:2048 sc1\n"
"v_mfma_f32_16x16x32_bf16 v[72:75], a[224:227], v[120:123], v[72:75]\n"
"s_add_u32 s99, s99, 5160960\n"
"s_add_u32 s99, s99, s55\n"
"v_mfma_f32_16x16x32_bf16 v[76:79], a[228:231], v[120:123], v[76:79]\n"
"s_mov_b32 m0, s55\n"
"s_cbranch_vccnz 2\n"
"buffer_load_dwordx4 a[156:159], v127, s[56:59], s13 offen offset:3072 sc1\n"
"v_mfma_f32_16x16x32_bf16 v[72:75], a[232:235], v[120:123], v[72:75]\n"
"v_mfma_f32_16x16x32_bf16 v[76:79], a[236:239], v[120:123], v[76:79]\n"
"buffer_load_dwordx4 v[128:131], v127, s[32:35], s14 offen offset:0 sc1\n"
"v_mfma_f32_16x16x32_bf16 v[72:75], a[240:243], v[120:123], v[72:75]\n"
"v_mfma_f32_16x16x32_bf16 v[76:79], a[244:247], v[120:123], v[76:79]\n"
"v_mfma_f32_16x16x32_bf16 v[72:75], a[248:251], v[120:123], v[72:75]\n"
"buffer_load_dwordx4 v[132:135], v127, s[32:35], s14 offen offset:1024 sc1\n"
"v_mfma_f32_16x16x32_bf16 v[76:79], a[252:255], v[120:123], v[76:79]\n"
"v_mfma_f32_16x16x32_bf16 v[64:67], a[0:3], v[120:123], 0\n"
"v_mfma_f32_16x16x32_bf16 v[68:71], a[4:7], v[120:123], 0\n"
"buffer_load_dwordx4 v[136:139], v127, s[32:35], s14 offen offset:2048 sc1\n"
"v_mfma_f32_16x16x32_bf16 v[64:67], a[8:11], v[120:123], v[64:67]\n"
"v_mfma_f32_16x16x32_bf16 v[68:71], a[12:15], v[120:123], v[68:71]\n"
"v_mfma_f32_16x16x32_bf16 v[64:67], a[16:19], v[120:123], v[64:67]\n"
"buffer_load_dwordx4 v[140:143], v127, s[32:35], s14 offen offset:3072 sc1\n"
"v_mfma_f32_16x16x32_bf16 v[68:71], a[20:23], v[120:123], v[68:71]\n"
"v_mfma_f32_16x16x32_bf16 v[64:67], a[24:27], v[120:123], v[64:67]\n"
"v_mfma_f32_16x16x32_bf16 v[68:71], a[28:31], v[120:123], v[68:71]\n"
"buffer_load_dwordx4 v[144:147], v127, s[32:35], s15 offen offset:0 sc1\n"
"v_mfma_f32_16x16x32_bf16 v[64:67], a[32:35], v[120:123], v[64:67]\n"
"v_mfma_f32_16x16x32_bf16 v[68:71], a[36:39], v[120:123], v[68:71]\n"
"v_mfma_f32_16x16x32_bf16 v[64:67], a[40:43], v[120:123], v[64:67]\n"
"buffer_load_dwordx4 v[148:151], v127, s[32:35], s15 offen offset:1024 sc1\n"
"v_mfma_f32_16x16x32_bf16 v[68:71], a[44:47], v[120:123], v[68:71]\n"
"v_mfma_f32_16x16x32_bf16 v[64:67], a[48:51], v[120:123], v[64:67]\n"
"v_mfma_f32_16x16x32_bf16 v[68:71], a[52:55], v[120:123], v[68:71]\n"
"buffer_load_dwordx4 v[152:155], v127, s[32:35], s15 offen offset:2048 sc1\n"
"v_mfma_f32_16x16x32_bf16 v[64:67], a[56:59], v[120:123], v[64:67]\n"
"v_mfma_f32_16x16x32_bf16 v[68:71], a[60:63], v[120:123], v[68:71]\n"
"v_mfma_f32_16x16x32_bf16 v[64:67], a[64:67], v[120:123], v[64:67]\n"
"buffer_load_dwordx4 v[156:159], v127, s[32:35], s15 offen offset:3072 sc1\n"
"v_mfma_f32_16x16x32_bf16 v[68:71], a[68:71], v[120:123], v[68:71]\n"
"v_mfma_f32_16x16x32_bf16 v[64:67], a[72:75], v[120:123], v[64:67]\n"
"v_mfma_f32_16x16x32_bf16 v[68:71], a[76:79], v[120:123], v[68:71]\n"
"buffer_load_dwordx4 v[160:163], v127, s[32:35], s16 offen offset:0 sc1\n"
"v_mfma_f32_16x16x32_bf16 v[64:67], a[80:83], v[120:123], v[64:67]\n"
"v_mfma_f32_16x16x32_bf16 v[68:71], a[84:87], v[120:123], v[68:71]\n"
"v_mfma_f32_16x16x32_bf16 v[64:67], a[88:91], v[120:123], v[64:67]\n"
"buffer_load_dwordx4 v[164:167], v127, s[32:35], s16 offen offset:1024 sc1\n"
"s_waitcnt vmcnt(28)\n"
"v_mfma_f32_16x16x32_bf16 v[68:71], a[92:95], v[120:123], v[68:71]\n"
"s_waitcnt vmcnt(27)\n"
"v_mfma_f32_16x16x32_bf16 v[64:67], a[96:99], v[120:123], v[64:67]\n"
"s_waitcnt vmcnt(26)\n"
"v_mfma_f32_16x16x32_bf16 v[68:71], a[100:103], v[120:123], v[68:71]\n"
"s_cbranch_vccz 1\n"
"s_waitcnt vmcnt(10)\n"
".Lmg_join:\n"
"s_waitcnt vmcnt(13)\n"
"v_cmp_ne_u32 vcc, s31, v39\n"
"s_cbranch_vccnz .Lmg_slow8\n"
".Lmg_ret8:\n"
"s_memrealtime s[96:97]\n"
"s_waitcnt lgkmcnt(0)\n"
"v_sub_u32 v38, s96, v38\n"
"s_nop 1\n"
"v_min_u32_dpp v38, v38, v38 quad_perm:[1,0,3,2] row_mask:0xf bank_mask:0xf\n"
"s_nop 1\n"
"v_min_u32_dpp v38, v38, v38 quad_perm:[2,3,0,1] row_mask:0xf bank_mask:0xf\n"
"s_nop 1\n"
"v_min_u32_dpp v38, v38, v38 row_half_mirror row_mask:0xf bank_mask:0xf\n"
"s_nop 0\n"
"v_readfirstlane_b32 s53, v38\n"
"s_nop 0\n"
"s_sub_u32 s53, s96, s53\n"
".Lmg_tw8:\n"
"s_memrealtime s[96:97]\n"
"s_waitcnt lgkmcnt(0)\n"
"s_sub_u32 s54, s96, s53\n"
"s_cmp_lt_u32 s54, 150\n"
"s_cbranch_scc1 .Lmg_tw8\n"
"s_mov_b32 s52, s98\n"
"buffer_load_dword v115, v113, s[32:35], s52 offen sc1\n"
"s_add_u32 s4, s62, 32768\n"
"s_add_u32 s5, s62, 36864\n"
"s_add_u32 s6, s62, 40960\n"
"s_add_u32 s7, s62, 45056\n"
"s_add_u32 s8, s62, 49152\n"
"s_add_u32 s9, s62, 53248\n"
"s_add_u32 s10, s62, 57344\n"
"s_add_u32 s11, s62, 61440\n"
"s_add_u32 s12, s62, 65536\n"
"s_add_u32 s13, s62, 69632\n"
"s_waitcnt vmcnt(11)\n"
"s_mov_b64 exec, 0\n"
"s_cmp_eq_u32 s30, 0\n"
"s_cbranch_scc1 .Lmg_hopM0\n"
"s_cmp_eq_u32 s30, 1\n"
"s_cbranch_scc1 .Lmg_hopM1\n"
"s_cmp_eq_u32 s30, 2\n"
"s_cbranch_scc1 .Lmg_hopM2\n"
"s_mov_b64 exec, -1\n"
"s_cmp_lg_u32 s30, 3\n"
"s_cbranch_scc1 .Lmg_noones\n"
"v_accvgpr_write_b32 a0, v120\n"
"v_accvgpr_write_b32 a1, v120\n"
"v_accvgpr_write_b32 a2, v120\n"
"v_accvgpr_write_b32 a3, v120\n"
"v_accvgpr_write_b32 a8, v120\n"
"v_accvgpr_write_b32 a9, v120\n"
"v_accvgpr_write_b32 a10, v120\n"
"v_accvgpr_write_b32 a11, v120\n"
"v_accvgpr_write_b32 a16, v120\n"
"v_accvgpr_write_b32 a17, v120\n"
"v_accvgpr_write_b32 a18, v120\n"
"v_accvgpr_write_b32 a19, v120\n"
"v_accvgpr_write_b32 a24, v120\n"
"v_accvgpr_write_b32 a25, v120\n"
"v_accvgpr_write_b32 a26, v120\n"
"v_accvgpr_write_b32 a27, v120\n"
"v_accvgpr_write_b32 a32, v120\n"
"v_accvgpr_write_b32 a33, v120\n"
"v_accvgpr_write_b32 a34, v120\n"
"v_accvgpr_write_b32 a35, v120\n"
"v_accvgpr_write_b32 a40, v120\n"
"v_accvgpr_write_b32 a41, v120\n"
"v_accvgpr_write_b32 a42, v120\n"
"v_accvgpr_write_b32 a43, v120\n"
"v_accvgpr_write_b32 a48, v120\n"
"v_accvgpr_write_b32 a49, v120\n"
"v_accvgpr_write_b32 a50, v120\n"
"v_accvgpr_write_b32 a51, v120\n"
"v_accvgpr_write_b32 a56, v120\n"
"v_accvgpr_write_b32 a57, v120\n"
"v_accvgpr_write_b32 a58, v120\n"
"v_accvgpr_write_b32 a59, v120\n"
"v_accvgpr_write_b32 a64, v120\n"
"v_accvgpr_write_b32 a65, v120\n"
"v_accvgpr_write_b32 a66, v120\n"
"v_accvgpr_write_b32 a67, v120\n"
"v_accvgpr_write_b32 a72, v120\n"
"v_accvgpr_write_b32 a73, v120\n"
"v_accvgpr_write_b32 a74, v120\n"
"v_accvgpr_write_b32 a75, v120\n"
"v_accvgpr_write_b32 a80, v120\n"
"v_accvgpr_write_b32 a81, v120\n"
"v_accvgpr_write_b32 a82, v120\n"
"v_accvgpr_write_b32 a83, v120\n"
"v_accvgpr_write_b32 a88, v120\n"
"v_accvgpr_write_b32 a89, v120\n"
"v_accvgpr_write_b32 a90, v120\n"
"v_accvgpr_write_b32 a91, v120\n"
"v_accvgpr_write_b32 a96, v120\n"
"v_accvgpr_write_b32 a97, v120\n"
"v_accvgpr_write_b32 a98, v120\n"
"v_accvgpr_write_b32 a99, v120\n"
"v_accvgpr_write_b32 a104, v120\n"
"v_accvgpr_write_b32 a105, v120\n"
"v_accvgpr_write_b32 a106, v120\n"
"v_accvgpr_write_b32 a107, v120\n"
"v_accvgpr_write_b32 a112, v120\n"
"v_accvgpr_write_b32 a113, v120\n"
"v_accvgpr_write_b32 a114, v120\n"
"v_accvgpr_write_b32 a115, v120\n"
"v_accvgpr_write_b32 a120, v120\n"
"v_accvgpr_write_b32 a121, v120\n"
"v_accvgpr_write_b32 a122, v120\n"
"v_accvgpr_write_b32 a123, v120\n"
"v_accvgpr_write_b32 a128, v120\n"
"v_accvgpr_write_b32 a129, v120\n"
"v_accvgpr_write_b32 a130, v120\n"
"v_accvgpr_write_b32 a131, v120\n"
"v_accvgpr_write_b32 a136, v120\n"
"v_accvgpr_write_b32 a137, v120\n"
"v_accvgpr_write_b32 a138, v120\n"
"v_accvgpr_write_b32 a139, v120\n"
"v_accvgpr_write_b32 a144, v120\n"
"v_accvgpr_write_b32 a145, v120\n"
"v_accvgpr_write_b32 a146, v120\n"
"v_accvgpr_write_b32 a147, v120\n"
"v_accvgpr_write_b32 a152, v120\n"
"v_accvgpr_write_b32 a153, v120\n"
"v_accvgpr_write_b32 a154, v120\n"
"v_accvgpr_write_b32 a155, v120\n"
"s_nop 1\n"
".Lmg_noones:\n"
"s_mov_b64 exec, -1\n"
"s_waitcnt vmcnt(0)\n"
"v_cmp_ne_u32 vcc, s31, v115\n"
"s_cbranch_vccnz .Lmg_slow9\n"
".Lmg_ret9:\n"
"buffer_load_dwordx4 v127, s[32:35], s99 offen offset:0 sc1 lds\n"
"v_mfma_f32_16x16x32_bf16 v[64:67], a[104:107], v[120:123], v[64:67]\n"
"v_mfma_f32_16x16x32_bf16 v[68:71], a[108:111], v[120:123], v[68:71]\n"
"buffer_load_dwordx4 v127, s[32:35], s99 offen offset:1024 sc1 lds\n"
"v_mfma_f32_16x16x32_bf16 v[64:67], a[112:115], v[120:123], v[64:67]\n"
"v_mfma_f32_16x16x32_bf16 v[68:71], a[116:119], v[120:123], v[68:71]\n"
"buffer_load_dwordx4 v127, s[32:35], s99 offen offset:2048 sc1 lds\n"
"v_mfma_f32_16x16x32_bf16 v[64:67], a[120:123], v[120:123], v[64:67]\n"
"v_mfma_f32_16x16x32_bf16 v[68:71], a[124:127], v[120:123], v[68:71]\n"
"buffer_load_dwordx4 v127, s[32:35], s99 offen offset:3072 sc1 lds\n"
"v_mfma_f32_16x16x32_bf16 v[64:67], a[128:131], v[120:123], v[64:67]\n"
"v_mfma_f32_16x16x32_bf16 v[68:71], a[132:135], v[120:123], v[68:71]\n"
"s_add_u32 m0, m0, 16384\n"
"s_add_u32 s99, s99, 16384\n"
"buffer_load_dwordx4 v127, s[32:35], s99 offen offset:0 sc1 lds\n"
"v_mfma_f32_16x16x32_bf16 v[64:67], a[136:139], v[120:123], v[64:67]\n"
"v_mfma_f32_16x16x32_bf16 v[68:71], a[140:143], v[120:123], v[68:71]\n"
"buffer_load_dwordx4 v127, s[32:35], s99 offen offset:1024 sc1 lds\n"
"v_mfma_f32_16x16x32_bf16 v[64:67], a[144:147], v[120:123], v[64:67]\n"
"v_mfma_f32_16x16x32_bf16 v[68:71], a[148:151], v[120:123], v[68:71]\n"
"buffer_load_dwordx4 v127, s[32:35], s99 offen offset:2048 sc1 lds\n"
"v_mfma_f32_16x16x32_bf16 v[64:67], a[152:155], v[120:123], v[64:67]\n"
"v_mfma_f32_16x16x32_bf16 v[68:71], a[156:159], v[120:123], v[68:71]\n"
"buffer_load_dwordx4 v127, s[32:35], s99 offen offset:3072 sc1 lds\n"
"s_add_u32 m0, m0, 16384\n"
"s_add_u32 s99, s99, 16384\n"
"buffer_load_dwordx4 v127, s[32:35], s99 offen offset:0 sc1 lds\n"
"buffer_load_dwordx4 v127, s[32:35], s99 offen offset:1024 sc1 lds\n"
"buffer_load_dwordx4 v127, s[32:35], s99 offen offset:2048 sc1 lds\n"
"buffer_load_dwordx4 v127, s[32:35], s99 offen offset:3072 sc1 lds\n"
"v_add_u32 v112, 0x10000, v117\n"
"v_add_u32 v113, 0x10000, v118\n"
"v_add_u32 v114, 0x10000, v116\n"
"v_add_u32 v115, 0x10000, v124\n"
"s_waitcnt vmcnt(8)\n"
"s_barrier\n"
"ds_read_b64_tr_b16 v[32:33], v117 offset:0\n"
"ds_read_b64_tr_b16 v[34:35], v117 offset:2048\n"
"ds_read_b64_tr_b16 v[36:37], v118 offset:0\n"
"ds_read_b64_tr_b16 v[38:39], v118 offset:2048\n"
"ds_read_b64_tr_b16 v[40:41], v116 offset:0\n"
"ds_read_b64_tr_b16 v[42:43], v116 offset:2048\n"
"ds_read_b64_tr_b16 v[44:45], v124 offset:0\n"
"ds_read_b64_tr_b16 v[46:47], v124 offset:2048\n"
"s_waitcnt lgkmcnt(4)\n"
"v_mfma_f32_16x16x32_bf16 v[0:3], a[0:3], v[32:35], 0\n"
"ds_read_b64_tr_b16 v[48:49], v117 offset:4096\n"
"v_mfma_f32_16x16x32_bf16 v[16:19], a[4:7], v[32:35], 0\n"
"ds_read_b64_tr_b16 v[50:51], v117 offset:6144\n"
"v_mfma_f32_16x16x32_bf16 v[4:7], a[0:3], v[36:39], 0\n"
"s_add_u32 m0, m0, 16384\n"
"s_add_u32 s99, s99, 16384\n"
"buffer_load_dwordx4 v127, s[32:35], s99 offen offset:0 sc1 lds\n"
"ds_read_b64_tr_b16 v[52:53], v118 offset:4096\n"
"v_mfma_f32_16x16x32_bf16 v[20:23], a[4:7], v[36:39], 0\n"
"ds_read_b64_tr_b16 v[54:55], v118 offset:6144\n"
"s_waitcnt lgkmcnt(4)\n"
"v_mfma_f32_16x16x32_bf16 v[8:11], a[0:3], v[40:43], 0\n"
"ds_read_b64_tr_b16 v[56:57], v116 offset:4096\n"
"v_mfma_f32_16x16x32_bf16 v[24:27], a[4:7], v[40:43], 0\n"
"ds_read_b64_tr_b16 v[58:59], v116 offset:6144\n"
"v_mfma_f32_16x16x32_bf16 v[12:15], a[0:3], v[44:47], 0\n"
"buffer_load_dwordx4 v127, s[32:35], s99 offen offset:1024 sc1 lds\n"
"ds_read_b64_tr_b16 v[60:61], v124 offset:4096\n"
"v_mfma_f32_16x16x32_bf16 v[28:31], a[4:7], v[44:47], 0\n"
"ds_read_b64_tr_b16 v[62:63], v124 offset:6144\n"
"s_waitcnt lgkmcnt(4)\n"
"v_mfma_f32_16x16x32_bf16 v[0:3], a[8:11], v[48:51], v[0:3]\n"
"ds_read_b64_tr_b16 v[32:33], v117 offset:8192\n"
"v_mfma_f32_16x16x32_bf16 v[16:19], a[12:15], v[48:51], v[16:19]\n"
"ds_read_b64_tr_b16 v[34:35], v117 offset:10240\n"
"v_mfma_f32_16x16x32_bf16 v[4:7], a[8:11], v[52:55], v[4:7]\n"
"buffer_load_dwordx4 v127, s[32:35], s99 offen offset:2048 sc1 lds\n"
"ds_read_b64_tr_b16 v[36:37], v118 offset:8192\n"
"v_mfma_f32_16x16x32_bf16 v[20:23], a[12:15], v[52:55], v[20:23]\n"
"ds_read_b64_tr_b16 v[38:39], v118 offset:10240\n"
"s_waitcnt lgkmcnt(4)\n"
"v_mfma_f32_16x16x32_bf16 v[8:11], a[8:11], v[56:59], v[8:11]\n"
"ds_read_b64_tr_b16 v[40:41], v116 offset:8192\n"
"v_mfma_f32_16x16x32_bf16 v[24:27], a[12:15], v[56:59], v[24:27]\n"
"ds_read_b64_tr_b16 v[42:43], v116 offset:10240\n"
"v_mfma_f32_16x16x32_bf16 v[12:15], a[8:11], v[60:63], v[12:15]\n"
"buffer_load_dwordx4 v127, s[32:35], s99 offen offset:3072 sc1 lds\n"
"ds_read_b64_tr_b16 v[44:45], v124 offset:8192\n"
"v_mfma_f32_16x16x32_bf16 v[28:31], a[12:15], v[60:63], v[28:31]\n"
"ds_read_b64_tr_b16 v[46:47], v124 offset:10240\n"
"s_waitcnt lgkmcnt(4)\n"
"v_mfma_f32_16x16x32_bf16 v[0:3], a[16:19], v[32:35], v[0:3]\n"
"ds_read_b64_tr_b16 v[48:49], v117 offset:12288\n"
"v_mfma_f32_16x16x32_bf16 v[16:19], a[20:23], v[32:35], v[16:19]\n"
"ds_read_b64_tr_b16 v[50:51], v117 offset:14336\n"
"v_mfma_f32_16x16x32_bf16 v[4:7], a[16:19], v[36:39], v[4:7]\n"
"s_add_u32 m0, m0, 16384\n"
"s_add_u32 s99, s99, 16384\n"
"buffer_load_dwordx4 v127, s[32:35], s99 offen offset:0 sc1 lds\n"
"ds_read_b64_tr_b16 v[52:53], v118 offset:12288\n"
"v_mfma_f32_16x16x32_bf16 v[20:23], a[20:23], v[36:39], v[20:23]\n"
"ds_read_b64_tr_b16 v[54:55], v118 offset:14336\n"
"s_waitcnt lgkmcnt(4)\n"
"v_mfma_f32_16x16x32_bf16 v[8:11], a[16:19], v[40:43], v[8:11]\n"
"ds_read_b64_tr_b16 v[56:57], v116 offset:12288\n"
"v_mfma_f32_16x16x32_bf16 v[24:27], a[20:23], v[40:43], v[24:27]\n"
"ds_read_b64_tr_b16 v[58:59], v116 offset:14336\n"
"v_mfma_f32_16x16x32_bf16 v[12:15], a[16:19], v[44:47], v[12:15]\n"
"buffer_load_dwordx4 v127, s[32:35], s99 offen offset:1024 sc1 lds\n"
"ds_read_b64_tr_b16 v[60:61], v124 offset:12288\n"
"v_mfma_f32_16x16x32_bf16 v[28:31], a[20:23], v[44:47], v[28:31]\n"
"ds_read_b64_tr_b16 v[62:63], v124 offset:14336\n"
"s_waitcnt lgkmcnt(4)\n"
"v_mfma_f32_16x16x32_bf16 v[0:3], a[24:27], v[48:51], v[0:3]\n"
"s_waitcnt vmcnt(10)\n"
"s_barrier\n"
"ds_read_b64_tr_b16 v[32:33], v117 offset:16384\n"
"v_mfma_f32_16x16x32_bf16 v[16:19], a[28:31], v[48:51], v[16:19]\n"
"ds_read_b64_tr_b16 v[34:35], v117 offset:18432\n"
"v_mfma_f32_16x16x32_bf16 v[4:7], a[24:27], v[52:55], v[4:7]\n"
"buffer_load_dwordx4 v127, s[32:35], s99 offen offset:2048 sc1 lds\n"
"ds_read_b64_tr_b16 v[36:37], v118 offset:16384\n"
"v_mfma_f32_16x16x32_bf16 v[20:23], a[28:31], v[52:55], v[20:23]\n"
"ds_read_b64_tr_b16 v[38:39], v118 offset:18432\n"
"s_waitcnt lgkmcnt(4)\n"
"v_mfma_f32_16x16x32_bf16 v[8:11], a[24:27], v[56:59], v[8:11]\n"
"ds_read_b64_tr_b16 v[40:41], v116 offset:16384\n"
"v_mfma_f32_16x16x32_bf16 v[24:27], a[28:31], v[56:59], v[24:27]\n"
"ds_read_b64_tr_b16 v[42:43], v116 offset:18432\n"
"v_mfma_f32_16x16x32_bf16 v[12:15], a[24:27], v[60:63], v[12:15]\n"
"buffer_load_dwordx4 v127, s[32:35], s99 offen offset:3072 sc1 lds\n"
"ds_read_b64_tr_b16 v[44:45], v124 offset:16384\n"
"v_mfma_f32_16x16x32_bf16 v[28:31], a[28:31], v[60:63], v[28:31]\n"
"ds_read_b64_tr_b16 v[46:47], v124 offset:18432\n"
"s_waitcnt lgkmcnt(4)\n"
"v_mfma_f32_16x16x32_bf16 v[0:3], a[32:35], v[32:35], v[0:3]\n"
"ds_read_b64_tr_b16 v[48:49], v117 offset:20480\n"
"v_mfma_f32_16x16x32_bf16 v[16:19], a[36:39], v[32:35], v[16:19]\n"
"ds_read_b64_tr_b16 v[50:51], v117 offset:22528\n"
"v_mfma_f32_16x16x32_bf16 v[4:7], a[32:35], v[36:39], v[4:7]\n"
"buffer_load_dwordx4 a[0:3], v127, s[32:35], s16 offen offset:2048 sc1\n"
"ds_read_b64_tr_b16 v[52:53], v118 offset:20480\n"
"v_mfma_f32_16x16x32_bf16 v[20:23], a[36:39], v[36:39], v[20:23]\n"
"ds_read_b64_tr_b16 v[54:55], v118 offset:22528\n"
"s_waitcnt lgkmcnt(4)\n"
"v_mfma_f32_16x16x32_bf16 v[8:11], a[32:35], v[40:43], v[8:11]\n"
"ds_read_b64_tr_b16 v[56:57], v116 offset:20480\n"
"v_mfma_f32_16x16x32_bf16 v[24:27], a[36:39], v[40:43], v[24:27]\n"
"ds_read_b64_tr_b16 v[58:59], v116 offset:22528\n"
"v_mfma_f32_16x16x32_bf16 v[12:15], a[32:35], v[44:47], v[12:15]\n"
"buffer_load_dwordx4 a[4:7], v127, s[32:35], s16 offen offset:3072 sc1\n"
"ds_read_b64_tr_b16 v[60:61], v124 offset:20480\n"
"v_mfma_f32_16x16x32_bf16 v[28:31], a[36:39], v[44:47], v[28:31]\n"
"ds_read_b64_tr_b16 v[62:63], v124 offset:22528\n"
"s_waitcnt lgkmcnt(4)\n"
"v_mfma_f32_16x16x32_bf16 v[0:3], a[40:43], v[48:51], v[0:3]\n"
"ds_read_b64_tr_b16 v[32:33], v117 offset:24576\n"
"v_mfma_f32_16x16x32_bf16 v[16:19], a[44:47], v[48:51], v[16:19]\n"
"s_getpc_b64 s[98:99]\n"
".Lmg_pf10:\n"
"ds_read_b64_tr_b16 v[34:35], v117 offset:26624\n"
"v_mfma_f32_16x16x32_bf16 v[4:7], a[40:43], v[52:55], v[4:7]\n"
"buffer_load_dwordx4 a[8:11], v127, s[32:35], s17 offen offset:0 sc1\n"
"ds_read_b64_tr_b16 v[36:37], v118 offset:24576\n"
"v_mfma_f32_16x16x32_bf16 v[20:23], a[44:47], v[52:55], v[20:23]\n"
"ds_read_b64_tr_b16 v[38:39], v118 offset:26624\n"
"s_waitcnt lgkmcnt(4)\n"
"v_mfma_f32_16x16x32_bf16 v[8:11], a[40:43], v[56:59], v[8:11]\n"
"s_add_u32 s98, s98, .Lmg_nomean-.Lmg_pf10\n"
"ds_read_b64_tr_b16 v[40:41], v116 offset:24576\n"
"v_mfma_f32_16x16x32_bf16 v[24:27], a[44:47], v[56:59], v[24:27]\n"
"s_addc_u32 s99, s99, 0\n"
"ds_read_b64_tr_b16 v[42:43], v116 offset:26624\n"
"v_mfma_f32_16x16x32_bf16 v[12:15], a[40:43], v[60:63], v[12:15]\n"
"buffer_load_dwordx4 a[12:15], v127, s[32:35], s17 offen offset:1024 sc1\n"
"ds_read_b64_tr_b16 v[44:45], v124 offset:24576\n"
"v_mfma_f32_16x16x32_bf16 v[28:31], a[44:47], v[60:63], v[28:31]\n"
"ds_read_b64_tr_b16 v[46:47], v124 offset:26624\n"
"s_waitcnt lgkmcnt(4)\n"
"v_mfma_f32_16x16x32_bf16 v[0:3], a[48:51], v[32:35], v[0:3]\n"
"ds_read_b64_tr_b16 v[48:49], v117 offset:28672\n"
"v_mfma_f32_16x16x32_bf16 v[16:19], a[52:55], v[32:35], v[16:19]\n"
"s_lshl_b32 s55, s45, 9\n"
"ds_read_b64_tr_b16 v[50:51], v117 offset:30720\n"
"v_mfma_f32_16x16x32_bf16 v[4:7], a[48:51], v[36:39], v[4:7]\n"
"buffer_load_dwordx4 a[16:19], v127, s[32:35], s17 offen offset:2048 sc1\n"
"ds_read_b64_tr_b16 v[52:53], v118 offset:28672\n"
"v_mfma_f32_16x16x32_bf16 v[20:23], a[52:55], v[36:39], v[20:23]\n"
"ds_read_b64_tr_b16 v[54:55], v118 offset:30720\n"
"s_waitcnt lgkmcnt(4)\n"
"v_mfma_f32_16x16x32_bf16 v[8:11], a[48:51], v[40:43], v[8:11]\n"
"s_mov_b32 m0, 145408\n"
"ds_read_b64_tr_b16 v[56:57], v116 offset:28672\n"
"v_mfma_f32_16x16x32_bf16 v[24:27], a[52:55], v[40:43], v[24:27]\n"
"v_bfe_u32 v119, v127, 4, 2\n"
"ds_read_b64_tr_b16 v[58:59], v116 offset:30720\n"
"v_mfma_f32_16x16x32_bf16 v[12:15], a[48:51], v[44:47], v[12:15]\n"
"buffer_load_dwordx4 a[20:23], v127, s[32:35], s17 offen offset:3072 sc1\n"
"ds_read_b64_tr_b16 v[60:61], v124 offset:28672\n"
"v_mfma_f32_16x16x32_bf16 v[28:31], a[52:55], v[44:47], v[28:31]\n"
"ds_read_b64_tr_b16 v[62:63], v124 offset:30720\n"
"s_waitcnt lgkmcnt(4)\n"
"v_mfma_f32_16x16x32_bf16 v[0:3], a[56:59], v[48:51], v[0:3]\n"
"s_waitcnt vmcnt(14)\n"
"s_barrier\n"
"ds_read_b64_tr_b16 v[32:33], v117 offset:32768\n"
"v_mfma_f32_16x16x32_bf16 v[16:19], a[60:63], v[48:51], v[16:19]\n"
"v_lshl_add_u32 v119, v119, 7, s55\n"
"ds_read_b64_tr_b16 v[34:35], v117 offset:34816\n"
"v_mfma_f32_16x16x32_bf16 v[4:7], a[56:59], v[52:55], v[4:7]\n"
"buffer_load_dwordx4 a[24:27], v127, s[32:35], s18 offen offset:0 sc1\n"
"ds_read_b64_tr_b16 v[36:37], v118 offset:32768\n"
"v_mfma_f32_16x16x32_bf16 v[20:23], a[60:63], v[52:55], v[20:23]\n"
"ds_read_b64_tr_b16 v[38:39], v118 offset:34816\n"
"s_waitcnt lgkmcnt(4)\n"
"v_mfma_f32_16x16x32_bf16 v[8:11], a[56:59], v[56:59], v[8:11]\n"
"s_mov_b32 s55, .Lmg_mainend-.Lmg_nomean\n"
".Lmg_hopM0:\n"
"s_cbranch_execz .Lmg_noones\n"
"ds_read_b64_tr_b16 v[40:41], v116 offset:32768\n"
"v_mfma_f32_16x16x32_bf16 v[24:27], a[60:63], v[56:59], v[24:27]\n"
"v_min_u32 v119, s55, v119\n"
"ds_read_b64_tr_b16 v[42:43], v116 offset:34816\n"
"v_mfma_f32_16x16x32_bf16 v[12:15], a[56:59], v[60:63], v[12:15]\n"
"buffer_load_dwordx4 a[28:31], v127, s[32:35], s18 offen offset:1024 sc1\n"
"ds_read_b64_tr_b16 v[44:45], v124 offset:32768\n"
"v_mfma_f32_16x16x32_bf16 v[28:31], a[60:63], v[60:63], v[28:31]\n"
"ds_read_b64_tr_b16 v[46:47], v124 offset:34816\n"
"s_waitcnt lgkmcnt(4)\n"
"v_mfma_f32_16x16x32_bf16 v[0:3], a[64:67], v[32:35], v[0:3]\n"
"ds_read_b64_tr_b16 v[48:49], v117 offset:36864\n"
"v_mfma_f32_16x16x32_bf16 v[16:19], a[68:71], v[32:35], v[16:19]\n"
"global_load_lds_dword v119, s[98:99]\n"
"ds_read_b64_tr_b16 v[50:51], v117 offset:38912\n"
"v_mfma_f32_16x16x32_bf16 v[4:7], a[64:67], v[36:39], v[4:7]\n"
"buffer_load_dwordx4 a[32:35], v127, s[32:35], s18 offen offset:2048 sc1\n"
"ds_read_b64_tr_b16 v[52:53], v118 offset:36864\n"
"v_mfma_f32_16x16x32_bf16 v[20:23], a[68:71], v[36:39], v[20:23]\n"
"ds_read_b64_tr_b16 v[54:55], v118 offset:38912\n"
"s_waitcnt lgkmcnt(4)\n"
"v_mfma_f32_16x16x32_bf16 v[8:11], a[64:67], v[40:43], v[8:11]\n"
"ds_read_b64_tr_b16 v[56:57], v116 offset:36864\n"
"v_mfma_f32_16x16x32_bf16 v[24:27], a[68:71], v[40:43], v[24:27]\n"
"ds_read_b64_tr_b16 v[58:59], v116 offset:38912\n"
"v_mfma_f32_16x16x32_bf16 v[12:15], a[64:67], v[44:47], v[12:15]\n"
"buffer_load_dwordx4 a[36:39], v127, s[32:35], s18 offen offset:3072 sc1\n"
"ds_read_b64_tr_b16 v[60:61], v124 offset:36864\n"
"v_mfma_f32_16x16x32_bf16 v[28:31], a[68:71], v[44:47], v[28:31]\n"
"ds_read_b64_tr_b16 v[62:63], v124 offset:38912\n"
"s_waitcnt lgkmcnt(4)\n"
"v_mfma_f32_16x16x32_bf16 v[0:3], a[72:75], v[48:51], v[0:3]\n"
"ds_read_b64_tr_b16 v[32:33], v117 offset:40960\n"
"v_mfma_f32_16x16x32_bf16 v[16:19], a[76:79], v[48:51], v[16:19]\n"
"ds_read_b64_tr_b16 v[34:35], v117 offset:43008\n"
"v_mfma_f32_16x16x32_bf16 v[4:7], a[72:75], v[52:55], v[4:7]\n"
"buffer_load_dwordx4 a[40:43], v127, s[32:35], s19 offen offset:0 sc1\n"
"ds_read_b64_tr_b16 v[36:37], v118 offset:40960\n"
"v_mfma_f32_16x16x32_bf16 v[20:23], a[76:79], v[52:55], v[20:23]\n"
"ds_read_b64_tr_b16 v[38:39], v118 offset:43008\n"
"s_waitcnt lgkmcnt(4)\n"
"v_mfma_f32_16x16x32_bf16 v[8:11], a[72:75], v[56:59], v[8:11]\n"
"ds_read_b64_tr_b16 v[40:41], v116 offset:40960\n"
"v_mfma_f32_16x16x32_bf16 v[24:27], a[76:79], v[56:59], v[24:27]\n"
"ds_read_b64_tr_b16 v[42:43], v116 offset:43008\n"
"v_mfma_f32_16x16x32_bf16 v[12:15], a[72:75], v[60:63], v[12:15]\n"
"buffer_load_dwordx4 a[44:47], v127, s[32:35], s19 offen offset:1024 sc1\n"
"ds_read_b64_tr_b16 v[44:45], v124 offset:40960\n"
"v_mfma_f32_16x16x32_bf16 v[28:31], a[76:79], v[60:63], v[28:31]\n"
"ds_read_b64_tr_b16 v[46:47], v124 offset:43008\n"
"s_waitcnt lgkmcnt(4)\n"
"v_mfma_f32_16x16x32_bf16 v[0:3], a[80:83], v[32:35], v[0:3]\n"
"ds_read_b64_tr_b16 v[48:49], v117 offset:45056\n"
"v_mfma_f32_16x16x32_bf16 v[16:19], a[84:87], v[32:35], v[16:19]\n"
"ds_read_b64_tr_b16 v[50:51], v117 offset:47104\n"
"v_mfma_f32_16x16x32_bf16 v[4:7], a[80:83], v[36:39], v[4:7]\n"
"buffer_load_dwordx4 a[48:51], v127, s[32:35], s19 offen offset:2048 sc1\n"
"ds_read_b64_tr_b16 v[52:53], v118 offset:45056\n"
"v_mfma_f32_16x16x32_bf16 v[20:23], a[84:87], v[36:39], v[20:23]\n"
"ds_read_b64_tr_b16 v[54:55], v118 offset:47104\n"
"s_waitcnt lgkmcnt(4)\n"
"v_mfma_f32_16x16x32_bf16 v[8:11], a[80:83], v[40:43], v[8:11]\n"
"ds_read_b64_tr_b16 v[56:57], v116 offset:45056\n"
"v_mfma_f32_16x16x32_bf16 v[24:27], a[84:87], v[40:43], v[24:27]\n"
"ds_read_b64_tr_b16 v[58:59], v116 offset:47104\n"
"v_mfma_f32_16x16x32_bf16 v[12:15], a[80:83], v[44:47], v[12:15]\n"
"buffer_load_dwordx4 a[52:55], v127, s[32:35], s19 offen offset:3072 sc1\n"
"ds_read_b64_tr_b16 v[60:61], v124 offset:45056\n"
"v_mfma_f32_16x16x32_bf16 v[28:31], a[84:87], v[44:47], v[28:31]\n"
"ds_read_b64_tr_b16 v[62:63], v124 offset:47104\n"
"s_waitcnt lgkmcnt(4)\n"
"v_mfma_f32_16x16x32_bf16 v[0:3], a[88:91], v[48:51], v[0:3]\n"
"s_waitcnt vmcnt(19)\n"
"s_barrier\n"
"ds_read_b64_tr_b16 v[32:33], v117 offset:49152\n"
"v_mfma_f32_16x16x32_bf16 v[16:19], a[92:95], v[48:51], v[16:19]\n"
"ds_read_b64_tr_b16 v[34:35], v117 offset:51200\n"
"v_mfma_f32_16x16x32_bf16 v[4:7], a[88:91], v[52:55], v[4:7]\n"
"buffer_load_dwordx4 a[56:59], v127, s[32:35], s20 offen offset:0 sc1\n"
"ds_read_b64_tr_b16 v[36:37], v118 offset:49152\n"
"v_mfma_f32_16x16x32_bf16 v[20:23], a[92:95], v[52:55], v[20:23]\n"
"ds_read_b64_tr_b16 v[38:39], v118 offset:51200\n"
"s_waitcnt lgkmcnt(4)\n"
"v_mfma_f32_16x16x32_bf16 v[8:11], a[88:91], v[56:59], v[8:11]\n"
"ds_read_b64_tr_b16 v[40:41], v116 offset:49152\n"
"v_mfma_f32_16x16x32_bf16 v[24:27], a[92:95], v[56:59], v[24:27]\n"
"ds_read_b64_tr_b16 v[42:43], v116 offset:51200\n"
"v_mfma_f32_16x16x32_bf16 v[12:15], a[88:91], v[60:63], v[12:15]\n"
"buffer_load_dwordx4 a[60:63], v127, s[32:35], s20 offen offset:1024 sc1\n"
"ds_read_b64_tr_b16 v[44:45], v124 offset:49152\n"
"v_mfma_f32_16x16x32_bf16 v[28:31], a[92:95], v[60:63], v[28:31]\n"
"ds_read_b64_tr_b16 v[46:47], v124 offset:51200\n"
"s_waitcnt lgkmcnt(4)\n"
"v_mfma_f32_16x16x32_bf16 v[0:3], a[96:99], v[32:35], v[0:3]\n"
"ds_read_b64_tr_b16 v[48:49], v117 offset:53248\n"
"v_mfma_f32_16x16x32_bf16 v[16:19], a[100:103], v[32:35], v[16:19]\n"
"v_bfe_u32 v120, v126, 4, 2\n"
"ds_read_b64_tr_b16 v[50:51], v117 offset:55296\n"
"v_mfma_f32_16x16x32_bf16 v[4:7], a[96:99], v[36:39], v[4:7]\n"
"buffer_load_dwordx4 a[64:67], v127, s[32:35], s20 offen offset:2048 sc1\n"
"ds_read_b64_tr_b16 v[52:53], v118 offset:53248\n"
"v_mfma_f32_16x16x32_bf16 v[20:23], a[100:103], v[36:39], v[20:23]\n"
"ds_read_b64_tr_b16 v[54:55], v118 offset:55296\n"
"s_waitcnt lgkmcnt(4)\n"
"v_mfma_f32_16x16x32_bf16 v[8:11], a[96:99], v[40:43], v[8:11]\n"
"ds_read_b64_tr_b16 v[56:57], v116 offset:53248\n"
"v_mfma_f32_16x16x32_bf16 v[24:27], a[100:103], v[40:43], v[24:27]\n"
"v_lshrrev_b32 v121, 2, v127\n"
"ds_read_b64_tr_b16 v[58:59], v116 offset:55296\n"
"v_mfma_f32_16x16x32_bf16 v[12:15], a[96:99], v[44:47], v[12:15]\n"
"buffer_load_dwordx4 a[68:71], v127, s[32:35], s20 offen offset:3072 sc1\n"
"ds_read_b64_tr_b16 v[60:61], v124 offset:53248\n"
"v_mfma_f32_16x16x32_bf16 v[28:31], a[100:103], v[44:47], v[28:31]\n"
"ds_read_b64_tr_b16 v[62:63], v124 offset:55296\n"
"s_waitcnt lgkmcnt(4)\n"
"v_mfma_f32_16x16x32_bf16 v[0:3], a[104:107], v[48:51], v[0:3]\n"
"ds_read_b64_tr_b16 v[32:33], v117 offset:57344\n"
"v_mfma_f32_16x16x32_bf16 v[16:19], a[108:111], v[48:51], v[16:19]\n"
"v_add_u32 v121, 81920, v121\n"
"ds_read_b64_tr_b16 v[34:35], v117 offset:59392\n"
"v_mfma_f32_16x16x32_bf16 v[4:7], a[104:107], v[52:55], v[4:7]\n"
"buffer_load_dwordx4 a[72:75], v127, s[32:35], s21 offen offset:0 sc1\n"
"ds_read_b64_tr_b16 v[36:37], v118 offset:57344\n"
"v_mfma_f32_16x16x32_bf16 v[20:23], a[108:111], v[52:55], v[20:23]\n"
"ds_read_b64_tr_b16 v[38:39], v118 offset:59392\n"
"s_waitcnt lgkmcnt(4)\n"
"v_mfma_f32_16x16x32_bf16 v[8:11], a[104:107], v[56:59], v[8:11]\n"
"ds_read_b64_tr_b16 v[40:41], v116 offset:57344\n"
"v_mfma_f32_16x16x32_bf16 v[24:27], a[108:111], v[56:59], v[24:27]\n"
"v_cmp_eq_u32 vcc, 1, v120\n"
"ds_read_b64_tr_b16 v[42:43], v116 offset:59392\n"
"v_mfma_f32_16x16x32_bf16 v[12:15], a[104:107], v[60:63], v[12:15]\n"
"buffer_load_dwordx4 a[76:79], v127, s[32:35], s21 offen offset:1024 sc1\n"
"ds_read_b64_tr_b16 v[44:45], v124 offset:57344\n"
"v_mfma_f32_16x16x32_bf16 v[28:31], a[108:111], v[60:63], v[28:31]\n"
"ds_read_b64_tr_b16 v[46:47], v124 offset:59392\n"
"s_waitcnt lgkmcnt(4)\n"
"v_mfma_f32_16x16x32_bf16 v[0:3], a[112:115], v[32:35], v[0:3]\n"
"ds_read_b64_tr_b16 v[48:49], v117 offset:61440\n"
"v_mfma_f32_16x16x32_bf16 v[16:19], a[116:119], v[32:35], v[16:19]\n"
"v_cmp_eq_u32 s[0:1], 2, v120\n"
"ds_read_b64_tr_b16 v[50:51], v117 offset:63488\n"
"v_mfma_f32_16x16x32_bf16 v[4:7], a[112:115], v[36:39], v[4:7]\n"
"buffer_load_dwordx4 a[80:83], v127, s[40:43], s21 offen offset:2048 sc1\n"
"ds_read_b64_tr_b16 v[52:53], v118 offset:61440\n"
"v_mfma_f32_16x16x32_bf16 v[20:23], a[116:119], v[36:39], v[20:23]\n"
"ds_read_b64_tr_b16 v[54:55], v118 offset:63488\n"
"s_waitcnt lgkmcnt(4)\n"
"v_mfma_f32_16x16x32_bf16 v[8:11], a[112:115], v[40:43], v[8:11]\n"
"ds_read_b64_tr_b16 v[56:57], v116 offset:61440\n"
"v_mfma_f32_16x16x32_bf16 v[24:27], a[116:119], v[40:43], v[24:27]\n"
"v_cmp_eq_u32 s[96:97], 3, v120\n"
"ds_read_b64_tr_b16 v[58:59], v116 offset:63488\n"
"v_mfma_f32_16x16x32_bf16 v[12:15], a[112:115], v[44:47], v[12:15]\n"
"buffer_load_dwordx4 a[84:87], v127, s[40:43], s21 offen offset:3072 sc1\n"
"ds_read_b64_tr_b16 v[60:61], v124 offset:61440\n"
"v_mfma_f32_16x16x32_bf16 v[28:31], a[116:119], v[44:47], v[28:31]\n"
"ds_read_b64_tr_b16 v[62:63], v124 offset:63488\n"
"s_waitcnt lgkmcnt(4)\n"
"v_mfma_f32_16x16x32_bf16 v[0:3], a[120:123], v[48:51], v[0:3]\n"
"s_waitcnt vmcnt(23)\n"
"s_barrier\n"
"ds_read_b64_tr_b16 v[32:33], v112 offset:0\n"
"v_mfma_f32_16x16x32_bf16 v[16:19], a[124:127], v[48:51], v[16:19]\n"
"v_lshrrev_b32 v119, 2, v127\n"
"ds_read_b64_tr_b16 v[34:35], v112 offset:2048\n"
"v_mfma_f32_16x16x32_bf16 v[4:7], a[120:123], v[52:55], v[4:7]\n"
"buffer_load_dwordx4 a[88:91], v127, s[40:43], s4 offen offset:0 sc1\n"
"ds_read_b64_tr_b16 v[36:37], v113 offset:0\n"
"v_mfma_f32_16x16x32_bf16 v[20:23], a[124:127], v[52:55], v[20:23]\n"
"ds_read_b64_tr_b16 v[38:39], v113 offset:2048\n"
"s_waitcnt lgkmcnt(4)\n"
"v_mfma_f32_16x16x32_bf16 v[8:11], a[120:123], v[56:59], v[8:11]\n"
"ds_read_b64_tr_b16 v[40:41], v114 offset:0\n"
"v_mfma_f32_16x16x32_bf16 v[24:27], a[124:127], v[56:59], v[24:27]\n"
"v_add_u32 v119, 4096, v119\n"
"ds_read_b64_tr_b16 v[42:43], v114 offset:2048\n"
"v_mfma_f32_16x16x32_bf16 v[12:15], a[120:123], v[60:63], v[12:15]\n"
"buffer_load_dwordx4 a[92:95], v127, s[40:43], s4 offen offset:1024 sc1\n"
"ds_read_b64_tr_b16 v[44:45], v115 offset:0\n"
"v_mfma_f32_16x16x32_bf16 v[28:31], a[124:127], v[60:63], v[28:31]\n"
"ds_read_b64_tr_b16 v[46:47], v115 offset:2048\n"
"s_waitcnt lgkmcnt(4)\n"
"v_mfma_f32_16x16x32_bf16 v[0:3], a[128:131], v[32:35], v[0:3]\n"
"ds_read_b64_tr_b16 v[48:49], v112 offset:4096\n"
"v_mfma_f32_16x16x32_bf16 v[16:19], a[132:135], v[32:35], v[16:19]\n"
"s_mul_i32 s61, s31, 0x9e3779b1\n"
"ds_read_b64_tr_b16 v[50:51], v112 offset:6144\n"
"v_mfma_f32_16x16x32_bf16 v[4:7], a[128:131], v[36:39], v[4:7]\n"
"buffer_load_dwordx4 a[96:99], v127, s[40:43], s4 offen offset:2048 sc1\n"
"ds_read_b64_tr_b16 v[52:53], v113 offset:4096\n"
"v_mfma_f32_16x16x32_bf16 v[20:23], a[132:135], v[36:39], v[20:23]\n"
"ds_read_b64_tr_b16 v[54:55], v113 offset:6144\n"
"s_waitcnt lgkmcnt(4)\n"
"v_mfma_f32_16x16x32_bf16 v[8:11], a[128:131], v[40:43], v[8:11]\n"
"ds_read_b64_tr_b16 v[56:57], v114 offset:4096\n"
"v_mfma_f32_16x16x32_bf16 v[24:27], a[132:135], v[40:43], v[24:27]\n"
"ds_read_b64_tr_b16 v[58:59], v114 offset:6144\n"
"v_mfma_f32_16x16x32_bf16 v[12:15], a[128:131], v[44:47], v[12:15]\n"
"buffer_load_dwordx4 a[100:103], v127, s[32:35], s4 offen offset:3072 sc1\n"
"ds_read_b64_tr_b16 v[60:61], v115 offset:4096\n"
"v_mfma_f32_16x16x32_bf16 v[28:31], a[132:135], v[44:47], v[28:31]\n"
"ds_read_b64_tr_b16 v[62:63], v115 offset:6144\n"
"s_waitcnt lgkmcnt(4)\n"
"v_mfma_f32_16x16x32_bf16 v[0:3], a[136:139], v[48:51], v[0:3]\n"
"ds_read_b64_tr_b16 v[32:33], v112 offset:8192\n"
"v_mfma_f32_16x16x32_bf16 v[16:19], a[140:143], v[48:51], v[16:19]\n"
"ds_read_b64_tr_b16 v[34:35], v112 offset:10240\n"
"v_mfma_f32_16x16x32_bf16 v[4:7], a[136:139], v[52:55], v[4:7]\n"
"buffer_load_dwordx4 a[104:107], v127, s[32:35], s5 offen offset:0 sc1\n"
"ds_read_b64_tr_b16 v[36:37], v113 offset:8192\n"
"v_mfma_f32_16x16x32_bf16 v[20:23], a[140:143], v[52:55], v[20:23]\n"
"ds_read_b64_tr_b16 v[38:39], v113 offset:10240\n"
"s_waitcnt lgkmcnt(4)\n"
"v_mfma_f32_16x16x32_bf16 v[8:11], a[136:139], v[56:59], v[8:11]\n"
"ds_read_b64_tr_b16 v[40:41], v114 offset:8192\n"
"v_mfma_f32_16x16x32_bf16 v[24:27], a[140:143], v[56:59], v[24:27]\n"
"ds_read_b64_tr_b16 v[42:43], v114 offset:10240\n"
"v_mfma_f32_16x16x32_bf16 v[12:15], a[136:139], v[60:63], v[12:15]\n"
"buffer_load_dwordx4 a[108:111], v127, s[32:35], s5 offen offset:1024 sc1\n"
"ds_read_b64_tr_b16 v[44:45], v115 offset:8192\n"
"v_mfma_f32_16x16x32_bf16 v[28:31], a[140:143], v[60:63], v[28:31]\n"
"ds_read_b64_tr_b16 v[46:47], v115 offset:10240\n"
"s_waitcnt lgkmcnt(4)\n"
"v_mfma_f32_16x16x32_bf16 v[0:3], a[144:147], v[32:35], v[0:3]\n"
"ds_read_b64_tr_b16 v[48:49], v112 offset:12288\n"
"v_mfma_f32_16x16x32_bf16 v[16:19], a[148:151], v[32:35], v[16:19]\n"
"ds_read_b64_tr_b16 v[50:51], v112 offset:14336\n"
"v_mfma_f32_16x16x32_bf16 v[4:7], a[144:147], v[36:39], v[4:7]\n"
"buffer_load_dwordx4 a[112:115], v127, s[32:35], s5 offen offset:2048 sc1\n"
"ds_read_b64_tr_b16 v[52:53], v113 offset:12288\n"
"v_mfma_f32_16x16x32_bf16 v[20:23], a[148:151], v[36:39], v[20:23]\n"
"ds_read_b64_tr_b16 v[54:55], v113 offset:14336\n"
"s_waitcnt lgkmcnt(4)\n"
"v_mfma_f32_16x16x32_bf16 v[8:11], a[144:147], v[40:43], v[8:11]\n"
"ds_read_b64_tr_b16 v[56:57], v114 offset:12288\n"
"v_mfma_f32_16x16x32_bf16 v[24:27], a[148:151], v[40:43], v[24:27]\n"
"ds_read_b64_tr_b16 v[58:59], v114 offset:14336\n"
"v_mfma_f32_16x16x32_bf16 v[12:15], a[144:147], v[44:47], v[12:15]\n"
"buffer_load_dwordx4 a[116:119], v127, s[32:35], s5 offen offset:3072 sc1\n"
"ds_read_b64_tr_b16 v[60:61], v115 offset:12288\n"
"v_mfma_f32_16x16x32_bf16 v[28:31], a[148:151], v[44:47], v[28:31]\n"
"ds_read_b64_tr_b16 v[62:63], v115 offset:14336\n"
"s_waitcnt lgkmcnt(4)\n"
"v_mfma_f32_16x16x32_bf16 v[0:3], a[152:155], v[48:51], v[0:3]\n"
"ds_read_b64_tr_b16 v[32:33], v117 offset:0\n"
"v_mfma_f32_16x16x32_bf16 v[16:19], a[156:159], v[48:51], v[16:19]\n"
"ds_read_b64_tr_b16 v[34:35], v117 offset:2048\n"
"v_mfma_f32_16x16x32_bf16 v[4:7], a[152:155], v[52:55], v[4:7]\n"
"buffer_load_dwordx4 a[120:123], v127, s[32:35], s6 offen offset:0 sc1\n"
"ds_read_b64_tr_b16 v[36:37], v118 offset:0\n"
"v_mfma_f32_16x16x32_bf16 v[20:23], a[156:159], v[52:55], v[20:23]\n"
"ds_read_b64_tr_b16 v[38:39], v118 offset:2048\n"
"s_waitcnt lgkmcnt(4)\n"
"v_mfma_f32_16x16x32_bf16 v[8:11], a[152:155], v[56:59], v[8:11]\n"
"ds_read_b64_tr_b16 v[40:41], v116 offset:0\n"
"v_mfma_f32_16x16x32_bf16 v[24:27], a[156:159], v[56:59], v[24:27]\n"
"ds_read_b64_tr_b16 v[42:43], v116 offset:2048\n"
"v_mfma_f32_16x16x32_bf16 v[12:15], a[152:155], v[60:63], v[12:15]\n"
"buffer_load_dwordx4 a[124:127], v127, s[32:35], s6 offen offset:1024 sc1\n"
"ds_read_b64_tr_b16 v[44:45], v124 offset:0\n"
"v_mfma_f32_16x16x32_bf16 v[28:31], a[156:159], v[60:63], v[28:31]\n"
"ds_read_b64_tr_b16 v[46:47], v124 offset:2048\n"
"s_cmp_lg_u32 s30, 3\n"
"s_cbranch_scc1 .Lmg_nomean\n"
"s_cmp_lg_u32 s2, 0\n"
"s_cbranch_scc1 .Lmg_noep\n"
"v_mov_b32 v122, s31\n"
"v_mov_b32 v123, 0\n"
"buffer_store_dword v122, v123, s[32:35], 0 offen sc1\n"
".Lmg_noep:\n"
"v_cndmask_b32 v0, v0, v4, vcc\n"
"v_cndmask_b32 v0, v0, v8, s[0:1]\n"
"v_cndmask_b32 v0, v0, v12, s[96:97]\n"
"v_mul_f32 v0, 0x3acccccd, v0\n"
"v_cvt_pk_bf16_f32 v1, -v0, -v0\n"
"v_and_b32 v1, 0xffff0000, v1\n"
"ds_write_b32 v121, v1\n"
"ds_write_b32 v121, v0 offset:512\n"
"s_waitcnt lgkmcnt(0)\n"
".Lmg_nomean:\n"
"s_waitcnt lgkmcnt(4)\n"
"v_mfma_f32_16x16x32_bf16 v[80:83], v[192:195], v[32:35], 0\n"
"ds_read_b64_tr_b16 v[48:49], v117 offset:4096\n"
"v_mfma_f32_16x16x32_bf16 v[96:99], v[196:199], v[32:35], 0\n"
"ds_read_b64_tr_b16 v[50:51], v117 offset:6144\n"
"v_mfma_f32_16x16x32_bf16 v[84:87], v[192:195], v[36:39], 0\n"
"buffer_load_dwordx4 a[128:131], v127, s[32:35], s6 offen offset:2048 sc1\n"
"ds_read_b64_tr_b16 v[52:53], v118 offset:4096\n"
"v_mfma_f32_16x16x32_bf16 v[100:103], v[196:199], v[36:39], 0\n"
"ds_read_b64_tr_b16 v[54:55], v118 offset:6144\n"
"s_waitcnt lgkmcnt(4)\n"
"v_mfma_f32_16x16x32_bf16 v[88:91], v[192:195], v[40:43], 0\n"
"ds_read_b64_tr_b16 v[56:57], v116 offset:4096\n"
"v_mfma_f32_16x16x32_bf16 v[104:107], v[196:199], v[40:43], 0\n"
"ds_read_b64_tr_b16 v[58:59], v116 offset:6144\n"
"v_mfma_f32_16x16x32_bf16 v[92:95], v[192:195], v[44:47], 0\n"
"buffer_load_dwordx4 a[132:135], v127, s[32:35], s6 offen offset:3072 sc1\n"
"ds_read_b64_tr_b16 v[60:61], v124 offset:4096\n"
"v_mfma_f32_16x16x32_bf16 v[108:111], v[196:199], v[44:47], 0\n"
"ds_read_b64_tr_b16 v[62:63], v124 offset:6144\n"
"s_waitcnt lgkmcnt(4)\n"
"v_mfma_f32_16x16x32_bf16 v[80:83], v[200:203], v[48:51], v[80:83]\n"
"ds_read_b64_tr_b16 v[32:33], v117 offset:8192\n"
"v_mfma_f32_16x16x32_bf16 v[96:99], v[204:207], v[48:51], v[96:99]\n"
"ds_read_b64_tr_b16 v[34:35], v117 offset:10240\n"
"v_mfma_f32_16x16x32_bf16 v[84:87], v[200:203], v[52:55], v[84:87]\n"
"buffer_load_dwordx4 a[136:139], v127, s[32:35], s7 offen offset:0 sc1\n"
"ds_read_b64_tr_b16 v[36:37], v118 offset:8192\n"
"v_mfma_f32_16x16x32_bf16 v[100:103], v[204:207], v[52:55], v[100:103]\n"
"s_barrier\n"
"ds_read_b32 v120, v125 offset:0\n"
"ds_read_b32 v121, v125 offset:64\n"
"ds_read_b32 v122, v125 offset:128\n"
"ds_read_b32 v123, v125 offset:192\n"
"ds_read_b64_tr_b16 v[38:39], v118 offset:10240\n"
"s_waitcnt lgkmcnt(8)\n"
"v_mfma_f32_16x16x32_bf16 v[88:91], v[200:203], v[56:59], v[88:91]\n"
"ds_read_b64_tr_b16 v[40:41], v116 offset:8192\n"
"v_mfma_f32_16x16x32_bf16 v[104:107], v[204:207], v[56:59], v[104:107]\n"
"ds_read_b64_tr_b16 v[42:43], v116 offset:10240\n"
"v_mfma_f32_16x16x32_bf16 v[92:95], v[200:203], v[60:63], v[92:95]\n"
"buffer_load_dwordx4 a[140:143], v127, s[32:35], s7 offen offset:1024 sc1\n"
"ds_read_b64_tr_b16 v[44:45], v124 offset:8192\n"
"v_mfma_f32_16x16x32_bf16 v[108:111], v[204:207], v[60:63], v[108:111]\n"
"ds_read_b64_tr_b16 v[46:47], v124 offset:10240\n"
"s_waitcnt lgkmcnt(4)\n"
"v_mfma_f32_16x16x32_bf16 v[80:83], v[208:211], v[32:35], v[80:83]\n"
"v_pk_fma_f32 v[0:1], v[64:65], v[120:121], v[0:1] op_sel:[0,0,0] op_sel_hi:[1,0,1]\n"
"v_pk_fma_f32 v[2:3], v[66:67], v[120:121], v[2:3] op_sel:[0,0,0] op_sel_hi:[1,0,1]\n"
"v_pk_fma_f32 v[16:17], v[68:69], v[120:121], v[16:17] op_sel:[0,0,0] op_sel_hi:[1,0,1]\n"
"v_pk_fma_f32 v[18:19], v[70:71], v[120:121], v[18:19] op_sel:[0,0,0] op_sel_hi:[1,0,1]\n"
"v_cvt_pk_bf16_f32 v0, v0, v1\n"
"v_cvt_pk_bf16_f32 v1, v2, v3\n"
"v_cvt_pk_bf16_f32 v2, v16, v17\n"
"v_cvt_pk_bf16_f32 v3, v18, v19\n"
"v_pk_max_i16 v0, v0, 0\n"
"v_pk_max_i16 v1, v1, 0\n"
"v_pk_max_i16 v2, v2, 0\n"
"v_pk_max_i16 v3, v3, 0\n"
"buffer_store_dwordx4 v[0:3], v127, s[56:59], s3 offen offset:0 sc1\n"
"v_add3_u32 v16, v0, v1, s61\n"
"v_add3_u32 v16, v2, v3, v16\n"
"v_pk_fma_f32 v[4:5], v[64:65], v[120:121], v[4:5] op_sel:[0,1,0] op_sel_hi:[1,1,1]\n"
"v_pk_fma_f32 v[6:7], v[66:67], v[120:121], v[6:7] op_sel:[0,1,0] op_sel_hi:[1,1,1]\n"
"v_pk_fma_f32 v[20:21], v[68:69], v[120:121], v[20:21] op_sel:[0,1,0] op_sel_hi:[1,1,1]\n"
"v_pk_fma_f32 v[22:23], v[70:71], v[120:121], v[22:23] op_sel:[0,1,0] op_sel_hi:[1,1,1]\n"
"v_cvt_pk_bf16_f32 v4, v4, v5\n"
"v_cvt_pk_bf16_f32 v5, v6, v7\n"
"v_cvt_pk_bf16_f32 v6, v20, v21\n"
"v_cvt_pk_bf16_f32 v7, v22, v23\n"
"v_pk_max_i16 v4, v4, 0\n"
"v_pk_max_i16 v5, v5, 0\n"
"v_pk_max_i16 v6, v6, 0\n"
"v_pk_max_i16 v7, v7, 0\n"
"buffer_store_dwordx4 v[4:7], v127, s[56:59], s3 offen offset:1024 sc1\n"
"v_add3_u32 v16, v4, v5, v16\n"
"v_add3_u32 v16, v6, v7, v16\n"
"v_pk_fma_f32 v[8:9], v[64:65], v[122:123], v[8:9] op_sel:[0,0,0] op_sel_hi:[1,0,1]\n"
"v_pk_fma_f32 v[10:11], v[66:67], v[122:123], v[10:11] op_sel:[0,0,0] op_sel_hi:[1,0,1]\n"
"v_pk_fma_f32 v[24:25], v[68:69], v[122:123], v[24:25] op_sel:[0,0,0] op_sel_hi:[1,0,1]\n"
"v_pk_fma_f32 v[26:27], v[70:71], v[122:123], v[26:27] op_sel:[0,0,0] op_sel_hi:[1,0,1]\n"
"v_cvt_pk_bf16_f32 v8, v8, v9\n"
"v_cvt_pk_bf16_f32 v9, v10, v11\n"
"v_cvt_pk_bf16_f32 v10, v24, v25\n"
"v_cvt_pk_bf16_f32 v11, v26, v27\n"
"v_pk_max_i16 v8, v8, 0\n"
"v_pk_max_i16 v9, v9, 0\n"
"v_pk_max_i16 v10, v10, 0\n"
"v_pk_max_i16 v11, v11, 0\n"
"buffer_store_dwordx4 v[8:11], v127, s[56:59], s3 offen offset:2048 sc1\n"
"v_add3_u32 v16, v8, v9, v16\n"
"v_add3_u32 v16, v10, v11, v16\n"
"v_pk_fma_f32 v[12:13], v[64:65], v[122:123], v[12:13] op_sel:[0,1,0] op_sel_hi:[1,1,1]\n"
"v_pk_fma_f32 v[14:15], v[66:67], v[122:123], v[14:15] op_sel:[0,1,0] op_sel_hi:[1,1,1]\n"
"v_pk_fma_f32 v[28:29], v[68:69], v[122:123], v[28:29] op_sel:[0,1,0] op_sel_hi:[1,1,1]\n"
"v_pk_fma_f32 v[30:31], v[70:71], v[122:123], v[30:31] op_sel:[0,1,0] op_sel_hi:[1,1,1]\n"
"v_cvt_pk_bf16_f32 v12, v12, v13\n"
"v_cvt_pk_bf16_f32 v13, v14, v15\n"
"v_cvt_pk_bf16_f32 v14, v28, v29\n"
"v_cvt_pk_bf16_f32 v15, v30, v31\n"
"v_pk_max_i16 v12, v12, 0\n"
"v_pk_max_i16 v13, v13, 0\n"
"v_pk_max_i16 v14, v14, 0\n"
"v_pk_max_i16 v15, v15, 0\n"
"buffer_store_dwordx4 v[12:15], v127, s[56:59], s3 offen offset:3072 sc1\n"
"v_add3_u32 v16, v12, v13, v16\n"
"v_add3_u32 v16, v14, v15, v16\n"
"buffer_store_dword v16, v119, s[56:59], s3 offen sc1\n"
"ds_read_b64_tr_b16 v[48:49], v117 offset:12288\n"
"v_mfma_f32_16x16x32_bf16 v[96:99], v[212:215], v[32:35], v[96:99]\n"
"ds_read_b64_tr_b16 v[50:51], v117 offset:14336\n"
"v_mfma_f32_16x16x32_bf16 v[84:87], v[208:211], v[36:39], v[84:87]\n"
"buffer_load_dwordx4 a[144:147], v127, s[32:35], s7 offen offset:2048 sc1\n"
"ds_read_b64_tr_b16 v[52:53], v118 offset:12288\n"
"v_mfma_f32_16x16x32_bf16 v[100:103], v[212:215], v[36:39], v[100:103]\n"
"ds_read_b64_tr_b16 v[54:55], v118 offset:14336\n"
"s_waitcnt lgkmcnt(4)\n"
"v_mfma_f32_16x16x32_bf16 v[88:91], v[208:211], v[40:43], v[88:91]\n"
"ds_read_b64_tr_b16 v[56:57], v116 offset:12288\n"
"v_mfma_f32_16x16x32_bf16 v[104:107], v[212:215], v[40:43], v[104:107]\n"
"ds_read_b64_tr_b16 v[58:59], v116 offset:14336\n"
"v_mfma_f32_16x16x32_bf16 v[92:95], v[208:211], v[44:47], v[92:95]\n"
"buffer_load_dwordx4 a[148:151], v127, s[32:35], s7 offen offset:3072 sc1\n"
"ds_read_b64_tr_b16 v[60:61], v124 offset:12288\n"
"v_mfma_f32_16x16x32_bf16 v[108:111], v[212:215], v[44:47], v[108:111]\n"
"ds_read_b64_tr_b16 v[62:63], v124 offset:14336\n"
"s_waitcnt lgkmcnt(4)\n"
"v_mfma_f32_16x16x32_bf16 v[80:83], v[216:219], v[48:51], v[80:83]\n"
"ds_read_b64_tr_b16 v[32:33], v117 offset:16384\n"
"v_mfma_f32_16x16x32_bf16 v[96:99], v[220:223], v[48:51], v[96:99]\n"
"s_lshl_b32 s53, s30, 10\n"
"ds_read_b64_tr_b16 v[34:35], v117 offset:18432\n"
"v_mfma_f32_16x16x32_bf16 v[84:87], v[216:219], v[52:55], v[84:87]\n"
"buffer_load_dwordx4 a[152:155], v127, s[32:35], s8 offen offset:0 sc1\n"
"ds_read_b64_tr_b16 v[36:37], v118 offset:16384\n"
"v_mfma_f32_16x16x32_bf16 v[100:103], v[220:223], v[52:55], v[100:103]\n"
"ds_read_b64_tr_b16 v[38:39], v118 offset:18432\n"
"s_waitcnt lgkmcnt(4)\n"
"v_mfma_f32_16x16x32_bf16 v[88:91], v[216:219], v[56:59], v[88:91]\n"
"v_add_u32 v20, s53, v127\n"
"ds_read_b64_tr_b16 v[40:41], v116 offset:16384\n"
"v_mfma_f32_16x16x32_bf16 v[104:107], v[220:223], v[56:59], v[104:107]\n"
"v_xor_b32 v21, 1024, v20\n"
"ds_read_b64_tr_b16 v[42:43], v116 offset:18432\n"
"v_mfma_f32_16x16x32_bf16 v[92:95], v[216:219], v[60:63], v[92:95]\n"
"buffer_load_dwordx4 a[156:159], v127, s[32:35], s8 offen offset:1024 sc1\n"
"ds_read_b64_tr_b16 v[44:45], v124 offset:16384\n"
"v_mfma_f32_16x16x32_bf16 v[108:111], v[220:223], v[60:63], v[108:111]\n"
"ds_read_b64_tr_b16 v[46:47], v124 offset:18432\n"
"s_waitcnt lgkmcnt(4)\n"
"v_mfma_f32_16x16x32_bf16 v[80:83], v[224:227], v[32:35], v[80:83]\n"
"ds_read_b64_tr_b16 v[48:49], v117 offset:20480\n"
"v_mfma_f32_16x16x32_bf16 v[96:99], v[228:231], v[32:35], v[96:99]\n"
"v_xor_b32 v22, 2048, v20\n"
"ds_read_b64_tr_b16 v[50:51], v117 offset:22528\n"
"v_mfma_f32_16x16x32_bf16 v[84:87], v[224:227], v[36:39], v[84:87]\n"
"buffer_load_dwordx4 v[168:171], v127, s[32:35], s8 offen offset:2048 sc1\n"
"ds_read_b64_tr_b16 v[52:53], v118 offset:20480\n"
"v_mfma_f32_16x16x32_bf16 v[100:103], v[228:231], v[36:39], v[100:103]\n"
"ds_read_b64_tr_b16 v[54:55], v118 offset:22528\n"
"s_waitcnt lgkmcnt(4)\n"
"v_mfma_f32_16x16x32_bf16 v[88:91], v[224:227], v[40:43], v[88:91]\n"
"v_xor_b32 v23, 3072, v20\n"
"ds_read_b64_tr_b16 v[56:57], v116 offset:20480\n"
"v_mfma_f32_16x16x32_bf16 v[104:107], v[228:231], v[40:43], v[104:107]\n"
"v_bfe_u32 v24, v126, 4, 2\n"
"ds_read_b64_tr_b16 v[58:59], v116 offset:22528\n"
"v_mfma_f32_16x16x32_bf16 v[92:95], v[224:227], v[44:47], v[92:95]\n"
"buffer_load_dwordx4 v[172:175], v127, s[32:35], s8 offen offset:3072 sc1\n"
"ds_read_b64_tr_b16 v[60:61], v124 offset:20480\n"
"v_mfma_f32_16x16x32_bf16 v[108:111], v[228:231], v[44:47], v[108:111]\n"
"ds_read_b64_tr_b16 v[62:63], v124 offset:22528\n"
"s_waitcnt lgkmcnt(4)\n"
"v_mfma_f32_16x16x32_bf16 v[80:83], v[232:235], v[48:51], v[80:83]\n"
"ds_read_b64_tr_b16 v[32:33], v117 offset:24576\n"
"v_mfma_f32_16x16x32_bf16 v[96:99], v[236:239], v[48:51], v[96:99]\n"
"v_bfe_u32 v25, v126, 2, 2\n"
"ds_read_b64_tr_b16 v[34:35], v117 offset:26624\n"
"v_mfma_f32_16x16x32_bf16 v[84:87], v[232:235], v[52:55], v[84:87]\n"
"buffer_load_dwordx4 v[176:179], v127, s[32:35], s9 offen offset:0 sc1\n"
"ds_read_b64_tr_b16 v[36:37], v118 offset:24576\n"
"v_mfma_f32_16x16x32_bf16 v[100:103], v[236:239], v[52:55], v[100:103]\n"
"ds_read_b64_tr_b16 v[38:39], v118 offset:26624\n"
"s_waitcnt lgkmcnt(4)\n"
"v_mfma_f32_16x16x32_bf16 v[88:91], v[232:235], v[56:59], v[88:91]\n"
"v_and_b32 v26, 3, v126\n"
"ds_read_b64_tr_b16 v[40:41], v116 offset:24576\n"
"v_mfma_f32_16x16x32_bf16 v[104:107], v[236:239], v[56:59], v[104:107]\n"
"s_mul_i32 s53, s48, 10240\n"
"ds_read_b64_tr_b16 v[42:43], v116 offset:26624\n"
"v_mfma_f32_16x16x32_bf16 v[92:95], v[232:235], v[60:63], v[92:95]\n"
"buffer_load_dwordx4 v[180:183], v127, s[32:35], s9 offen offset:1024 sc1\n"
"ds_read_b64_tr_b16 v[44:45], v124 offset:24576\n"
"v_mfma_f32_16x16x32_bf16 v[108:111], v[236:239], v[60:63], v[108:111]\n"
"ds_read_b64_tr_b16 v[46:47], v124 offset:26624\n"
"s_waitcnt lgkmcnt(4)\n"
"v_mfma_f32_16x16x32_bf16 v[80:83], v[240:243], v[32:35], v[80:83]\n"
"ds_read_b64_tr_b16 v[48:49], v117 offset:28672\n"
"v_mfma_f32_16x16x32_bf16 v[96:99], v[244:247], v[32:35], v[96:99]\n"
"v_lshl_add_u32 v26, v26, 3, s53\n"
"ds_read_b64_tr_b16 v[50:51], v117 offset:30720\n"
"v_mfma_f32_16x16x32_bf16 v[84:87], v[240:243], v[36:39], v[84:87]\n"
"buffer_load_dwordx4 v[184:187], v127, s[32:35], s9 offen offset:2048 sc1\n"
"ds_read_b64_tr_b16 v[52:53], v118 offset:28672\n"
"v_mfma_f32_16x16x32_bf16 v[100:103], v[244:247], v[36:39], v[100:103]\n"
"ds_read_b64_tr_b16 v[54:55], v118 offset:30720\n"
"s_waitcnt lgkmcnt(4)\n"
"v_mfma_f32_16x16x32_bf16 v[88:91], v[240:243], v[40:43], v[88:91]\n"
"v_lshl_add_u32 v27, v24, 3, v25\n"
"ds_read_b64_tr_b16 v[56:57], v116 offset:28672\n"
"v_mfma_f32_16x16x32_bf16 v[104:107], v[244:247], v[40:43], v[104:107]\n"
"v_lshrrev_b32 v28, 1, v25\n"
"ds_read_b64_tr_b16 v[58:59], v116 offset:30720\n"
"v_mfma_f32_16x16x32_bf16 v[92:95], v[240:243], v[44:47], v[92:95]\n"
"buffer_load_dwordx4 v[188:191], v127, s[32:35], s9 offen offset:3072 sc1\n"
"ds_read_b64_tr_b16 v[60:61], v124 offset:28672\n"
"v_mfma_f32_16x16x32_bf16 v[108:111], v[244:247], v[44:47], v[108:111]\n"
"ds_read_b64_tr_b16 v[62:63], v124 offset:30720\n"
"s_waitcnt lgkmcnt(4)\n"
"v_mfma_f32_16x16x32_bf16 v[80:83], v[248:251], v[48:51], v[80:83]\n"
"ds_read_b64_tr_b16 v[32:33], v117 offset:32768\n"
"v_mfma_f32_16x16x32_bf16 v[96:99], v[252:255], v[48:51], v[96:99]\n"
"v_xor_b32 v28, s30, v28\n"
"ds_read_b64_tr_b16 v[34:35], v117 offset:34816\n"
"v_mfma_f32_16x16x32_bf16 v[84:87], v[248:251], v[52:55], v[84:87]\n"
"ds_read_b64_tr_b16 v[36:37], v118 offset:32768\n"
"v_mfma_f32_16x16x32_bf16 v[100:103], v[252:255], v[52:55], v[100:103]\n"
"ds_read_b64_tr_b16 v[38:39], v118 offset:34816\n"
"s_waitcnt lgkmcnt(4)\n"
"v_mfma_f32_16x16x32_bf16 v[88:91], v[248:251], v[56:59], v[88:91]\n"
"v_lshl_add_u32 v27, v27, 7, v26\n"
"ds_read_b64_tr_b16 v[40:41], v116 offset:32768\n"
"v_mfma_f32_16x16x32_bf16 v[104:107], v[252:255], v[56:59], v[104:107]\n"
"v_lshl_add_u32 v27, v28, 5, v27\n"
"ds_read_b64_tr_b16 v[42:43], v116 offset:34816\n"
"v_mfma_f32_16x16x32_bf16 v[92:95], v[248:251], v[60:63], v[92:95]\n"
"ds_read_b64_tr_b16 v[44:45], v124 offset:32768\n"
"v_mfma_f32_16x16x32_bf16 v[108:111], v[252:255], v[60:63], v[108:111]\n"
"ds_read_b64_tr_b16 v[46:47], v124 offset:34816\n"
"s_waitcnt lgkmcnt(4)\n"
"v_mfma_f32_16x16x32_bf16 v[80:83], a[160:163], v[32:35], v[80:83]\n"
"ds_read_b64_tr_b16 v[48:49], v117 offset:36864\n"
"v_mfma_f32_16x16x32_bf16 v[96:99], a[164:167], v[32:35], v[96:99]\n"
"v_lshl_add_u32 v29, v24, 2, v25\n"
"ds_read_b64_tr_b16 v[50:51], v117 offset:38912\n"
"v_mfma_f32_16x16x32_bf16 v[84:87], a[160:163], v[36:39], v[84:87]\n"
"ds_read_b64_tr_b16 v[52:53], v118 offset:36864\n"
"v_mfma_f32_16x16x32_bf16 v[100:103], a[164:167], v[36:39], v[100:103]\n"
"ds_read_b64_tr_b16 v[54:55], v118 offset:38912\n"
"s_waitcnt lgkmcnt(4)\n"
"v_mfma_f32_16x16x32_bf16 v[88:91], a[160:163], v[40:43], v[88:91]\n"
"v_bfe_u32 v28, v29, 1, 2\n"
"ds_read_b64_tr_b16 v[56:57], v116 offset:36864\n"
"v_mfma_f32_16x16x32_bf16 v[104:107], a[164:167], v[40:43], v[104:107]\n"
"v_xor_b32 v28, s30, v28\n"
"ds_read_b64_tr_b16 v[58:59], v116 offset:38912\n"
"v_mfma_f32_16x16x32_bf16 v[92:95], a[160:163], v[44:47], v[92:95]\n"
"ds_read_b64_tr_b16 v[60:61], v124 offset:36864\n"
"v_mfma_f32_16x16x32_bf16 v[108:111], a[164:167], v[44:47], v[108:111]\n"
"ds_read_b64_tr_b16 v[62:63], v124 offset:38912\n"
"s_waitcnt lgkmcnt(4)\n"
"v_mfma_f32_16x16x32_bf16 v[80:83], a[168:171], v[48:51], v[80:83]\n"
"ds_read_b64_tr_b16 v[32:33], v117 offset:40960\n"
"v_mfma_f32_16x16x32_bf16 v[96:99], a[172:175], v[48:51], v[96:99]\n"
"v_lshl_add_u32 v29, v29, 7, v26\n"
".Lmg_hopM1:\n"
"s_cbranch_execz .Lmg_noones\n"
"ds_read_b64_tr_b16 v[34:35], v117 offset:43008\n"
"v_mfma_f32_16x16x32_bf16 v[84:87], a[168:171], v[52:55], v[84:87]\n"
"buffer_load_dwordx4 a[160:163], v127, s[32:35], s10 offen offset:0 sc1\n"
"ds_read_b64_tr_b16 v[36:37], v118 offset:40960\n"
"v_mfma_f32_16x16x32_bf16 v[100:103], a[172:175], v[52:55], v[100:103]\n"
"ds_read_b64_tr_b16 v[38:39], v118 offset:43008\n"
"s_waitcnt lgkmcnt(4)\n"
"v_mfma_f32_16x16x32_bf16 v[88:91], a[168:171], v[56:59], v[88:91]\n"
"v_lshl_add_u32 v29, v28, 5, v29\n"
"ds_read_b64_tr_b16 v[40:41], v116 offset:40960\n"
"v_mfma_f32_16x16x32_bf16 v[104:107], a[172:175], v[56:59], v[104:107]\n"
"v_and_b32 v30, 15, v126\n"
"ds_read_b64_tr_b16 v[42:43], v116 offset:43008\n"
"v_mfma_f32_16x16x32_bf16 v[92:95], a[168:171], v[60:63], v[92:95]\n"
"buffer_load_dwordx4 a[164:167], v127, s[32:35], s10 offen offset:1024 sc1\n"
"ds_read_b64_tr_b16 v[44:45], v124 offset:40960\n"
"v_mfma_f32_16x16x32_bf16 v[108:111], a[172:175], v[60:63], v[108:111]\n"
"ds_read_b64_tr_b16 v[46:47], v124 offset:43008\n"
"s_waitcnt lgkmcnt(4)\n"
"v_mfma_f32_16x16x32_bf16 v[80:83], a[176:179], v[32:35], v[80:83]\n"
"ds_read_b64_tr_b16 v[48:49], v117 offset:45056\n"
"v_mfma_f32_16x16x32_bf16 v[96:99], a[180:183], v[32:35], v[96:99]\n"
"s_lshl_b32 s53, s47, 2\n"
"ds_read_b64_tr_b16 v[50:51], v117 offset:47104\n"
"v_mfma_f32_16x16x32_bf16 v[84:87], a[176:179], v[36:39], v[84:87]\n"
"buffer_load_dwordx4 a[168:171], v127, s[32:35], s10 offen offset:2048 sc1\n"
"ds_read_b64_tr_b16 v[52:53], v118 offset:45056\n"
"v_mfma_f32_16x16x32_bf16 v[100:103], a[180:183], v[36:39], v[100:103]\n"
"ds_read_b64_tr_b16 v[54:55], v118 offset:47104\n"
"s_waitcnt lgkmcnt(4)\n"
"v_mfma_f32_16x16x32_bf16 v[88:91], a[176:179], v[40:43], v[88:91]\n"
"s_add_u32 s53, s53, s30\n"
"ds_read_b64_tr_b16 v[56:57], v116 offset:45056\n"
"v_mfma_f32_16x16x32_bf16 v[104:107], a[180:183], v[40:43], v[104:107]\n"
"s_lshl_b32 s22, s53, 10\n"
"ds_read_b64_tr_b16 v[58:59], v116 offset:47104\n"
"v_mfma_f32_16x16x32_bf16 v[92:95], a[176:179], v[44:47], v[92:95]\n"
"buffer_load_dwordx4 a[172:175], v127, s[32:35], s10 offen offset:3072 sc1\n"
"ds_read_b64_tr_b16 v[60:61], v124 offset:45056\n"
"v_mfma_f32_16x16x32_bf16 v[108:111], a[180:183], v[44:47], v[108:111]\n"
"ds_read_b64_tr_b16 v[62:63], v124 offset:47104\n"
"s_waitcnt lgkmcnt(4)\n"
"v_mfma_f32_16x16x32_bf16 v[80:83], a[184:187], v[48:51], v[80:83]\n"
"ds_read_b64_tr_b16 v[32:33], v117 offset:49152\n"
"v_mfma_f32_16x16x32_bf16 v[96:99], a[188:191], v[48:51], v[96:99]\n"
"s_add_u32 s22, s22, 393216\n"
"ds_read_b64_tr_b16 v[34:35], v117 offset:51200\n"
"v_mfma_f32_16x16x32_bf16 v[84:87], a[184:187], v[52:55], v[84:87]\n"
"buffer_load_dwordx4 a[176:179], v127, s[32:35], s11 offen offset:0 sc1\n"
"ds_read_b64_tr_b16 v[36:37], v118 offset:49152\n"
"v_mfma_f32_16x16x32_bf16 v[100:103], a[188:191], v[52:55], v[100:103]\n"
"ds_read_b64_tr_b16 v[38:39], v118 offset:51200\n"
"s_waitcnt lgkmcnt(4)\n"
"v_mfma_f32_16x16x32_bf16 v[88:91], a[184:187], v[56:59], v[88:91]\n"
"s_lshl_b32 s23, s48, 7\n"
"ds_read_b64_tr_b16 v[40:41], v116 offset:49152\n"
"v_mfma_f32_16x16x32_bf16 v[104:107], a[188:191], v[56:59], v[104:107]\n"
"s_add_u32 s23, s23, s22\n"
"ds_read_b64_tr_b16 v[42:43], v116 offset:51200\n"
"v_mfma_f32_16x16x32_bf16 v[92:95], a[184:187], v[60:63], v[92:95]\n"
"buffer_load_dwordx4 a[180:183], v127, s[32:35], s11 offen offset:1024 sc1\n"
"ds_read_b64_tr_b16 v[44:45], v124 offset:49152\n"
"v_mfma_f32_16x16x32_bf16 v[108:111], a[188:191], v[60:63], v[108:111]\n"
"ds_read_b64_tr_b16 v[46:47], v124 offset:51200\n"
"s_waitcnt lgkmcnt(4)\n"
"v_mfma_f32_16x16x32_bf16 v[80:83], a[192:195], v[32:35], v[80:83]\n"
"ds_read_b64_tr_b16 v[48:49], v117 offset:53248\n"
"v_mfma_f32_16x16x32_bf16 v[96:99], a[196:199], v[32:35], v[96:99]\n"
"s_mul_i32 s53, s53, 20480\n"
"ds_read_b64_tr_b16 v[50:51], v117 offset:55296\n"
"v_mfma_f32_16x16x32_bf16 v[84:87], a[192:195], v[36:39], v[84:87]\n"
"buffer_load_dwordx4 v[192:195], v20, s[32:35], s64 offen offset:0 sc1\n"
"ds_read_b64_tr_b16 v[52:53], v118 offset:53248\n"
"v_mfma_f32_16x16x32_bf16 v[100:103], a[196:199], v[36:39], v[100:103]\n"
"ds_read_b64_tr_b16 v[54:55], v118 offset:55296\n"
"s_waitcnt lgkmcnt(4)\n"
"v_mfma_f32_16x16x32_bf16 v[88:91], a[192:195], v[40:43], v[88:91]\n"
"s_mul_i32 s54, s48, 160\n"
"ds_read_b64_tr_b16 v[56:57], v116 offset:53248\n"
"v_mfma_f32_16x16x32_bf16 v[104:107], a[196:199], v[40:43], v[104:107]\n"
"s_add_u32 s53, s53, s54\n"
"ds_read_b64_tr_b16 v[58:59], v116 offset:55296\n"
"v_mfma_f32_16x16x32_bf16 v[92:95], a[192:195], v[44:47], v[92:95]\n"
"buffer_load_dwordx4 v[196:199], v21, s[32:35], s64 offen offset:0 sc1\n"
"ds_read_b64_tr_b16 v[60:61], v124 offset:53248\n"
"v_mfma_f32_16x16x32_bf16 v[108:111], a[196:199], v[44:47], v[108:111]\n"
"ds_read_b64_tr_b16 v[62:63], v124 offset:55296\n"
"s_waitcnt lgkmcnt(4)\n"
"v_mfma_f32_16x16x32_bf16 v[80:83], a[200:203], v[48:51], v[80:83]\n"
"ds_read_b64_tr_b16 v[32:33], v117 offset:57344\n"
"v_mfma_f32_16x16x32_bf16 v[96:99], a[204:207], v[48:51], v[96:99]\n"
"v_mul_u32_u24 v30, 1280, v30\n"
"ds_read_b64_tr_b16 v[34:35], v117 offset:59392\n"
"v_mfma_f32_16x16x32_bf16 v[84:87], a[200:203], v[52:55], v[84:87]\n"
"buffer_load_dwordx4 v[200:203], v22, s[32:35], s64 offen offset:0 sc1\n"
"ds_read_b64_tr_b16 v[36:37], v118 offset:57344\n"
"v_mfma_f32_16x16x32_bf16 v[100:103], a[204:207], v[52:55], v[100:103]\n"
"ds_read_b64_tr_b16 v[38:39], v118 offset:59392\n"
"s_waitcnt lgkmcnt(4)\n"
"v_mfma_f32_16x16x32_bf16 v[88:91], a[200:203], v[56:59], v[88:91]\n"
"v_add_u32 v30, s53, v30\n"
"ds_read_b64_tr_b16 v[40:41], v116 offset:57344\n"
"v_mfma_f32_16x16x32_bf16 v[104:107], a[204:207], v[56:59], v[104:107]\n"
"ds_read_b64_tr_b16 v[42:43], v116 offset:59392\n"
"v_mfma_f32_16x16x32_bf16 v[92:95], a[200:203], v[60:63], v[92:95]\n"
"buffer_load_dwordx4 v[204:207], v23, s[32:35], s64 offen offset:0 sc1\n"
"ds_read_b64_tr_b16 v[44:45], v124 offset:57344\n"
"v_mfma_f32_16x16x32_bf16 v[108:111], a[204:207], v[60:63], v[108:111]\n"
"ds_read_b64_tr_b16 v[46:47], v124 offset:59392\n"
"s_waitcnt lgkmcnt(4)\n"
"v_mfma_f32_16x16x32_bf16 v[80:83], a[208:211], v[32:35], v[80:83]\n"
"ds_read_b64_tr_b16 v[48:49], v117 offset:61440\n"
"v_mfma_f32_16x16x32_bf16 v[96:99], a[212:215], v[32:35], v[96:99]\n"
"ds_read_b64_tr_b16 v[50:51], v117 offset:63488\n"
"v_mfma_f32_16x16x32_bf16 v[84:87], a[208:211], v[36:39], v[84:87]\n"
"buffer_load_dwordx4 v[208:211], v20, s[32:35], s65 offen offset:0 sc1\n"
"ds_read_b64_tr_b16 v[52:53], v118 offset:61440\n"
"v_mfma_f32_16x16x32_bf16 v[100:103], a[212:215], v[36:39], v[100:103]\n"
"ds_read_b64_tr_b16 v[54:55], v118 offset:63488\n"
"s_waitcnt lgkmcnt(4)\n"
"v_mfma_f32_16x16x32_bf16 v[88:91], a[208:211], v[40:43], v[88:91]\n"
"ds_read_b64_tr_b16 v[56:57], v116 offset:61440\n"
"v_mfma_f32_16x16x32_bf16 v[104:107], a[212:215], v[40:43], v[104:107]\n"
"ds_read_b64_tr_b16 v[58:59], v116 offset:63488\n"
"v_mfma_f32_16x16x32_bf16 v[92:95], a[208:211], v[44:47], v[92:95]\n"
"buffer_load_dwordx4 v[212:215], v21, s[32:35], s65 offen offset:0 sc1\n"
"ds_read_b64_tr_b16 v[60:61], v124 offset:61440\n"
"v_mfma_f32_16x16x32_bf16 v[108:111], a[212:215], v[44:47], v[108:111]\n"
"ds_read_b64_tr_b16 v[62:63], v124 offset:63488\n"
"s_waitcnt lgkmcnt(4)\n"
"v_mfma_f32_16x16x32_bf16 v[80:83], a[216:219], v[48:51], v[80:83]\n"
"ds_read_b64_tr_b16 v[32:33], v112 offset:0\n"
"v_mfma_f32_16x16x32_bf16 v[96:99], a[220:223], v[48:51], v[96:99]\n"
"ds_read_b64_tr_b16 v[34:35], v112 offset:2048\n"
"v_mfma_f32_16x16x32_bf16 v[84:87], a[216:219], v[52:55], v[84:87]\n"
"buffer_load_dwordx4 v[216:219], v22, s[32:35], s65 offen offset:0 sc1\n"
"ds_read_b64_tr_b16 v[36:37], v113 offset:0\n"
"v_mfma_f32_16x16x32_bf16 v[100:103], a[220:223], v[52:55], v[100:103]\n"
"ds_read_b64_tr_b16 v[38:39], v113 offset:2048\n"
"s_waitcnt lgkmcnt(4)\n"
"v_mfma_f32_16x16x32_bf16 v[88:91], a[216:219], v[56:59], v[88:91]\n"
"ds_read_b64_tr_b16 v[40:41], v114 offset:0\n"
"v_mfma_f32_16x16x32_bf16 v[104:107], a[220:223], v[56:59], v[104:107]\n"
"ds_read_b64_tr_b16 v[42:43], v114 offset:2048\n"
"v_mfma_f32_16x16x32_bf16 v[92:95], a[216:219], v[60:63], v[92:95]\n"
"buffer_load_dwordx4 v[220:223], v23, s[32:35], s65 offen offset:0 sc1\n"
"ds_read_b64_tr_b16 v[44:45], v115 offset:0\n"
"v_mfma_f32_16x16x32_bf16 v[108:111], a[220:223], v[60:63], v[108:111]\n"
"ds_read_b64_tr_b16 v[46:47], v115 offset:2048\n"
"s_waitcnt lgkmcnt(4)\n"
"v_mfma_f32_16x16x32_bf16 v[80:83], a[224:227], v[32:35], v[80:83]\n"
"buffer_load_dword v14, v119, s[32:35], s64 offen sc1\n"
"ds_read_b64_tr_b16 v[48:49], v112 offset:4096\n"
"v_mfma_f32_16x16x32_bf16 v[96:99], a[228:231], v[32:35], v[96:99]\n"
"ds_read_b64_tr_b16 v[50:51], v112 offset:6144\n"
"v_mfma_f32_16x16x32_bf16 v[84:87], a[224:227], v[36:39], v[84:87]\n"
"buffer_load_dwordx4 v[224:227], v20, s[32:35], s66 offen offset:0 sc1\n"
"ds_read_b64_tr_b16 v[52:53], v113 offset:4096\n"
"v_mfma_f32_16x16x32_bf16 v[100:103], a[228:231], v[36:39], v[100:103]\n"
"ds_read_b64_tr_b16 v[54:55], v113 offset:6144\n"
"s_waitcnt lgkmcnt(4)\n"
"v_mfma_f32_16x16x32_bf16 v[88:91], a[224:227], v[40:43], v[88:91]\n"
"buffer_load_dword v15, v119, s[32:35], s65 offen sc1\n"
"ds_read_b64_tr_b16 v[56:57], v114 offset:4096\n"
"v_mfma_f32_16x16x32_bf16 v[104:107], a[228:231], v[40:43], v[104:107]\n"
"ds_read_b64_tr_b16 v[58:59], v114 offset:6144\n"
"v_mfma_f32_16x16x32_bf16 v[92:95], a[224:227], v[44:47], v[92:95]\n"
"buffer_load_dwordx4 v[228:231], v21, s[32:35], s66 offen offset:0 sc1\n"
"ds_read_b64_tr_b16 v[60:61], v115 offset:4096\n"
"v_mfma_f32_16x16x32_bf16 v[108:111], a[228:231], v[44:47], v[108:111]\n"
"ds_read_b64_tr_b16 v[62:63], v115 offset:6144\n"
"s_waitcnt lgkmcnt(4)\n"
"v_mfma_f32_16x16x32_bf16 v[80:83], a[232:235], v[48:51], v[80:83]\n"
"buffer_load_dword v16, v119, s[32:35], s66 offen sc1\n"
"ds_read_b64_tr_b16 v[32:33], v112 offset:8192\n"
"v_mfma_f32_16x16x32_bf16 v[96:99], a[236:239], v[48:51], v[96:99]\n"
"ds_read_b64_tr_b16 v[34:35], v112 offset:10240\n"
"v_mfma_f32_16x16x32_bf16 v[84:87], a[232:235], v[52:55], v[84:87]\n"
"buffer_load_dwordx4 v[232:235], v22, s[32:35], s66 offen offset:0 sc1\n"
"ds_read_b64_tr_b16 v[36:37], v113 offset:8192\n"
"v_mfma_f32_16x16x32_bf16 v[100:103], a[236:239], v[52:55], v[100:103]\n"
"ds_read_b64_tr_b16 v[38:39], v113 offset:10240\n"
"s_waitcnt lgkmcnt(4)\n"
"v_mfma_f32_16x16x32_bf16 v[88:91], a[232:235], v[56:59], v[88:91]\n"
"buffer_load_dword v17, v119, s[32:35], s67 offen sc1\n"
"ds_read_b64_tr_b16 v[40:41], v114 offset:8192\n"
"v_mfma_f32_16x16x32_bf16 v[104:107], a[236:239], v[56:59], v[104:107]\n"
"ds_read_b64_tr_b16 v[42:43], v114 offset:10240\n"
"v_mfma_f32_16x16x32_bf16 v[92:95], a[232:235], v[60:63], v[92:95]\n"
"buffer_load_dwordx4 v[236:239], v23, s[32:35], s66 offen offset:0 sc1\n"
"ds_read_b64_tr_b16 v[44:45], v115 offset:8192\n"
"v_mfma_f32_16x16x32_bf16 v[108:111], a[236:239], v[60:63], v[108:111]\n"
"ds_read_b64_tr_b16 v[46:47], v115 offset:10240\n"
"s_waitcnt lgkmcnt(4)\n"
"v_mfma_f32_16x16x32_bf16 v[80:83], a[240:243], v[32:35], v[80:83]\n"
"buffer_load_dword v18, v119, s[32:35], s68 offen sc1\n"
"ds_read_b64_tr_b16 v[48:49], v112 offset:12288\n"
"v_mfma_f32_16x16x32_bf16 v[96:99], a[244:247], v[32:35], v[96:99]\n"
"ds_read_b64_tr_b16 v[50:51], v112 offset:14336\n"
"v_mfma_f32_16x16x32_bf16 v[84:87], a[240:243], v[36:39], v[84:87]\n"
"buffer_load_dwordx4 v[240:243], v20, s[32:35], s67 offen offset:0 sc1\n"
"ds_read_b64_tr_b16 v[52:53], v113 offset:12288\n"
"v_mfma_f32_16x16x32_bf16 v[100:103], a[244:247], v[36:39], v[100:103]\n"
"ds_read_b64_tr_b16 v[54:55], v113 offset:14336\n"
"s_waitcnt lgkmcnt(4)\n"
"v_mfma_f32_16x16x32_bf16 v[88:91], a[240:243], v[40:43], v[88:91]\n"
"buffer_load_dword v19, v119, s[32:35], s69 offen sc1\n"
"ds_read_b64_tr_b16 v[56:57], v114 offset:12288\n"
"v_mfma_f32_16x16x32_bf16 v[104:107], a[244:247], v[40:43], v[104:107]\n"
"ds_read_b64_tr_b16 v[58:59], v114 offset:14336\n"
"v_mfma_f32_16x16x32_bf16 v[92:95], a[240:243], v[44:47], v[92:95]\n"
"buffer_load_dwordx4 v[244:247], v21, s[32:35], s67 offen offset:0 sc1\n"
"ds_read_b64_tr_b16 v[60:61], v115 offset:12288\n"
"v_mfma_f32_16x16x32_bf16 v[108:111], a[244:247], v[44:47], v[108:111]\n"
"ds_read_b64_tr_b16 v[62:63], v115 offset:14336\n"
"s_waitcnt lgkmcnt(4)\n"
"v_mfma_f32_16x16x32_bf16 v[80:83], a[248:251], v[48:51], v[80:83]\n"
"v_mfma_f32_16x16x32_bf16 v[96:99], a[252:255], v[48:51], v[96:99]\n"
"v_mfma_f32_16x16x32_bf16 v[84:87], a[248:251], v[52:55], v[84:87]\n"
"buffer_load_dwordx4 v[248:251], v22, s[32:35], s67 offen offset:0 sc1\n"
"v_mfma_f32_16x16x32_bf16 v[100:103], a[252:255], v[52:55], v[100:103]\n"
"s_waitcnt lgkmcnt(0)\n"
"v_mfma_f32_16x16x32_bf16 v[88:91], a[248:251], v[56:59], v[88:91]\n"
"v_mfma_f32_16x16x32_bf16 v[104:107], a[252:255], v[56:59], v[104:107]\n"
"v_mfma_f32_16x16x32_bf16 v[92:95], a[248:251], v[60:63], v[92:95]\n"
"buffer_load_dwordx4 v[252:255], v23, s[32:35], s67 offen offset:0 sc1\n"
"v_mfma_f32_16x16x32_bf16 v[108:111], a[252:255], v[60:63], v[108:111]\n"
".Lmg_g2:\n"
"v_mov_b32 v112, v20\n"
"v_mov_b32 v113, v21\n"
"v_mov_b32 v114, v22\n"
"v_mov_b32 v115, v23\n"
"v_mov_b32 v118, v27\n"
"v_mov_b32 v116, v29\n"
"v_lshl_add_u32 v124, v24, 4, v30\n"
"v_lshl_add_u32 v126, v24, 3, v30\n"
"v_pk_fma_f32 v[80:81], v[72:73], v[120:121], v[80:81] op_sel:[0,0,0] op_sel_hi:[1,0,1]\n"
"v_pk_fma_f32 v[82:83], v[74:75], v[120:121], v[82:83] op_sel:[0,0,0] op_sel_hi:[1,0,1]\n"
"v_pk_fma_f32 v[96:97], v[76:77], v[120:121], v[96:97] op_sel:[0,0,0] op_sel_hi:[1,0,1]\n"
"v_pk_fma_f32 v[98:99], v[78:79], v[120:121], v[98:99] op_sel:[0,0,0] op_sel_hi:[1,0,1]\n"
"v_cvt_pk_bf16_f32 v80, v80, v81\n"
"v_cvt_pk_bf16_f32 v81, v82, v83\n"
"v_cvt_pk_bf16_f32 v82, v96, v97\n"
"v_cvt_pk_bf16_f32 v83, v98, v99\n"
"v_pk_max_i16 v80, v80, 0\n"
"v_pk_max_i16 v81, v81, 0\n"
"v_pk_max_i16 v82, v82, 0\n"
"v_pk_max_i16 v83, v83, 0\n"
"buffer_store_dwordx4 v[80:83], v127, s[36:39], s60 offen offset:0 sc1\n"
"v_add3_u32 v96, v80, v81, s61\n"
"v_add3_u32 v96, v82, v83, v96\n"
"v_pk_fma_f32 v[84:85], v[72:73], v[120:121], v[84:85] op_sel:[0,1,0] op_sel_hi:[1,1,1]\n"
"v_pk_fma_f32 v[86:87], v[74:75], v[120:121], v[86:87] op_sel:[0,1,0] op_sel_hi:[1,1,1]\n"
"v_pk_fma_f32 v[100:101], v[76:77], v[120:121], v[100:101] op_sel:[0,1,0] op_sel_hi:[1,1,1]\n"
"v_pk_fma_f32 v[102:103], v[78:79], v[120:121], v[102:103] op_sel:[0,1,0] op_sel_hi:[1,1,1]\n"
"v_cvt_pk_bf16_f32 v84, v84, v85\n"
"v_cvt_pk_bf16_f32 v85, v86, v87\n"
"v_cvt_pk_bf16_f32 v86, v100, v101\n"
"v_cvt_pk_bf16_f32 v87, v102, v103\n"
"v_pk_max_i16 v84, v84, 0\n"
"v_pk_max_i16 v85, v85, 0\n"
"v_pk_max_i16 v86, v86, 0\n"
"v_pk_max_i16 v87, v87, 0\n"
"buffer_store_dwordx4 v[84:87], v127, s[36:39], s60 offen offset:1024 sc1\n"
"v_add3_u32 v96, v84, v85, v96\n"
"v_add3_u32 v96, v86, v87, v96\n"
"v_pk_fma_f32 v[88:89], v[72:73], v[122:123], v[88:89] op_sel:[0,0,0] op_sel_hi:[1,0,1]\n"
"v_pk_fma_f32 v[90:91], v[74:75], v[122:123], v[90:91] op_sel:[0,0,0] op_sel_hi:[1,0,1]\n"
"v_pk_fma_f32 v[104:105], v[76:77], v[122:123], v[104:105] op_sel:[0,0,0] op_sel_hi:[1,0,1]\n"
"v_pk_fma_f32 v[106:107], v[78:79], v[122:123], v[106:107] op_sel:[0,0,0] op_sel_hi:[1,0,1]\n"
"v_cvt_pk_bf16_f32 v88, v88, v89\n"
"v_cvt_pk_bf16_f32 v89, v90, v91\n"
"v_cvt_pk_bf16_f32 v90, v104, v105\n"
"v_cvt_pk_bf16_f32 v91, v106, v107\n"
"v_pk_max_i16 v88, v88, 0\n"
"v_pk_max_i16 v89, v89, 0\n"
"v_pk_max_i16 v90, v90, 0\n"
"v_pk_max_i16 v91, v91, 0\n"
"buffer_store_dwordx4 v[88:91], v127, s[36:39], s60 offen offset:2048 sc1\n"
"v_add3_u32 v96, v88, v89, v96\n"
"v_add3_u32 v96, v90, v91, v96\n"
"v_pk_fma_f32 v[92:93], v[72:73], v[122:123], v[92:93] op_sel:[0,1,0] op_sel_hi:[1,1,1]\n"
"v_pk_fma_f32 v[94:95], v[74:75], v[122:123], v[94:95] op_sel:[0,1,0] op_sel_hi:[1,1,1]\n"
"v_pk_fma_f32 v[108:109], v[76:77], v[122:123], v[108:109] op_sel:[0,1,0] op_sel_hi:[1,1,1]\n"
"v_pk_fma_f32 v[110:111], v[78:79], v[122:123], v[110:111] op_sel:[0,1,0] op_sel_hi:[1,1,1]\n"
"v_cvt_pk_bf16_f32 v92, v92, v93\n"
"v_cvt_pk_bf16_f32 v93, v94, v95\n"
"v_cvt_pk_bf16_f32 v94, v108, v109\n"
"v_cvt_pk_bf16_f32 v95, v110, v111\n"
"v_pk_max_i16 v92, v92, 0\n"
"v_pk_max_i16 v93, v93, 0\n"
"v_pk_max_i16 v94, v94, 0\n"
"v_pk_max_i16 v95, v95, 0\n"
"buffer_store_dwordx4 v[92:95], v127, s[36:39], s60 offen offset:3072 sc1\n"
"v_add3_u32 v96, v92, v93, v96\n"
"v_add3_u32 v96, v94, v95, v96\n"
"buffer_store_dword v96, v119, s[36:39], s60 offen sc1\n"
"s_waitcnt vmcnt(18)\n"
"v_mov_b32 v100, v14\n"
"s_waitcnt vmcnt(16)\n"
"v_mov_b32 v101, v15\n"
"s_waitcnt vmcnt(14)\n"
"v_mov_b32 v108, v16\n"
"s_waitcnt vmcnt(12)\n"
"v_mov_b32 v109, v17\n"
"s_waitcnt vmcnt(10)\n"
"v_mov_b32 v110, v18\n"
"s_waitcnt vmcnt(8)\n"
"v_mov_b32 v111, v19\n"
"ds_read_b64_tr_b16 v[98:99], v116 offset:8192\n"
"v_add3_u32 v116, v192, v193, s61\n"
"v_add3_u32 v116, v194, v195, v116\n"
"v_add3_u32 v116, v196, v197, v116\n"
"v_add3_u32 v116, v198, v199, v116\n"
"v_add3_u32 v116, v200, v201, v116\n"
"v_add3_u32 v116, v202, v203, v116\n"
"v_add3_u32 v116, v204, v205, v116\n"
"v_add3_u32 v116, v206, v207, v116\n"
"v_cmp_ne_u32 vcc, v116, v100\n"
"s_cbranch_vccnz .Lmg_hs11\n"
".Lmg_hr11:\n"
"v_mfma_f32_16x16x32_bf16 v[0:3], v[128:131], v[192:195], 0\n"
"s_movk_i32 s24, 0x80\n"
"s_lshl_b32 s53, s30, 6\n"
"v_mfma_f32_16x16x32_bf16 v[4:7], v[132:135], v[192:195], 0\n"
"v_add_u32 v125, s53, v125\n"
"v_mfma_f32_16x16x32_bf16 v[8:11], v[136:139], v[192:195], 0\n"
"ds_read_b32 v120, v125 offset:512\n"
"v_mfma_f32_16x16x32_bf16 v[12:15], v[140:143], v[192:195], 0\n"
"buffer_load_dwordx4 v[40:43], v112, s[32:35], s68 offen offset:0 sc1\n"
"v_mfma_f32_16x16x32_bf16 v[16:19], v[144:147], v[192:195], 0\n"
"v_xor_b32 v107, 64, v118\n"
"v_mfma_f32_16x16x32_bf16 v[20:23], v[128:131], v[196:199], 0\n"
"ds_read_b64_tr_b16 v[82:83], v118 offset:0\n"
"v_mfma_f32_16x16x32_bf16 v[24:27], v[132:135], v[196:199], 0\n"
"ds_read_b64_tr_b16 v[86:87], v107 offset:512\n"
"v_mfma_f32_16x16x32_bf16 v[28:31], v[136:139], v[196:199], 0\n"
"buffer_load_dwordx4 v[44:47], v113, s[32:35], s68 offen offset:0 sc1\n"
"v_mfma_f32_16x16x32_bf16 v[32:35], v[140:143], v[196:199], 0\n"
"ds_read_b64_tr_b16 v[90:91], v118 offset:4096\n"
"v_mfma_f32_16x16x32_bf16 v[36:39], v[144:147], v[196:199], 0\n"
"ds_read_b64_tr_b16 v[94:95], v107 offset:4608\n"
"v_mfma_f32_16x16x32_bf16 a[216:219], v[128:131], v[200:203], 0\n"
"v_lshrrev_b32 v106, 1, v127\n"
"v_bfrev_b32 v107, 2\n"
"v_mfma_f32_16x16x32_bf16 a[220:223], v[132:135], v[200:203], 0\n"
"buffer_load_dwordx4 v[48:51], v114, s[32:35], s68 offen offset:0 sc1\n"
"v_mfma_f32_16x16x32_bf16 a[224:227], v[136:139], v[200:203], 0\n"
"v_add3_u32 v116, v208, v209, s61\n"
"v_mfma_f32_16x16x32_bf16 a[228:231], v[140:143], v[200:203], 0\n"
"v_add3_u32 v116, v210, v211, v116\n"
"v_add3_u32 v116, v212, v213, v116\n"
"v_mfma_f32_16x16x32_bf16 a[232:235], v[144:147], v[200:203], 0\n"
"v_add3_u32 v116, v214, v215, v116\n"
"v_add3_u32 v116, v216, v217, v116\n"
"v_mfma_f32_16x16x32_bf16 a[236:239], v[128:131], v[204:207], 0\n"
"buffer_load_dwordx4 v[52:55], v115, s[32:35], s68 offen offset:0 sc1\n"
"v_mfma_f32_16x16x32_bf16 a[240:243], v[132:135], v[204:207], 0\n"
"v_add3_u32 v116, v218, v219, v116\n"
"v_add3_u32 v116, v220, v221, v116\n"
"v_mfma_f32_16x16x32_bf16 a[244:247], v[136:139], v[204:207], 0\n"
"v_add3_u32 v116, v222, v223, v116\n"
"v_cmp_ne_u32 vcc, v116, v101\n"
"v_mfma_f32_16x16x32_bf16 a[248:251], v[140:143], v[204:207], 0\n"
"s_cbranch_vccnz .Lmg_hs12\n"
".Lmg_hr12:\n"
"v_mfma_f32_16x16x32_bf16 a[252:255], v[144:147], v[204:207], 0\n"
"buffer_load_dwordx4 v[56:59], v112, s[32:35], s69 offen offset:0 sc1\n"
"v_mfma_f32_16x16x32_bf16 v[0:3], v[148:151], v[208:211], v[0:3]\n"
"v_cmp_gt_u32 s[0:1], s24, v106\n"
"v_mov_b32 v103, s31\n"
"v_mfma_f32_16x16x32_bf16 v[4:7], v[152:155], v[208:211], v[4:7]\n"
"v_cndmask_b32 v107, v107, v106, s[0:1]\n"
"v_and_b32 v104, 0x78, v106\n"
"v_mfma_f32_16x16x32_bf16 v[8:11], v[156:159], v[208:211], v[8:11]\n"
"v_and_b32 v106, 0x180, v106\n"
"v_lshl_add_u32 v106, v106, 1, v104\n"
"v_mfma_f32_16x16x32_bf16 v[12:15], v[160:163], v[208:211], v[12:15]\n"
"buffer_load_dwordx4 v[60:63], v113, s[32:35], s69 offen offset:0 sc1\n"
"v_mfma_f32_16x16x32_bf16 v[16:19], v[164:167], v[208:211], v[16:19]\n"
"s_waitcnt lgkmcnt(5)\n"
"v_lshlrev_b32 v96, 16, v98\n"
"v_and_b32 v97, 0xffff0000, v98\n"
"v_mfma_f32_16x16x32_bf16 v[20:23], v[148:151], v[212:215], v[20:23]\n"
"v_lshlrev_b32 v98, 16, v99\n"
"v_and_b32 v99, 0xffff0000, v99\n"
"v_mfma_f32_16x16x32_bf16 v[24:27], v[152:155], v[212:215], v[24:27]\n"
"s_waitcnt lgkmcnt(3)\n"
"v_lshlrev_b32 v80, 16, v82\n"
"v_and_b32 v81, 0xffff0000, v82\n"
"v_mfma_f32_16x16x32_bf16 v[28:31], v[156:159], v[212:215], v[28:31]\n"
"buffer_load_dwordx4 v[64:67], v114, s[32:35], s69 offen offset:0 sc1\n"
"v_mfma_f32_16x16x32_bf16 v[32:35], v[160:163], v[212:215], v[32:35]\n"
"v_lshlrev_b32 v82, 16, v83\n"
"v_and_b32 v83, 0xffff0000, v83\n"
"v_mfma_f32_16x16x32_bf16 v[36:39], v[164:167], v[212:215], v[36:39]\n"
"s_waitcnt lgkmcnt(2)\n"
"v_lshlrev_b32 v84, 16, v86\n"
"v_and_b32 v85, 0xffff0000, v86\n"
"v_mfma_f32_16x16x32_bf16 a[216:219], v[148:151], v[216:219], a[216:219]\n"
"v_lshlrev_b32 v86, 16, v87\n"
"v_and_b32 v87, 0xffff0000, v87\n"
"v_mfma_f32_16x16x32_bf16 a[220:223], v[152:155], v[216:219], a[220:223]\n"
"buffer_load_dwordx4 v[68:71], v115, s[32:35], s69 offen offset:0 sc1\n"
"v_mfma_f32_16x16x32_bf16 a[224:227], v[156:159], v[216:219], a[224:227]\n"
"v_add3_u32 v116, v224, v225, s61\n"
"v_mfma_f32_16x16x32_bf16 a[228:231], v[160:163], v[216:219], a[228:231]\n"
"v_add3_u32 v116, v226, v227, v116\n"
"v_add3_u32 v116, v228, v229, v116\n"
"v_mfma_f32_16x16x32_bf16 a[232:235], v[164:167], v[216:219], a[232:235]\n"
"v_add3_u32 v116, v230, v231, v116\n"
"v_add3_u32 v116, v232, v233, v116\n"
"v_mfma_f32_16x16x32_bf16 a[236:239], v[148:151], v[220:223], a[236:239]\n"
"buffer_load_dwordx4 a[184:187], v127, s[32:35], s11 offen offset:2048 sc1\n"
"v_mfma_f32_16x16x32_bf16 a[240:243], v[152:155], v[220:223], a[240:243]\n"
"v_add3_u32 v116, v234, v235, v116\n"
"v_add3_u32 v116, v236, v237, v116\n"
"v_mfma_f32_16x16x32_bf16 a[244:247], v[156:159], v[220:223], a[244:247]\n"
"v_add3_u32 v116, v238, v239, v116\n"
"v_cmp_ne_u32 vcc, v116, v108\n"
"v_mfma_f32_16x16x32_bf16 a[248:251], v[160:163], v[220:223], a[248:251]\n"
"s_cbranch_vccnz .Lmg_hs13\n"
".Lmg_hr13:\n"
"v_mfma_f32_16x16x32_bf16 a[252:255], v[164:167], v[220:223], a[252:255]\n"
"buffer_load_dwordx4 a[188:191], v127, s[32:35], s11 offen offset:3072 sc1\n"
"v_mfma_f32_16x16x32_bf16 v[0:3], a[0:3], v[224:227], v[0:3]\n"
"s_waitcnt lgkmcnt(1)\n"
"v_lshlrev_b32 v88, 16, v90\n"
"v_and_b32 v89, 0xffff0000, v90\n"
"v_mfma_f32_16x16x32_bf16 v[4:7], a[4:7], v[224:227], v[4:7]\n"
"v_lshlrev_b32 v90, 16, v91\n"
"v_and_b32 v91, 0xffff0000, v91\n"
"v_mfma_f32_16x16x32_bf16 v[8:11], a[8:11], v[224:227], v[8:11]\n"
"s_waitcnt lgkmcnt(0)\n"
"v_lshlrev_b32 v92, 16, v94\n"
"v_and_b32 v93, 0xffff0000, v94\n"
"v_mfma_f32_16x16x32_bf16 v[12:15], a[12:15], v[224:227], v[12:15]\n"
"buffer_load_dwordx4 a[192:195], v127, s[32:35], s12 offen offset:0 sc1\n"
"v_mfma_f32_16x16x32_bf16 v[16:19], a[16:19], v[224:227], v[16:19]\n"
"v_lshlrev_b32 v94, 16, v95\n"
"v_and_b32 v95, 0xffff0000, v95\n"
"v_mfma_f32_16x16x32_bf16 v[20:23], a[0:3], v[228:231], v[20:23]\n"
"v_mul_f32 v102, v80, v80\n"
"v_mul_f32 v104, v81, v81\n"
"v_mfma_f32_16x16x32_bf16 v[24:27], a[4:7], v[228:231], v[24:27]\n"
"v_fmac_f32 v102, v82, v82\n"
"v_fmac_f32 v104, v83, v83\n"
"v_mfma_f32_16x16x32_bf16 v[28:31], a[8:11], v[228:231], v[28:31]\n"
"buffer_load_dwordx4 a[196:199], v127, s[32:35], s12 offen offset:1024 sc1\n"
"v_mfma_f32_16x16x32_bf16 v[32:35], a[12:15], v[228:231], v[32:35]\n"
"v_fmac_f32 v102, v84, v84\n"
"v_fmac_f32 v104, v85, v85\n"
"v_mfma_f32_16x16x32_bf16 v[36:39], a[16:19], v[228:231], v[36:39]\n"
"v_fmac_f32 v102, v86, v86\n"
"v_fmac_f32 v104, v87, v87\n"
"v_mfma_f32_16x16x32_bf16 a[216:219], a[0:3], v[232:235], a[216:219]\n"
"v_fmac_f32 v102, v88, v88\n"
"v_fmac_f32 v104, v89, v89\n"
"v_mfma_f32_16x16x32_bf16 a[220:223], a[4:7], v[232:235], a[220:223]\n"
"buffer_load_dwordx4 a[200:203], v127, s[32:35], s12 offen offset:2048 sc1\n"
"v_mfma_f32_16x16x32_bf16 a[224:227], a[8:11], v[232:235], a[224:227]\n"
"s_waitcnt vmcnt(18)\n"
"v_add3_u32 v116, v240, v241, s61\n"
"v_mfma_f32_16x16x32_bf16 a[228:231], a[12:15], v[232:235], a[228:231]\n"
"v_add3_u32 v116, v242, v243, v116\n"
"v_add3_u32 v116, v244, v245, v116\n"
"v_mfma_f32_16x16x32_bf16 a[232:235], a[16:19], v[232:235], a[232:235]\n"
"v_add3_u32 v116, v246, v247, v116\n"
"v_add3_u32 v116, v248, v249, v116\n"
"v_mfma_f32_16x16x32_bf16 a[236:239], a[0:3], v[236:239], a[236:239]\n"
"buffer_load_dwordx4 a[204:207], v127, s[32:35], s12 offen offset:3072 sc1\n"
"v_mfma_f32_16x16x32_bf16 a[240:243], a[4:7], v[236:239], a[240:243]\n"
"v_add3_u32 v116, v250, v251, v116\n"
"v_add3_u32 v116, v252, v253, v116\n"
"v_mfma_f32_16x16x32_bf16 a[244:247], a[8:11], v[236:239], a[244:247]\n"
"v_add3_u32 v116, v254, v255, v116\n"
"v_cmp_ne_u32 vcc, v116, v109\n"
"v_mfma_f32_16x16x32_bf16 a[248:251], a[12:15], v[236:239], a[248:251]\n"
"s_cbranch_vccnz .Lmg_hs14\n"
".Lmg_hr14:\n"
"v_mfma_f32_16x16x32_bf16 a[252:255], a[16:19], v[236:239], a[252:255]\n"
"buffer_load_dwordx4 a[208:211], v127, s[32:35], s13 offen offset:0 sc1\n"
"v_mfma_f32_16x16x32_bf16 v[0:3], a[20:23], v[240:243], v[0:3]\n"
"v_fmac_f32 v102, v90, v90\n"
"v_fmac_f32 v104, v91, v91\n"
"v_mfma_f32_16x16x32_bf16 v[4:7], a[24:27], v[240:243], v[4:7]\n"
"v_fmac_f32 v102, v92, v92\n"
"v_fmac_f32 v104, v93, v93\n"
"v_mfma_f32_16x16x32_bf16 v[8:11], a[28:31], v[240:243], v[8:11]\n"
"v_fmac_f32 v102, v94, v94\n"
"v_fmac_f32 v104, v95, v95\n"
"v_mfma_f32_16x16x32_bf16 v[12:15], a[32:35], v[240:243], v[12:15]\n"
"buffer_load_dword v100, v119, s[40:43], s70 offen sc1\n"
"buffer_load_dwordx4 v[192:195], v112, s[40:43], s70 offen offset:0 sc1\n"
"v_mfma_f32_16x16x32_bf16 v[16:19], a[36:39], v[240:243], v[16:19]\n"
"v_fmac_f32 v102, v96, v96\n"
"v_fmac_f32 v104, v97, v97\n"
"v_mfma_f32_16x16x32_bf16 v[20:23], a[20:23], v[244:247], v[20:23]\n"
"v_fmac_f32 v102, v98, v98\n"
"v_fmac_f32 v104, v99, v99\n"
"v_mfma_f32_16x16x32_bf16 v[24:27], a[24:27], v[244:247], v[24:27]\n"
"v_add_f32 v102, v102, v104\n"
"s_xor_b32 s53, s30, 1\n"
"v_mfma_f32_16x16x32_bf16 v[28:31], a[28:31], v[244:247], v[28:31]\n"
"buffer_load_dwordx4 v[196:199], v113, s[40:43], s70 offen offset:0 sc1\n"
"v_mfma_f32_16x16x32_bf16 v[32:35], a[32:35], v[244:247], v[32:35]\n"
"v_mov_b32 v104, v102\n"
"s_mul_i32 s53, s53, 15360\n"
"v_mfma_f32_16x16x32_bf16 v[36:39], a[36:39], v[244:247], v[36:39]\n"
"v_permlane16_swap_b32 v102, v104\n"
"v_add_f32 v102, v102, v104\n"
"v_mfma_f32_16x16x32_bf16 a[216:219], a[20:23], v[248:251], a[216:219]\n"
"v_mov_b32 v104, v102\n"
"s_add_u32 s53, s53, 83968\n"
"v_mfma_f32_16x16x32_bf16 a[220:223], a[24:27], v[248:251], a[220:223]\n"
"buffer_load_dwordx4 v[200:203], v114, s[40:43], s70 offen offset:0 sc1\n"
"v_mfma_f32_16x16x32_bf16 a[224:227], a[28:31], v[248:251], a[224:227]\n"
"s_waitcnt vmcnt(15)\n"
"v_add3_u32 v116, v40, v41, s61\n"
"v_mfma_f32_16x16x32_bf16 a[228:231], a[32:35], v[248:251], a[228:231]\n"
"v_add3_u32 v116, v42, v43, v116\n"
"v_add3_u32 v116, v44, v45, v116\n"
"v_mfma_f32_16x16x32_bf16 a[232:235], a[36:39], v[248:251], a[232:235]\n"
"v_add3_u32 v116, v46, v47, v116\n"
"v_add3_u32 v116, v48, v49, v116\n"
"v_mfma_f32_16x16x32_bf16 a[236:239], a[20:23], v[252:255], a[236:239]\n"
"buffer_load_dwordx4 v[204:207], v115, s[40:43], s70 offen offset:0 sc1\n"
"v_mfma_f32_16x16x32_bf16 a[240:243], a[24:27], v[252:255], a[240:243]\n"
"v_add3_u32 v116, v50, v51, v116\n"
"v_add3_u32 v116, v52, v53, v116\n"
"v_mfma_f32_16x16x32_bf16 a[244:247], a[28:31], v[252:255], a[244:247]\n"
"v_add3_u32 v116, v54, v55, v116\n"
"v_cmp_ne_u32 vcc, v116, v110\n"
"v_mfma_f32_16x16x32_bf16 a[248:251], a[32:35], v[252:255], a[248:251]\n"
"s_cbranch_vccnz .Lmg_hs15\n"
".Lmg_hr15:\n"
"v_mfma_f32_16x16x32_bf16 a[252:255], a[36:39], v[252:255], a[252:255]\n"
"buffer_load_dword v101, v119, s[32:35], s71 offen sc1\n"
"buffer_load_dwordx4 v[208:211], v112, s[32:35], s71 offen offset:0 sc1\n"
"v_mfma_f32_16x16x32_bf16 v[0:3], a[40:43], v[40:43], v[0:3]\n"
"v_permlane32_swap_b32 v102, v104\n"
"v_add_f32 v102, v102, v104\n"
"v_mfma_f32_16x16x32_bf16 v[4:7], a[44:47], v[40:43], v[4:7]\n"
"buffer_store_dwordx2 v[102:103], v107, s[32:35], s23 offen sc1\n"
"v_mfma_f32_16x16x32_bf16 v[8:11], a[48:51], v[40:43], v[8:11]\n"
"v_add_u32 v121, s53, v127\n"
"s_xor_b32 s53, s30, 2\n"
"v_mfma_f32_16x16x32_bf16 v[12:15], a[52:55], v[40:43], v[12:15]\n"
"buffer_load_dwordx4 v[212:215], v113, s[32:35], s71 offen offset:0 sc1\n"
"v_mfma_f32_16x16x32_bf16 v[16:19], a[56:59], v[40:43], v[16:19]\n"
"s_mul_i32 s53, s53, 15360\n"
"s_add_u32 s53, s53, 89088\n"
"v_mfma_f32_16x16x32_bf16 v[20:23], a[40:43], v[44:47], v[20:23]\n"
"v_add_u32 v122, s53, v127\n"
"s_xor_b32 s53, s30, 3\n"
"v_mfma_f32_16x16x32_bf16 v[24:27], a[44:47], v[44:47], v[24:27]\n"
"s_mul_i32 s53, s53, 15360\n"
"s_add_u32 s53, s53, 94208\n"
"v_mfma_f32_16x16x32_bf16 v[28:31], a[48:51], v[44:47], v[28:31]\n"
"buffer_load_dwordx4 v[216:219], v114, s[32:35], s71 offen offset:0 sc1\n"
"v_mfma_f32_16x16x32_bf16 v[32:35], a[52:55], v[44:47], v[32:35]\n"
"v_add_u32 v123, s53, v127\n"
"s_mul_i32 s53, s30, 15360\n"
"v_mfma_f32_16x16x32_bf16 v[36:39], a[56:59], v[44:47], v[36:39]\n"
"s_add_u32 s53, s53, 83968\n"
"v_add_u32 v125, s53, v127\n"
"v_mfma_f32_16x16x32_bf16 a[216:219], a[40:43], v[48:51], a[216:219]\n"
"v_mfma_f32_16x16x32_bf16 a[220:223], a[44:47], v[48:51], a[220:223]\n"
"buffer_load_dwordx4 v[220:223], v115, s[32:35], s71 offen offset:0 sc1\n"
"v_mfma_f32_16x16x32_bf16 a[224:227], a[48:51], v[48:51], a[224:227]\n"
"s_waitcnt vmcnt(18)\n"
"v_add3_u32 v116, v56, v57, s61\n"
"v_mfma_f32_16x16x32_bf16 a[228:231], a[52:55], v[48:51], a[228:231]\n"
"v_add3_u32 v116, v58, v59, v116\n"
"v_add3_u32 v116, v60, v61, v116\n"
"v_mfma_f32_16x16x32_bf16 a[232:235], a[56:59], v[48:51], a[232:235]\n"
".Lmg_hopM2:\n"
"s_cbranch_execz .Lmg_noones\n"
"v_add3_u32 v116, v62, v63, v116\n"
"v_add3_u32 v116, v64, v65, v116\n"
"v_mfma_f32_16x16x32_bf16 a[236:239], a[40:43], v[52:55], a[236:239]\n"
"buffer_load_dword v108, v119, s[32:35], s72 offen sc1\n"
"buffer_load_dwordx4 v[224:227], v112, s[32:35], s72 offen offset:0 sc1\n"
"v_mfma_f32_16x16x32_bf16 a[240:243], a[44:47], v[52:55], a[240:243]\n"
"v_add3_u32 v116, v66, v67, v116\n"
"v_add3_u32 v116, v68, v69, v116\n"
"v_mfma_f32_16x16x32_bf16 a[244:247], a[48:51], v[52:55], a[244:247]\n"
"v_add3_u32 v116, v70, v71, v116\n"
"v_cmp_ne_u32 vcc, v116, v111\n"
"v_mfma_f32_16x16x32_bf16 a[248:251], a[52:55], v[52:55], a[248:251]\n"
"s_cbranch_vccnz .Lmg_hs16\n"
".Lmg_hr16:\n"
"v_mfma_f32_16x16x32_bf16 a[252:255], a[56:59], v[52:55], a[252:255]\n"
"buffer_load_dwordx4 v[228:231], v113, s[32:35], s72 offen offset:0 sc1\n"
"v_mfma_f32_16x16x32_bf16 v[0:3], a[60:63], v[56:59], v[0:3]\n"
"v_mfma_f32_16x16x32_bf16 v[4:7], a[64:67], v[56:59], v[4:7]\n"
"v_mfma_f32_16x16x32_bf16 v[8:11], a[68:71], v[56:59], v[8:11]\n"
"v_mfma_f32_16x16x32_bf16 v[12:15], a[72:75], v[56:59], v[12:15]\n"
"buffer_load_dwordx4 v[232:235], v114, s[32:35], s72 offen offset:0 sc1\n"
"v_mfma_f32_16x16x32_bf16 v[16:19], a[76:79], v[56:59], v[16:19]\n"
"v_mfma_f32_16x16x32_bf16 v[20:23], a[60:63], v[60:63], v[20:23]\n"
"v_mfma_f32_16x16x32_bf16 v[24:27], a[64:67], v[60:63], v[24:27]\n"
"v_mfma_f32_16x16x32_bf16 v[28:31], a[68:71], v[60:63], v[28:31]\n"
"buffer_load_dwordx4 v[236:239], v115, s[32:35], s72 offen offset:0 sc1\n"
"v_mfma_f32_16x16x32_bf16 v[32:35], a[72:75], v[60:63], v[32:35]\n"
"v_mfma_f32_16x16x32_bf16 v[36:39], a[76:79], v[60:63], v[36:39]\n"
"v_mfma_f32_16x16x32_bf16 a[216:219], a[60:63], v[64:67], a[216:219]\n"
"v_mfma_f32_16x16x32_bf16 a[220:223], a[64:67], v[64:67], a[220:223]\n"
"buffer_load_dword v109, v119, s[32:35], s73 offen sc1\n"
"buffer_load_dwordx4 v[240:243], v112, s[32:35], s73 offen offset:0 sc1\n"
"v_mfma_f32_16x16x32_bf16 a[224:227], a[68:71], v[64:67], a[224:227]\n"
"v_mfma_f32_16x16x32_bf16 a[228:231], a[72:75], v[64:67], a[228:231]\n"
"v_mfma_f32_16x16x32_bf16 a[232:235], a[76:79], v[64:67], a[232:235]\n"
"v_mfma_f32_16x16x32_bf16 a[236:239], a[60:63], v[68:71], a[236:239]\n"
"buffer_load_dwordx4 v[244:247], v113, s[32:35], s73 offen offset:0 sc1\n"
"v_mfma_f32_16x16x32_bf16 a[240:243], a[64:67], v[68:71], a[240:243]\n"
"v_mfma_f32_16x16x32_bf16 a[244:247], a[68:71], v[68:71], a[244:247]\n"
"v_mfma_f32_16x16x32_bf16 a[248:251], a[72:75], v[68:71], a[248:251]\n"
"v_mfma_f32_16x16x32_bf16 a[252:255], a[76:79], v[68:71], a[252:255]\n"
"buffer_load_dwordx4 v[248:251], v114, s[32:35], s73 offen offset:0 sc1\n"
"s_cmp_ge_u32 s30, 2\n"
"s_cbranch_scc1 .Lmg_skip6\n"
"s_waitcnt vmcnt(15)\n"
"v_add3_u32 v116, v192, v193, s61\n"
"v_add3_u32 v116, v194, v195, v116\n"
"v_add3_u32 v116, v196, v197, v116\n"
"v_add3_u32 v116, v198, v199, v116\n"
"v_add3_u32 v116, v200, v201, v116\n"
"v_add3_u32 v116, v202, v203, v116\n"
"v_add3_u32 v116, v204, v205, v116\n"
"v_add3_u32 v116, v206, v207, v116\n"
"v_cmp_ne_u32 vcc, v116, v100\n"
"s_cbranch_vccnz .Lmg_hs17\n"
".Lmg_hr17:\n"
"v_mfma_f32_16x16x32_bf16 v[0:3], a[80:83], v[192:195], v[0:3]\n"
"v_mfma_f32_16x16x32_bf16 v[4:7], a[84:87], v[192:195], v[4:7]\n"
"v_mfma_f32_16x16x32_bf16 v[8:11], a[88:91], v[192:195], v[8:11]\n"
"v_mfma_f32_16x16x32_bf16 v[12:15], a[92:95], v[192:195], v[12:15]\n"
"buffer_load_dwordx4 v[252:255], v115, s[32:35], s73 offen offset:0 sc1\n"
"v_mfma_f32_16x16x32_bf16 v[16:19], a[96:99], v[192:195], v[16:19]\n"
"v_mfma_f32_16x16x32_bf16 v[20:23], a[80:83], v[196:199], v[20:23]\n"
"v_mfma_f32_16x16x32_bf16 v[24:27], a[84:87], v[196:199], v[24:27]\n"
"v_mfma_f32_16x16x32_bf16 v[28:31], a[88:91], v[196:199], v[28:31]\n"
"buffer_load_dword v110, v119, s[32:35], s74 offen sc1\n"
"buffer_load_dwordx4 v[40:43], v112, s[32:35], s74 offen offset:0 sc1\n"
"v_mfma_f32_16x16x32_bf16 v[32:35], a[92:95], v[196:199], v[32:35]\n"
"v_mfma_f32_16x16x32_bf16 v[36:39], a[96:99], v[196:199], v[36:39]\n"
"v_mfma_f32_16x16x32_bf16 a[216:219], a[80:83], v[200:203], a[216:219]\n"
"v_mfma_f32_16x16x32_bf16 a[220:223], a[84:87], v[200:203], a[220:223]\n"
"buffer_load_dwordx4 v[44:47], v113, s[32:35], s74 offen offset:0 sc1\n"
"v_mfma_f32_16x16x32_bf16 a[224:227], a[88:91], v[200:203], a[224:227]\n"
"s_waitcnt vmcnt(13)\n"
"v_add3_u32 v116, v208, v209, s61\n"
"v_mfma_f32_16x16x32_bf16 a[228:231], a[92:95], v[200:203], a[228:231]\n"
"v_add3_u32 v116, v210, v211, v116\n"
"v_add3_u32 v116, v212, v213, v116\n"
"v_mfma_f32_16x16x32_bf16 a[232:235], a[96:99], v[200:203], a[232:235]\n"
"v_add3_u32 v116, v214, v215, v116\n"
"v_add3_u32 v116, v216, v217, v116\n"
"v_mfma_f32_16x16x32_bf16 a[236:239], a[80:83], v[204:207], a[236:239]\n"
"buffer_load_dwordx4 v[48:51], v114, s[32:35], s74 offen offset:0 sc1\n"
"v_mfma_f32_16x16x32_bf16 a[240:243], a[84:87], v[204:207], a[240:243]\n"
"v_add3_u32 v116, v218, v219, v116\n"
"v_add3_u32 v116, v220, v221, v116\n"
"v_mfma_f32_16x16x32_bf16 a[244:247], a[88:91], v[204:207], a[244:247]\n"
"v_add3_u32 v116, v222, v223, v116\n"
"v_cmp_ne_u32 vcc, v116, v101\n"
"v_mfma_f32_16x16x32_bf16 a[248:251], a[92:95], v[204:207], a[248:251]\n"
"s_cbranch_vccnz .Lmg_hs18\n"
".Lmg_hr18:\n"
"v_mfma_f32_16x16x32_bf16 a[252:255], a[96:99], v[204:207], a[252:255]\n"
"buffer_load_dwordx4 v[52:55], v115, s[32:35], s74 offen offset:0 sc1\n"
".Lmg_back6:\n"
"v_mfma_f32_16x16x32_bf16 v[0:3], a[100:103], v[208:211], v[0:3]\n"
"v_mfma_f32_16x16x32_bf16 v[4:7], a[104:107], v[208:211], v[4:7]\n"
"v_mfma_f32_16x16x32_bf16 v[8:11], a[108:111], v[208:211], v[8:11]\n"
"v_mfma_f32_16x16x32_bf16 v[12:15], a[112:115], v[208:211], v[12:15]\n"
"buffer_load_dword v111, v119, s[32:35], s75 offen sc1\n"
"buffer_load_dwordx4 v[56:59], v112, s[32:35], s75 offen offset:0 sc1\n"
"v_mfma_f32_16x16x32_bf16 v[16:19], a[116:119], v[208:211], v[16:19]\n"
"v_mfma_f32_16x16x32_bf16 v[20:23], a[100:103], v[212:215], v[20:23]\n"
"v_mfma_f32_16x16x32_bf16 v[24:27], a[104:107], v[212:215], v[24:27]\n"
"v_mfma_f32_16x16x32_bf16 v[28:31], a[108:111], v[212:215], v[28:31]\n"
"buffer_load_dwordx4 v[60:63], v113, s[32:35], s75 offen offset:0 sc1\n"
"v_mfma_f32_16x16x32_bf16 v[32:35], a[112:115], v[212:215], v[32:35]\n"
"v_mfma_f32_16x16x32_bf16 v[36:39], a[116:119], v[212:215], v[36:39]\n"
"v_mfma_f32_16x16x32_bf16 a[216:219], a[100:103], v[216:219], a[216:219]\n"
"v_mfma_f32_16x16x32_bf16 a[220:223], a[104:107], v[216:219], a[220:223]\n"
"buffer_load_dwordx4 v[64:67], v114, s[32:35], s75 offen offset:0 sc1\n"
"v_mfma_f32_16x16x32_bf16 a[224:227], a[108:111], v[216:219], a[224:227]\n"
"s_waitcnt vmcnt(14)\n"
"v_add3_u32 v116, v224, v225, s61\n"
"v_mfma_f32_16x16x32_bf16 a[228:231], a[112:115], v[216:219], a[228:231]\n"
"v_add3_u32 v116, v226, v227, v116\n"
"v_add3_u32 v116, v228, v229, v116\n"
"v_mfma_f32_16x16x32_bf16 a[232:235], a[116:119], v[216:219], a[232:235]\n"
"v_add3_u32 v116, v230, v231, v116\n"
"v_add3_u32 v116, v232, v233, v116\n"
"v_mfma_f32_16x16x32_bf16 a[236:239], a[100:103], v[220:223], a[236:239]\n"
"buffer_load_dwordx4 v[68:71], v115, s[32:35], s75 offen offset:0 sc1\n"
"v_mfma_f32_16x16x32_bf16 a[240:243], a[104:107], v[220:223], a[240:243]\n"
"v_add3_u32 v116, v234, v235, v116\n"
"v_add3_u32 v116, v236, v237, v116\n"
"v_mfma_f32_16x16x32_bf16 a[244:247], a[108:111], v[220:223], a[244:247]\n"
"v_add3_u32 v116, v238, v239, v116\n"
"v_cmp_ne_u32 vcc, v116, v108\n"
"v_mfma_f32_16x16x32_bf16 a[248:251], a[112:115], v[220:223], a[248:251]\n"
"s_cbranch_vccnz .Lmg_hs20\n"
".Lmg_hr20:\n"
"v_mfma_f32_16x16x32_bf16 a[252:255], a[116:119], v[220:223], a[252:255]\n"
"buffer_load_dword v100, v119, s[32:35], s76 offen sc1\n"
"buffer_load_dwordx4 v[192:195], v112, s[32:35], s76 offen offset:0 sc1\n"
"v_mfma_f32_16x16x32_bf16 v[0:3], a[120:123], v[224:227], v[0:3]\n"
"v_mfma_f32_16x16x32_bf16 v[4:7], a[124:127], v[224:227], v[4:7]\n"
"v_mfma_f32_16x16x32_bf16 v[8:11], a[128:131], v[224:227], v[8:11]\n"
"v_mfma_f32_16x16x32_bf16 v[12:15], a[132:135], v[224:227], v[12:15]\n"
"buffer_load_dwordx4 v[196:199], v113, s[32:35], s76 offen offset:0 sc1\n"
"v_mfma_f32_16x16x32_bf16 v[16:19], a[136:139], v[224:227], v[16:19]\n"
"v_mfma_f32_16x16x32_bf16 v[20:23], a[120:123], v[228:231], v[20:23]\n"
"v_mfma_f32_16x16x32_bf16 v[24:27], a[124:127], v[228:231], v[24:27]\n"
"v_mfma_f32_16x16x32_bf16 v[28:31], a[128:131], v[228:231], v[28:31]\n"
"buffer_load_dwordx4 v[200:203], v114, s[32:35], s76 offen offset:0 sc1\n"
"v_mfma_f32_16x16x32_bf16 v[32:35], a[132:135], v[228:231], v[32:35]\n"
"v_mfma_f32_16x16x32_bf16 v[36:39], a[136:139], v[228:231], v[36:39]\n"
"v_mfma_f32_16x16x32_bf16 a[216:219], a[120:123], v[232:235], a[216:219]\n"
"v_mfma_f32_16x16x32_bf16 a[220:223], a[124:127], v[232:235], a[220:223]\n"
"buffer_load_dwordx4 v[204:207], v115, s[32:35], s76 offen offset:0 sc1\n"
"v_mfma_f32_16x16x32_bf16 a[224:227], a[128:131], v[232:235], a[224:227]\n"
"s_waitcnt vmcnt(15)\n"
"v_add3_u32 v116, v240, v241, s61\n"
"v_mfma_f32_16x16x32_bf16 a[228:231], a[132:135], v[232:235], a[228:231]\n"
"v_add3_u32 v116, v242, v243, v116\n"
"v_add3_u32 v116, v244, v245, v116\n"
"v_mfma_f32_16x16x32_bf16 a[232:235], a[136:139], v[232:235], a[232:235]\n"
"v_add3_u32 v116, v246, v247, v116\n"
"v_add3_u32 v116, v248, v249, v116\n"
"v_mfma_f32_16x16x32_bf16 a[236:239], a[120:123], v[236:239], a[236:239]\n"
"buffer_load_dwordx4 a[212:215], v127, s[32:35], s13 offen offset:1024 sc1\n"
"v_mfma_f32_16x16x32_bf16 a[240:243], a[124:127], v[236:239], a[240:243]\n"
"v_add3_u32 v116, v250, v251, v116\n"
"v_add3_u32 v116, v252, v253, v116\n"
"v_mfma_f32_16x16x32_bf16 a[244:247], a[128:131], v[236:239], a[244:247]\n"
"v_add3_u32 v116, v254, v255, v116\n"
"v_cmp_ne_u32 vcc, v116, v109\n"
"v_mfma_f32_16x16x32_bf16 a[248:251], a[132:135], v[236:239], a[248:251]\n"
"s_cbranch_vccnz .Lmg_hs21\n"
".Lmg_hr21:\n"
"v_mfma_f32_16x16x32_bf16 a[252:255], a[136:139], v[236:239], a[252:255]\n"
"buffer_load_dword v101, v119, s[32:35], s77 offen sc1\n"
"buffer_load_dwordx4 v[208:211], v112, s[32:35], s77 offen offset:0 sc1\n"
"v_mfma_f32_16x16x32_bf16 v[0:3], a[140:143], v[240:243], v[0:3]\n"
"v_mfma_f32_16x16x32_bf16 v[4:7], a[144:147], v[240:243], v[4:7]\n"
"v_mfma_f32_16x16x32_bf16 v[8:11], a[148:151], v[240:243], v[8:11]\n"
"v_mfma_f32_16x16x32_bf16 v[12:15], a[152:155], v[240:243], v[12:15]\n"
"buffer_load_dwordx4 v[212:215], v113, s[32:35], s77 offen offset:0 sc1\n"
"v_mfma_f32_16x16x32_bf16 v[16:19], a[156:159], v[240:243], v[16:19]\n"
"v_mfma_f32_16x16x32_bf16 v[20:23], a[140:143], v[244:247], v[20:23]\n"
"v_mfma_f32_16x16x32_bf16 v[24:27], a[144:147], v[244:247], v[24:27]\n"
"v_mfma_f32_16x16x32_bf16 v[28:31], a[148:151], v[244:247], v[28:31]\n"
"buffer_load_dwordx4 v[216:219], v114, s[32:35], s77 offen offset:0 sc1\n"
"v_mfma_f32_16x16x32_bf16 v[32:35], a[152:155], v[244:247], v[32:35]\n"
"v_mfma_f32_16x16x32_bf16 v[36:39], a[156:159], v[244:247], v[36:39]\n"
"v_mfma_f32_16x16x32_bf16 a[216:219], a[140:143], v[248:251], a[216:219]\n"
"v_mfma_f32_16x16x32_bf16 a[220:223], a[144:147], v[248:251], a[220:223]\n"
"buffer_load_dwordx4 v[220:223], v115, s[32:35], s77 offen offset:0 sc1\n"
"v_mfma_f32_16x16x32_bf16 a[224:227], a[148:151], v[248:251], a[224:227]\n"
"s_waitcnt vmcnt(16)\n"
"v_add3_u32 v116, v40, v41, s61\n"
"v_mfma_f32_16x16x32_bf16 a[228:231], a[152:155], v[248:251], a[228:231]\n"
"v_add3_u32 v116, v42, v43, v116\n"
"v_add3_u32 v116, v44, v45, v116\n"
"v_mfma_f32_16x16x32_bf16 a[232:235], a[156:159], v[248:251], a[232:235]\n"
"v_add3_u32 v116, v46, v47, v116\n"
"v_add3_u32 v116, v48, v49, v116\n"
"v_mfma_f32_16x16x32_bf16 a[236:239], a[140:143], v[252:255], a[236:239]\n"
"v_add3_u32 v116, v50, v51, v116\n"
"v_add3_u32 v116, v52, v53, v116\n"
"v_mfma_f32_16x16x32_bf16 a[240:243], a[144:147], v[252:255], a[240:243]\n"
"v_add3_u32 v116, v54, v55, v116\n"
"v_cmp_ne_u32 vcc, v116, v110\n"
"v_mfma_f32_16x16x32_bf16 a[244:247], a[148:151], v[252:255], a[244:247]\n"
"s_cbranch_vccnz .Lmg_hs22\n"
".Lmg_hr22:\n"
"v_mfma_f32_16x16x32_bf16 a[248:251], a[152:155], v[252:255], a[248:251]\n"
"v_mfma_f32_16x16x32_bf16 a[252:255], a[156:159], v[252:255], a[252:255]\n"
"v_mfma_f32_16x16x32_bf16 v[0:3], v[168:171], v[40:43], v[0:3]\n"
"buffer_load_dwordx2 v[72:73], v106, s[32:35], s22 offen sc1\n"
"v_mfma_f32_16x16x32_bf16 v[4:7], v[172:175], v[40:43], v[4:7]\n"
"buffer_load_dwordx2 v[74:75], v106, s[32:35], s22 offen offset:128 sc1\n"
"v_mfma_f32_16x16x32_bf16 v[8:11], v[176:179], v[40:43], v[8:11]\n"
"v_mfma_f32_16x16x32_bf16 v[12:15], v[180:183], v[40:43], v[12:15]\n"
"v_mfma_f32_16x16x32_bf16 v[16:19], v[184:187], v[40:43], v[16:19]\n"
"v_mfma_f32_16x16x32_bf16 v[20:23], v[168:171], v[44:47], v[20:23]\n"
"s_waitcnt vmcnt(13)\n"
"v_add3_u32 v116, v56, v57, s61\n"
"v_mfma_f32_16x16x32_bf16 v[24:27], v[172:175], v[44:47], v[24:27]\n"
"v_add3_u32 v116, v58, v59, v116\n"
"v_add3_u32 v116, v60, v61, v116\n"
"v_mfma_f32_16x16x32_bf16 v[28:31], v[176:179], v[44:47], v[28:31]\n"
"v_add3_u32 v116, v62, v63, v116\n"
"v_add3_u32 v116, v64, v65, v116\n"
"v_mfma_f32_16x16x32_bf16 v[32:35], v[180:183], v[44:47], v[32:35]\n"
"v_add3_u32 v116, v66, v67, v116\n"
"v_add3_u32 v116, v68, v69, v116\n"
"v_mfma_f32_16x16x32_bf16 v[36:39], v[184:187], v[44:47], v[36:39]\n"
"v_add3_u32 v116, v70, v71, v116\n"
"v_cmp_ne_u32 vcc, v116, v111\n"
"v_mfma_f32_16x16x32_bf16 a[216:219], v[168:171], v[48:51], a[216:219]\n"
"s_cbranch_vccnz .Lmg_hs23\n"
".Lmg_hr23:\n"
"v_mfma_f32_16x16x32_bf16 a[220:223], v[172:175], v[48:51], a[220:223]\n"
"v_mfma_f32_16x16x32_bf16 a[224:227], v[176:179], v[48:51], a[224:227]\n"
"s_waitcnt vmcnt(8)\n"
"v_add3_u32 v116, v192, v193, s61\n"
"v_mfma_f32_16x16x32_bf16 a[228:231], v[180:183], v[48:51], a[228:231]\n"
"v_add3_u32 v116, v194, v195, v116\n"
"v_add3_u32 v116, v196, v197, v116\n"
"v_mfma_f32_16x16x32_bf16 a[232:235], v[184:187], v[48:51], a[232:235]\n"
"v_add3_u32 v116, v198, v199, v116\n"
"v_add3_u32 v116, v200, v201, v116\n"
"v_mfma_f32_16x16x32_bf16 a[236:239], v[168:171], v[52:55], a[236:239]\n"
"v_add3_u32 v116, v202, v203, v116\n"
"v_add3_u32 v116, v204, v205, v116\n"
"v_mfma_f32_16x16x32_bf16 a[240:243], v[172:175], v[52:55], a[240:243]\n"
"v_add3_u32 v116, v206, v207, v116\n"
"v_cmp_ne_u32 vcc, v116, v100\n"
"v_mfma_f32_16x16x32_bf16 a[244:247], v[176:179], v[52:55], a[244:247]\n"
"s_cbranch_vccnz .Lmg_hs24\n"
".Lmg_hr24:\n"
"v_mfma_f32_16x16x32_bf16 a[248:251], v[180:183], v[52:55], a[248:251]\n"
"v_mfma_f32_16x16x32_bf16 a[252:255], v[184:187], v[52:55], a[252:255]\n"
"v_mfma_f32_16x16x32_bf16 v[20:23], v[188:191], v[60:63], v[20:23]\n"
"v_mfma_f32_16x16x32_bf16 v[24:27], a[160:163], v[60:63], v[24:27]\n"
"v_mfma_f32_16x16x32_bf16 v[28:31], a[164:167], v[60:63], v[28:31]\n"
"v_mfma_f32_16x16x32_bf16 v[32:35], a[168:171], v[60:63], v[32:35]\n"
"s_waitcnt vmcnt(2)\n"
"v_add3_u32 v116, v208, v209, s61\n"
"v_mfma_f32_16x16x32_bf16 v[36:39], a[172:175], v[60:63], v[36:39]\n"
"v_add3_u32 v116, v210, v211, v116\n"
"v_add3_u32 v116, v212, v213, v116\n"
"v_mfma_f32_16x16x32_bf16 v[20:23], a[176:179], v[196:199], v[20:23]\n"
"v_add3_u32 v116, v214, v215, v116\n"
"v_add3_u32 v116, v216, v217, v116\n"
"v_mfma_f32_16x16x32_bf16 v[24:27], a[180:183], v[196:199], v[24:27]\n"
"v_add3_u32 v116, v218, v219, v116\n"
"v_add3_u32 v116, v220, v221, v116\n"
"v_mfma_f32_16x16x32_bf16 v[28:31], a[184:187], v[196:199], v[28:31]\n"
"v_add3_u32 v116, v222, v223, v116\n"
"v_cmp_ne_u32 vcc, v116, v101\n"
"v_mfma_f32_16x16x32_bf16 v[32:35], a[188:191], v[196:199], v[32:35]\n"
"s_cbranch_vccnz .Lmg_hs25\n"
".Lmg_hr25:\n"
"v_mfma_f32_16x16x32_bf16 v[36:39], a[192:195], v[196:199], v[36:39]\n"
"v_mfma_f32_16x16x32_bf16 v[20:23], a[196:199], v[212:215], v[20:23]\n"
"v_mfma_f32_16x16x32_bf16 v[24:27], a[200:203], v[212:215], v[24:27]\n"
"v_mfma_f32_16x16x32_bf16 v[28:31], a[204:207], v[212:215], v[28:31]\n"
"v_mfma_f32_16x16x32_bf16 v[32:35], a[208:211], v[212:215], v[32:35]\n"
"v_mfma_f32_16x16x32_bf16 v[36:39], a[212:215], v[212:215], v[36:39]\n"
"v_mfma_f32_16x16x32_bf16 a[216:219], v[188:191], v[64:67], a[216:219]\n"
"s_nop 2\n"
"ds_write_b128 v121, v[20:23] offset:0\n"
"v_mfma_f32_16x16x32_bf16 a[216:219], a[176:179], v[200:203], a[216:219]\n"
"s_waitcnt vmcnt(1)\n"
"v_xor_b32 v104, s31, v73\n"
"s_waitcnt vmcnt(0)\n"
"v_bitop3_b32 v104, v104, v75, s31 bitop3:0xf6\n"
"v_mfma_f32_16x16x32_bf16 a[216:219], a[196:199], v[216:219], a[216:219]\n"
"v_cmp_ne_u32 vcc, 0, v104\n"
"s_nop 0\n"
"v_mfma_f32_16x16x32_bf16 a[220:223], a[160:163], v[64:67], a[220:223]\n"
"ds_write_b128 v121, v[24:27] offset:1024\n"
"v_mfma_f32_16x16x32_bf16 a[220:223], a[180:183], v[200:203], a[220:223]\n"
"s_cbranch_vccnz .Lmg_sqs\n"
".Lmg_sqr:\n"
"v_add_f32 v118, v72, v74\n"
"v_mfma_f32_16x16x32_bf16 a[220:223], a[200:203], v[216:219], a[220:223]\n"
"v_mov_b32 v104, v118\n"
"s_nop 0\n"
"v_mfma_f32_16x16x32_bf16 a[224:227], a[164:167], v[64:67], a[224:227]\n"
"ds_write_b128 v121, v[28:31] offset:2048\n"
"v_mfma_f32_16x16x32_bf16 a[224:227], a[184:187], v[200:203], a[224:227]\n"
"v_permlane16_swap_b32 v118, v104\n"
"v_add_f32 v118, v118, v104\n"
"v_mfma_f32_16x16x32_bf16 a[224:227], a[204:207], v[216:219], a[224:227]\n"
"v_mov_b32 v104, v118\n"
"s_nop 0\n"
"v_mfma_f32_16x16x32_bf16 a[228:231], a[168:171], v[64:67], a[228:231]\n"
"ds_write_b128 v121, v[32:35] offset:3072\n"
"v_mfma_f32_16x16x32_bf16 a[228:231], a[188:191], v[200:203], a[228:231]\n"
"v_permlane32_swap_b32 v118, v104\n"
"v_add_f32 v118, v118, v104\n"
"v_mfma_f32_16x16x32_bf16 a[228:231], a[208:211], v[216:219], a[228:231]\n"
"v_mul_f32 v118, 0x3acccccd, v118\n"
"v_fma_f32 v118, -v120, v120, v118\n"
"v_mfma_f32_16x16x32_bf16 a[232:235], a[172:175], v[64:67], a[232:235]\n"
"ds_write_b128 v121, v[36:39] offset:4096\n"
"v_mfma_f32_16x16x32_bf16 a[232:235], a[192:195], v[200:203], a[232:235]\n"
"v_max_f32 v118, 0, v118\n"
"v_cvt_pk_bf16_f32 v118, v118, v118\n"
"v_mfma_f32_16x16x32_bf16 a[232:235], a[212:215], v[216:219], a[232:235]\n"
"v_and_b32 v118, 0xffff0000, v118\n"
"v_add_f32 v118, 0x3727c5ac, v118\n"
"v_mfma_f32_16x16x32_bf16 a[236:239], v[188:191], v[68:71], a[236:239]\n"
"ds_write_b128 v122, a[216:219] offset:0\n"
"v_mfma_f32_16x16x32_bf16 a[236:239], a[176:179], v[204:207], a[236:239]\n"
"v_cvt_pk_bf16_f32 v118, v118, v118\n"
"v_and_b32 v118, 0xffff0000, v118\n"
"v_mfma_f32_16x16x32_bf16 a[236:239], a[196:199], v[220:223], a[236:239]\n"
"v_sqrt_f32 v118, v118\n"
"s_nop 0\n"
"v_mfma_f32_16x16x32_bf16 a[240:243], a[160:163], v[68:71], a[240:243]\n"
"ds_write_b128 v122, a[220:223] offset:1024\n"
"v_mfma_f32_16x16x32_bf16 a[240:243], a[180:183], v[204:207], a[240:243]\n"
"v_cvt_pk_bf16_f32 v118, v118, v118\n"
"v_and_b32 v118, 0xffff0000, v118\n"
"v_mfma_f32_16x16x32_bf16 a[240:243], a[200:203], v[220:223], a[240:243]\n"
"v_rcp_f32 v118, v118\n"
"v_mfma_f32_16x16x32_bf16 a[244:247], a[164:167], v[68:71], a[244:247]\n"
"ds_write_b128 v122, a[224:227] offset:2048\n"
"v_mfma_f32_16x16x32_bf16 a[244:247], a[184:187], v[204:207], a[244:247]\n"
"v_mfma_f32_16x16x32_bf16 a[244:247], a[204:207], v[220:223], a[244:247]\n"
"v_mfma_f32_16x16x32_bf16 a[248:251], a[168:171], v[68:71], a[248:251]\n"
"ds_write_b128 v122, a[228:231] offset:3072\n"
"v_mfma_f32_16x16x32_bf16 a[248:251], a[188:191], v[204:207], a[248:251]\n"
"v_mfma_f32_16x16x32_bf16 a[248:251], a[208:211], v[220:223], a[248:251]\n"
"v_mfma_f32_16x16x32_bf16 a[252:255], a[172:175], v[68:71], a[252:255]\n"
"ds_write_b128 v122, a[232:235] offset:4096\n"
"v_mfma_f32_16x16x32_bf16 a[252:255], a[192:195], v[204:207], a[252:255]\n"
"v_mfma_f32_16x16x32_bf16 a[252:255], a[212:215], v[220:223], a[252:255]\n"
"v_mfma_f32_16x16x32_bf16 v[0:3], v[188:191], v[56:59], v[0:3]\n"
"ds_write_b128 v123, a[236:239] offset:0\n"
"v_mfma_f32_16x16x32_bf16 v[0:3], a[176:179], v[192:195], v[0:3]\n"
"v_mfma_f32_16x16x32_bf16 v[0:3], a[196:199], v[208:211], v[0:3]\n"
"v_mfma_f32_16x16x32_bf16 v[4:7], a[160:163], v[56:59], v[4:7]\n"
"ds_write_b128 v123, a[240:243] offset:1024\n"
"v_mfma_f32_16x16x32_bf16 v[4:7], a[180:183], v[192:195], v[4:7]\n"
"v_mfma_f32_16x16x32_bf16 v[4:7], a[200:203], v[208:211], v[4:7]\n"
"v_mfma_f32_16x16x32_bf16 v[8:11], a[164:167], v[56:59], v[8:11]\n"
"ds_write_b128 v123, a[244:247] offset:2048\n"
"v_mfma_f32_16x16x32_bf16 v[8:11], a[184:187], v[192:195], v[8:11]\n"
"v_mfma_f32_16x16x32_bf16 v[8:11], a[204:207], v[208:211], v[8:11]\n"
"v_mfma_f32_16x16x32_bf16 v[12:15], a[168:171], v[56:59], v[12:15]\n"
"ds_write_b128 v123, a[248:251] offset:3072\n"
"v_mfma_f32_16x16x32_bf16 v[12:15], a[188:191], v[192:195], v[12:15]\n"
"v_mfma_f32_16x16x32_bf16 v[12:15], a[208:211], v[208:211], v[12:15]\n"
"v_mfma_f32_16x16x32_bf16 v[16:19], a[172:175], v[56:59], v[16:19]\n"
"ds_write_b128 v123, a[252:255] offset:4096\n"
"v_mfma_f32_16x16x32_bf16 v[16:19], a[192:195], v[192:195], v[16:19]\n"
"v_mfma_f32_16x16x32_bf16 v[16:19], a[212:215], v[208:211], v[16:19]\n"
"s_waitcnt lgkmcnt(0)\n"
"s_barrier\n"
"ds_read_b128 v[20:23], v125 offset:0\n"
"ds_read_b128 v[40:43], v125 offset:5120\n"
"ds_read_b128 v[60:63], v125 offset:10240\n"
"ds_read_b128 v[24:27], v125 offset:1024\n"
"ds_read_b128 v[44:47], v125 offset:6144\n"
"ds_read_b128 v[64:67], v125 offset:11264\n"
"ds_read_b128 v[28:31], v125 offset:2048\n"
"ds_read_b128 v[48:51], v125 offset:7168\n"
"ds_read_b128 v[68:71], v125 offset:12288\n"
"ds_read_b128 v[32:35], v125 offset:3072\n"
"ds_read_b128 v[52:55], v125 offset:8192\n"
"ds_read_b128 v[72:75], v125 offset:13312\n"
"ds_read_b128 v[36:39], v125 offset:4096\n"
"ds_read_b128 v[56:59], v125 offset:9216\n"
"ds_read_b128 v[76:79], v125 offset:14336\n"
"s_waitcnt lgkmcnt(12)\n"
"v_pk_add_f32 v[0:1], v[0:1], v[20:21]\n"
"v_pk_add_f32 v[2:3], v[2:3], v[22:23]\n"
"v_pk_add_f32 v[40:41], v[40:41], v[60:61]\n"
"v_pk_add_f32 v[42:43], v[42:43], v[62:63]\n"
"v_pk_add_f32 v[0:1], v[0:1], v[40:41]\n"
"v_pk_add_f32 v[2:3], v[2:3], v[42:43]\n"
"v_pk_fma_f32 v[0:1], v[0:1], v[118:119], v[80:81] op_sel_hi:[1,0,1]\n"
"v_pk_fma_f32 v[2:3], v[2:3], v[118:119], v[82:83] op_sel_hi:[1,0,1]\n"
"v_cvt_pk_bf16_f32 v102, v0, v1\n"
"v_cvt_pk_bf16_f32 v103, v2, v3\n"
"s_waitcnt lgkmcnt(9)\n"
"v_pk_add_f32 v[4:5], v[4:5], v[24:25]\n"
"v_pk_add_f32 v[6:7], v[6:7], v[26:27]\n"
"v_pk_add_f32 v[44:45], v[44:45], v[64:65]\n"
"v_pk_add_f32 v[46:47], v[46:47], v[66:67]\n"
"v_pk_add_f32 v[4:5], v[4:5], v[44:45]\n"
"v_pk_add_f32 v[6:7], v[6:7], v[46:47]\n"
"v_pk_fma_f32 v[4:5], v[4:5], v[118:119], v[84:85] op_sel_hi:[1,0,1]\n"
"v_pk_fma_f32 v[6:7], v[6:7], v[118:119], v[86:87] op_sel_hi:[1,0,1]\n"
"v_cvt_pk_bf16_f32 v104, v4, v5\n"
"v_cvt_pk_bf16_f32 v105, v6, v7\n"
"global_store_dwordx4 v124, v[102:105], s[26:27]\n"
"s_waitcnt lgkmcnt(6)\n"
"v_pk_add_f32 v[8:9], v[8:9], v[28:29]\n"
"v_pk_add_f32 v[10:11], v[10:11], v[30:31]\n"
"v_pk_add_f32 v[48:49], v[48:49], v[68:69]\n"
"v_pk_add_f32 v[50:51], v[50:51], v[70:71]\n"
"v_pk_add_f32 v[8:9], v[8:9], v[48:49]\n"
"v_pk_add_f32 v[10:11], v[10:11], v[50:51]\n"
"v_pk_fma_f32 v[8:9], v[8:9], v[118:119], v[88:89] op_sel_hi:[1,0,1]\n"
"v_pk_fma_f32 v[10:11], v[10:11], v[118:119], v[90:91] op_sel_hi:[1,0,1]\n"
"v_cvt_pk_bf16_f32 v106, v8, v9\n"
"v_cvt_pk_bf16_f32 v107, v10, v11\n"
"s_waitcnt lgkmcnt(3)\n"
"v_pk_add_f32 v[12:13], v[12:13], v[32:33]\n"
"v_pk_add_f32 v[14:15], v[14:15], v[34:35]\n"
"v_pk_add_f32 v[52:53], v[52:53], v[72:73]\n"
"v_pk_add_f32 v[54:55], v[54:55], v[74:75]\n"
"v_pk_add_f32 v[12:13], v[12:13], v[52:53]\n"
"v_pk_add_f32 v[14:15], v[14:15], v[54:55]\n"
"v_pk_fma_f32 v[12:13], v[12:13], v[118:119], v[92:93] op_sel_hi:[1,0,1]\n"
"v_pk_fma_f32 v[14:15], v[14:15], v[118:119], v[94:95] op_sel_hi:[1,0,1]\n"
"v_cvt_pk_bf16_f32 v108, v12, v13\n"
"v_cvt_pk_bf16_f32 v109, v14, v15\n"
"global_store_dwordx4 v124, v[106:109], s[26:27] offset:64\n"
"s_waitcnt lgkmcnt(0)\n"
"v_pk_add_f32 v[16:17], v[16:17], v[36:37]\n"
"v_pk_add_f32 v[18:19], v[18:19], v[38:39]\n"
"v_pk_add_f32 v[56:57], v[56:57], v[76:77]\n"
"v_pk_add_f32 v[58:59], v[58:59], v[78:79]\n"
"v_pk_add_f32 v[16:17], v[16:17], v[56:57]\n"
"v_pk_add_f32 v[18:19], v[18:19], v[58:59]\n"
"v_pk_fma_f32 v[16:17], v[16:17], v[118:119], v[96:97] op_sel_hi:[1,0,1]\n"
"v_pk_fma_f32 v[18:19], v[18:19], v[118:119], v[98:99] op_sel_hi:[1,0,1]\n"
"v_cvt_pk_bf16_f32 v110, v16, v17\n"
"v_cvt_pk_bf16_f32 v111, v18, v19\n"
"global_store_dwordx2 v126, v[110:111], s[26:27] offset:128\n"
".Lmg_mainend:\n"
"s_endpgm\n"
".Lmg_hw2:\n"
"s_mul_i32 s98, s53, 245760\n"
"s_lshl_b32 s54, s54, 11\n"
"s_add_u32 s98, s98, s54\n"
"s_add_u32 s98, s98, 655360\n"
"s_mov_b32 s32, s28\n"
"s_and_b32 s33, s29, 0xffff\n"
"s_mov_b32 s34, 31457280\n"
"s_mov_b32 s35, 0x20000\n"
"s_mul_i32 s53, s30, 16896\n"
"v_mul_u32_u24 v119, 4096, v126\n"
"v_cmp_gt_u32 vcc, 8, v126\n"
"v_bfrev_b32 v49, 2\n"
"v_add_u32 v102, s53, v127\n"
"v_cndmask_b32 v119, v49, v119, vcc\n"
"v_min_u32 v103, 368, v127\n"
"s_mov_b32 s51, 0x3f803f80\n"
"v_bfe_u32 v41, v126, 2, 2\n"
"v_and_b32 v42, 3, v126\n"
"v_lshl_add_u32 v44, v40, 2, v41\n"
"v_mul_u32_u24 v44, 160, v44\n"
"v_add_u32 v44, s53, v44\n"
"v_lshl_add_u32 v50, v42, 4, v44\n"
"v_lshl_add_u32 v56, v42, 3, v44\n"
"v_lshrrev_b32 v119, 1, v119\n"
"s_sub_u32 s52, s2, 160\n"
"s_lshl_b32 s52, s52, 4\n"
"s_add_u32 s52, s52, 131072\n"
"v_add_u32 v12, 0, v58\n"
"v_mul_u32_u24 v13, 6554, v12\n"
"v_lshrrev_b32 v13, 16, v13\n"
"v_mul_u32_u24 v13, 1120, v13\n"
"v_lshl_add_u32 v51, v12, 4, v13\n"
"v_add_u32 v12, 64, v58\n"
"v_mul_u32_u24 v13, 6554, v12\n"
"v_lshrrev_b32 v13, 16, v13\n"
"v_mul_u32_u24 v13, 1120, v13\n"
"v_lshl_add_u32 v52, v12, 4, v13\n"
"v_add_u32 v12, 128, v58\n"
"v_mul_u32_u24 v13, 6554, v12\n"
"v_lshrrev_b32 v13, 16, v13\n"
"v_mul_u32_u24 v13, 1120, v13\n"
"v_lshl_add_u32 v53, v12, 4, v13\n"
"v_add_u32 v12, 192, v58\n"
"v_mul_u32_u24 v13, 6554, v12\n"
"v_lshrrev_b32 v13, 16, v13\n"
"v_mul_u32_u24 v13, 1120, v13\n"
"v_lshl_add_u32 v54, v12, 4, v13\n"
"v_add_u32 v12, 256, v58\n"
"v_mul_u32_u24 v13, 6554, v12\n"
"v_lshrrev_b32 v13, 16, v13\n"
"v_mul_u32_u24 v13, 1120, v13\n"
"v_lshl_add_u32 v55, v12, 4, v13\n"
"s_waitcnt lgkmcnt(0)\n"
"s_add_u32 s31, s31, 1\n"
"v_lshrrev_b32 v45, 1, v40\n"
"v_mul_u32_u24 v45, 3072, v45\n"
"v_and_b32 v46, 1, v40\n"
"v_lshl_add_u32 v46, v46, 2, v41\n"
"v_lshl_add_u32 v45, v46, 7, v45\n"
"v_lshrrev_b32 v46, 1, v46\n"
"v_lshl_add_u32 v45, v46, 5, v45\n"
"v_lshl_add_u32 v45, v42, 3, v45\n"
"v_xor_b32 v46, 32, v45\n"
"v_xor_b32 v47, 64, v45\n"
"v_xor_b32 v48, 96, v45\n"
"v_add_u32 v45, s53, v45\n"
"v_add_u32 v46, s53, v46\n"
"v_add_u32 v47, s53, v47\n"
"v_add_u32 v48, s53, v48\n"
"s_cmp_ge_u32 s2, 250\n"
"s_waitcnt vmcnt(0)\n"
"s_barrier\n"
"s_cbranch_scc1 .Lmg_hj\n"
"ds_read_b64_tr_b16 v[128:129], v45 offset:0\n"
"ds_read_b64_tr_b16 v[130:131], v45 offset:6144\n"
"ds_read_b64_tr_b16 v[132:133], v46 offset:0\n"
"ds_read_b64_tr_b16 v[134:135], v46 offset:6144\n"
"ds_read_b64_tr_b16 v[136:137], v47 offset:0\n"
"ds_read_b64_tr_b16 v[138:139], v47 offset:6144\n"
"ds_read_b64_tr_b16 v[140:141], v48 offset:0\n"
"ds_read_b64_tr_b16 v[142:143], v48 offset:6144\n"
"ds_read_b64_tr_b16 v[144:145], v45 offset:1024\n"
"ds_read_b64_tr_b16 v[146:147], v45 offset:7168\n"
"ds_read_b64_tr_b16 v[148:149], v46 offset:1024\n"
"ds_read_b64_tr_b16 v[150:151], v46 offset:7168\n"
"ds_read_b64_tr_b16 v[152:153], v47 offset:1024\n"
"ds_read_b64_tr_b16 v[154:155], v47 offset:7168\n"
"ds_read_b64_tr_b16 v[156:157], v48 offset:1024\n"
"ds_read_b64_tr_b16 v[158:159], v48 offset:7168\n"
"ds_read_b64_tr_b16 v[160:161], v45 offset:2048\n"
"ds_read_b64_tr_b16 v[162:163], v45 offset:8192\n"
"ds_read_b64_tr_b16 v[164:165], v46 offset:2048\n"
"ds_read_b64_tr_b16 v[166:167], v46 offset:8192\n"
"ds_read_b64_tr_b16 v[168:169], v47 offset:2048\n"
"ds_read_b64_tr_b16 v[170:171], v47 offset:8192\n"
"ds_read_b64_tr_b16 v[172:173], v48 offset:2048\n"
"ds_read_b64_tr_b16 v[174:175], v48 offset:8192\n"
"s_waitcnt lgkmcnt(14)\n"
"buffer_store_dwordx4 v[128:131], v127, s[32:35], s98 offen offset:0 sc1\n"
"buffer_store_dwordx4 v[132:135], v127, s[32:35], s98 offen offset:1024 sc1\n"
"s_add_u32 s98, s98, 40960\n"
"buffer_store_dwordx4 v[136:139], v127, s[32:35], s98 offen offset:0 sc1\n"
"buffer_store_dwordx4 v[140:143], v127, s[32:35], s98 offen offset:1024 sc1\n"
"s_add_u32 s98, s98, 40960\n"
"buffer_store_dwordx4 v[144:147], v127, s[32:35], s98 offen offset:0 sc1\n"
"s_waitcnt lgkmcnt(12)\n"
"buffer_store_dwordx4 v[148:151], v127, s[32:35], s98 offen offset:1024 sc1\n"
"s_add_u32 s98, s98, 40960\n"
"s_waitcnt lgkmcnt(10)\n"
"buffer_store_dwordx4 v[152:155], v127, s[32:35], s98 offen offset:0 sc1\n"
"s_waitcnt lgkmcnt(8)\n"
"buffer_store_dwordx4 v[156:159], v127, s[32:35], s98 offen offset:1024 sc1\n"
"s_add_u32 s98, s98, 40960\n"
"s_waitcnt lgkmcnt(6)\n"
"buffer_store_dwordx4 v[160:163], v127, s[32:35], s98 offen offset:0 sc1\n"
"s_waitcnt lgkmcnt(4)\n"
"buffer_store_dwordx4 v[164:167], v127, s[32:35], s98 offen offset:1024 sc1\n"
"s_add_u32 s98, s98, 40960\n"
"s_waitcnt lgkmcnt(2)\n"
"buffer_store_dwordx4 v[168:171], v127, s[32:35], s98 offen offset:0 sc1\n"
"s_waitcnt lgkmcnt(0)\n"
"buffer_store_dwordx4 v[172:175], v127, s[32:35], s98 offen offset:1024 sc1\n"
"s_branch .Lmg_hj\n"
".Lmg_hs:\n"
"s_mov_b32 s32, s28\n"
"s_and_b32 s33, s29, 0xffff\n"
"s_mov_b32 s34, 31457280\n"
"s_mov_b32 s35, 0x20000\n"
"s_mul_i32 s53, s30, 16896\n"
"v_mul_u32_u24 v119, 4096, v126\n"
"v_cmp_gt_u32 vcc, 8, v126\n"
"v_bfrev_b32 v49, 2\n"
"v_add_u32 v102, s53, v127\n"
"v_cndmask_b32 v119, v49, v119, vcc\n"
"v_min_u32 v103, 368, v127\n"
"s_mov_b32 s51, 0x3f803f80\n"
"v_bfe_u32 v41, v126, 2, 2\n"
"v_and_b32 v42, 3, v126\n"
"v_lshl_add_u32 v44, v40, 2, v41\n"
"v_mul_u32_u24 v44, 160, v44\n"
"v_add_u32 v44, s53, v44\n"
"v_lshl_add_u32 v50, v42, 4, v44\n"
"v_lshl_add_u32 v56, v42, 3, v44\n"
"v_lshrrev_b32 v119, 1, v119\n"
"s_sub_u32 s52, s2, 160\n"
"s_lshl_b32 s52, s52, 4\n"
"s_add_u32 s52, s52, 131072\n"
"v_add_u32 v12, 0, v58\n"
"v_mul_u32_u24 v13, 6554, v12\n"
"v_lshrrev_b32 v13, 16, v13\n"
"v_mul_u32_u24 v13, 1120, v13\n"
"v_lshl_add_u32 v51, v12, 4, v13\n"
"v_add_u32 v12, 64, v58\n"
"v_mul_u32_u24 v13, 6554, v12\n"
"v_lshrrev_b32 v13, 16, v13\n"
"v_mul_u32_u24 v13, 1120, v13\n"
"v_lshl_add_u32 v52, v12, 4, v13\n"
"v_add_u32 v12, 128, v58\n"
"v_mul_u32_u24 v13, 6554, v12\n"
"v_lshrrev_b32 v13, 16, v13\n"
"v_mul_u32_u24 v13, 1120, v13\n"
"v_lshl_add_u32 v53, v12, 4, v13\n"
"v_add_u32 v12, 192, v58\n"
"v_mul_u32_u24 v13, 6554, v12\n"
"v_lshrrev_b32 v13, 16, v13\n"
"v_mul_u32_u24 v13, 1120, v13\n"
"v_lshl_add_u32 v54, v12, 4, v13\n"
"v_add_u32 v12, 256, v58\n"
"v_mul_u32_u24 v13, 6554, v12\n"
"v_lshrrev_b32 v13, 16, v13\n"
"v_mul_u32_u24 v13, 1120, v13\n"
"v_lshl_add_u32 v55, v12, 4, v13\n"
"s_waitcnt lgkmcnt(0)\n"
"s_add_u32 s31, s31, 1\n"
"v_lshrrev_b32 v2, 1, v40\n"
"v_lshlrev_b32 v2, 12, v2\n"
"v_and_b32 v3, 1, v40\n"
"v_lshl_add_u32 v2, v3, 3, v2\n"
"v_lshl_add_u32 v2, v43, 14, v2\n"
"v_cmp_gt_u32 vcc, 8, v43\n"
"v_mov_b32 v11, s31\n"
"v_mov_b32 v13, s31\n"
"v_cndmask_b32 v2, v49, v2, vcc\n"
"v_mov_b32 v15, s31\n"
"s_add_u32 s54, s97, 0\n"
"s_min_u32 s54, s54, 511\n"
"s_and_b32 s55, s54, 1\n"
"s_lshl_b32 s55, s55, 13\n"
"s_lshr_b32 s54, s54, 1\n"
"s_lshl_b32 s54, s54, 4\n"
"s_add_u32 s54, s54, s55\n"
"s_add_u32 s8, s54, 524288\n"
"s_add_u32 s54, s97, 1\n"
"s_min_u32 s54, s54, 511\n"
"s_and_b32 s55, s54, 1\n"
"s_lshl_b32 s55, s55, 13\n"
"s_lshr_b32 s54, s54, 1\n"
"s_lshl_b32 s54, s54, 4\n"
"s_add_u32 s54, s54, s55\n"
"s_add_u32 s9, s54, 524288\n"
"s_add_u32 s54, s97, 2\n"
"s_min_u32 s54, s54, 511\n"
"s_and_b32 s55, s54, 1\n"
"s_lshl_b32 s55, s55, 13\n"
"s_lshr_b32 s54, s54, 1\n"
"s_lshl_b32 s54, s54, 4\n"
"s_add_u32 s54, s54, s55\n"
"s_add_u32 s10, s54, 524288\n"
"v_mov_b32 v4, 0\n"
"v_mov_b32 v5, 0\n"
"v_mov_b32 v6, 0\n"
"v_mov_b32 v7, 0\n"
"v_mov_b32 v8, 0\n"
"v_mov_b32 v9, 0\n"
"s_cmp_ge_u32 s97, 512\n"
"s_cbranch_scc1 .Lmg_q3e\n"
"s_waitcnt vmcnt(10)\n"
"v_dot2c_f32_bf16 v4, s51, v128\n"
"v_dot2c_f32_bf16 v5, v128, v128\n"
"v_dot2c_f32_bf16 v4, s51, v129\n"
"v_dot2c_f32_bf16 v5, v129, v129\n"
"v_dot2c_f32_bf16 v4, s51, v130\n"
"v_dot2c_f32_bf16 v5, v130, v130\n"
"v_dot2c_f32_bf16 v4, s51, v131\n"
"v_dot2c_f32_bf16 v5, v131, v131\n"
"v_dot2c_f32_bf16 v4, s51, v132\n"
"v_dot2c_f32_bf16 v5, v132, v132\n"
"v_dot2c_f32_bf16 v4, s51, v133\n"
"v_dot2c_f32_bf16 v5, v133, v133\n"
"v_dot2c_f32_bf16 v4, s51, v134\n"
"v_dot2c_f32_bf16 v5, v134, v134\n"
"v_dot2c_f32_bf16 v4, s51, v135\n"
"v_dot2c_f32_bf16 v5, v135, v135\n"
"v_dot2c_f32_bf16 v4, s51, v136\n"
"v_dot2c_f32_bf16 v5, v136, v136\n"
"v_dot2c_f32_bf16 v4, s51, v137\n"
"v_dot2c_f32_bf16 v5, v137, v137\n"
"v_dot2c_f32_bf16 v4, s51, v138\n"
"v_dot2c_f32_bf16 v5, v138, v138\n"
"v_dot2c_f32_bf16 v4, s51, v139\n"
"v_dot2c_f32_bf16 v5, v139, v139\n"
"v_dot2c_f32_bf16 v4, s51, v140\n"
"v_dot2c_f32_bf16 v5, v140, v140\n"
"v_dot2c_f32_bf16 v4, s51, v141\n"
"v_dot2c_f32_bf16 v5, v141, v141\n"
"v_dot2c_f32_bf16 v4, s51, v142\n"
"v_dot2c_f32_bf16 v5, v142, v142\n"
"v_dot2c_f32_bf16 v4, s51, v143\n"
"v_dot2c_f32_bf16 v5, v143, v143\n"
"v_dot2c_f32_bf16 v4, s51, v144\n"
"v_dot2c_f32_bf16 v5, v144, v144\n"
"v_dot2c_f32_bf16 v4, s51, v145\n"
"v_dot2c_f32_bf16 v5, v145, v145\n"
"v_dot2c_f32_bf16 v4, s51, v146\n"
"v_dot2c_f32_bf16 v5, v146, v146\n"
"v_dot2c_f32_bf16 v4, s51, v147\n"
"v_dot2c_f32_bf16 v5, v147, v147\n"
"s_waitcnt vmcnt(5)\n"
"v_dot2c_f32_bf16 v6, s51, v148\n"
"v_dot2c_f32_bf16 v7, v148, v148\n"
"v_dot2c_f32_bf16 v6, s51, v149\n"
"v_dot2c_f32_bf16 v7, v149, v149\n"
"v_dot2c_f32_bf16 v6, s51, v150\n"
"v_dot2c_f32_bf16 v7, v150, v150\n"
"v_dot2c_f32_bf16 v6, s51, v151\n"
"v_dot2c_f32_bf16 v7, v151, v151\n"
"v_dot2c_f32_bf16 v6, s51, v152\n"
"v_dot2c_f32_bf16 v7, v152, v152\n"
"v_dot2c_f32_bf16 v6, s51, v153\n"
"v_dot2c_f32_bf16 v7, v153, v153\n"
"v_dot2c_f32_bf16 v6, s51, v154\n"
"v_dot2c_f32_bf16 v7, v154, v154\n"
"v_dot2c_f32_bf16 v6, s51, v155\n"
"v_dot2c_f32_bf16 v7, v155, v155\n"
"v_dot2c_f32_bf16 v6, s51, v156\n"
"v_dot2c_f32_bf16 v7, v156, v156\n"
"v_dot2c_f32_bf16 v6, s51, v157\n"
"v_dot2c_f32_bf16 v7, v157, v157\n"
"v_dot2c_f32_bf16 v6, s51, v158\n"
"v_dot2c_f32_bf16 v7, v158, v158\n"
"v_dot2c_f32_bf16 v6, s51, v159\n"
"v_dot2c_f32_bf16 v7, v159, v159\n"
"v_dot2c_f32_bf16 v6, s51, v160\n"
"v_dot2c_f32_bf16 v7, v160, v160\n"
"v_dot2c_f32_bf16 v6, s51, v161\n"
"v_dot2c_f32_bf16 v7, v161, v161\n"
"v_dot2c_f32_bf16 v6, s51, v162\n"
"v_dot2c_f32_bf16 v7, v162, v162\n"
"v_dot2c_f32_bf16 v6, s51, v163\n"
"v_dot2c_f32_bf16 v7, v163, v163\n"
"v_dot2c_f32_bf16 v6, s51, v164\n"
"v_dot2c_f32_bf16 v7, v164, v164\n"
"v_dot2c_f32_bf16 v6, s51, v165\n"
"v_dot2c_f32_bf16 v7, v165, v165\n"
"v_dot2c_f32_bf16 v6, s51, v166\n"
"v_dot2c_f32_bf16 v7, v166, v166\n"
"v_dot2c_f32_bf16 v6, s51, v167\n"
"v_dot2c_f32_bf16 v7, v167, v167\n"
"s_waitcnt vmcnt(0)\n"
"v_dot2c_f32_bf16 v8, s51, v168\n"
"v_dot2c_f32_bf16 v9, v168, v168\n"
"v_dot2c_f32_bf16 v8, s51, v169\n"
"v_dot2c_f32_bf16 v9, v169, v169\n"
"v_dot2c_f32_bf16 v8, s51, v170\n"
"v_dot2c_f32_bf16 v9, v170, v170\n"
"v_dot2c_f32_bf16 v8, s51, v171\n"
"v_dot2c_f32_bf16 v9, v171, v171\n"
"v_dot2c_f32_bf16 v8, s51, v172\n"
"v_dot2c_f32_bf16 v9, v172, v172\n"
"v_dot2c_f32_bf16 v8, s51, v173\n"
"v_dot2c_f32_bf16 v9, v173, v173\n"
"v_dot2c_f32_bf16 v8, s51, v174\n"
"v_dot2c_f32_bf16 v9, v174, v174\n"
"v_dot2c_f32_bf16 v8, s51, v175\n"
"v_dot2c_f32_bf16 v9, v175, v175\n"
"v_dot2c_f32_bf16 v8, s51, v176\n"
"v_dot2c_f32_bf16 v9, v176, v176\n"
"v_dot2c_f32_bf16 v8, s51, v177\n"
"v_dot2c_f32_bf16 v9, v177, v177\n"
"v_dot2c_f32_bf16 v8, s51, v178\n"
"v_dot2c_f32_bf16 v9, v178, v178\n"
"v_dot2c_f32_bf16 v8, s51, v179\n"
"v_dot2c_f32_bf16 v9, v179, v179\n"
"v_dot2c_f32_bf16 v8, s51, v180\n"
"v_dot2c_f32_bf16 v9, v180, v180\n"
"v_dot2c_f32_bf16 v8, s51, v181\n"
"v_dot2c_f32_bf16 v9, v181, v181\n"
"v_dot2c_f32_bf16 v8, s51, v182\n"
"v_dot2c_f32_bf16 v9, v182, v182\n"
"v_dot2c_f32_bf16 v8, s51, v183\n"
"v_dot2c_f32_bf16 v9, v183, v183\n"
"v_dot2c_f32_bf16 v8, s51, v184\n"
"v_dot2c_f32_bf16 v9, v184, v184\n"
"v_dot2c_f32_bf16 v8, s51, v185\n"
"v_dot2c_f32_bf16 v9, v185, v185\n"
"v_dot2c_f32_bf16 v8, s51, v186\n"
"v_dot2c_f32_bf16 v9, v186, v186\n"
"v_dot2c_f32_bf16 v8, s51, v187\n"
"v_dot2c_f32_bf16 v9, v187, v187\n"
"v_add_f32_dpp v4, v4, v4 quad_perm:[1,0,3,2] row_mask:0xf bank_mask:0xf\n"
"v_add_f32_dpp v5, v5, v5 quad_perm:[1,0,3,2] row_mask:0xf bank_mask:0xf\n"
"v_add_f32_dpp v6, v6, v6 quad_perm:[1,0,3,2] row_mask:0xf bank_mask:0xf\n"
"v_add_f32_dpp v7, v7, v7 quad_perm:[1,0,3,2] row_mask:0xf bank_mask:0xf\n"
"v_add_f32_dpp v8, v8, v8 quad_perm:[1,0,3,2] row_mask:0xf bank_mask:0xf\n"
"v_add_f32_dpp v9, v9, v9 quad_perm:[1,0,3,2] row_mask:0xf bank_mask:0xf\n"
"v_add_f32_dpp v4, v4, v4 quad_perm:[2,3,0,1] row_mask:0xf bank_mask:0xf\n"
"v_add_f32_dpp v5, v5, v5 quad_perm:[2,3,0,1] row_mask:0xf bank_mask:0xf\n"
"v_add_f32_dpp v6, v6, v6 quad_perm:[2,3,0,1] row_mask:0xf bank_mask:0xf\n"
"v_add_f32_dpp v7, v7, v7 quad_perm:[2,3,0,1] row_mask:0xf bank_mask:0xf\n"
"v_add_f32_dpp v8, v8, v8 quad_perm:[2,3,0,1] row_mask:0xf bank_mask:0xf\n"
"v_add_f32_dpp v9, v9, v9 quad_perm:[2,3,0,1] row_mask:0xf bank_mask:0xf\n"
"v_add_f32_dpp v4, v4, v4 row_half_mirror row_mask:0xf bank_mask:0xf\n"
"v_add_f32_dpp v5, v5, v5 row_half_mirror row_mask:0xf bank_mask:0xf\n"
"v_add_f32_dpp v6, v6, v6 row_half_mirror row_mask:0xf bank_mask:0xf\n"
"v_add_f32_dpp v7, v7, v7 row_half_mirror row_mask:0xf bank_mask:0xf\n"
"v_add_f32_dpp v8, v8, v8 row_half_mirror row_mask:0xf bank_mask:0xf\n"
"v_add_f32_dpp v9, v9, v9 row_half_mirror row_mask:0xf bank_mask:0xf\n"
"v_add_f32_dpp v4, v4, v4 row_mirror row_mask:0xf bank_mask:0xf\n"
"v_add_f32_dpp v5, v5, v5 row_mirror row_mask:0xf bank_mask:0xf\n"
"v_add_f32_dpp v6, v6, v6 row_mirror row_mask:0xf bank_mask:0xf\n"
"v_add_f32_dpp v7, v7, v7 row_mirror row_mask:0xf bank_mask:0xf\n"
"v_add_f32_dpp v8, v8, v8 row_mirror row_mask:0xf bank_mask:0xf\n"
"v_add_f32_dpp v9, v9, v9 row_mirror row_mask:0xf bank_mask:0xf\n"
"v_mul_f32 v4, 0x3acccccd, v4\n"
"v_mul_f32 v5, 0x3acccccd, v5\n"
"v_fma_f32 v5, -v4, v4, v5\n"
"v_max_f32 v5, 0, v5\n"
"v_cvt_pk_bf16_f32 v5, v5, v5\n"
"v_and_b32 v5, 0xffff0000, v5\n"
"v_add_f32 v5, 0x3727c5ac, v5\n"
"v_cvt_pk_bf16_f32 v5, v5, v5\n"
"v_and_b32 v5, 0xffff0000, v5\n"
"v_sqrt_f32 v5, v5\n"
"s_nop 0\n"
"v_cvt_pk_bf16_f32 v10, v4, v5\n"
"buffer_store_dwordx2 v[10:11], v2, s[32:35], s8 offen sc1\n"
"v_mul_f32 v6, 0x3acccccd, v6\n"
"v_mul_f32 v7, 0x3acccccd, v7\n"
"v_fma_f32 v7, -v6, v6, v7\n"
"v_max_f32 v7, 0, v7\n"
"v_cvt_pk_bf16_f32 v7, v7, v7\n"
"v_and_b32 v7, 0xffff0000, v7\n"
"v_add_f32 v7, 0x3727c5ac, v7\n"
"v_cvt_pk_bf16_f32 v7, v7, v7\n"
"v_and_b32 v7, 0xffff0000, v7\n"
"v_sqrt_f32 v7, v7\n"
"s_nop 0\n"
"v_cvt_pk_bf16_f32 v12, v6, v7\n"
"buffer_store_dwordx2 v[12:13], v2, s[32:35], s9 offen sc1\n"
"v_mul_f32 v8, 0x3acccccd, v8\n"
"v_mul_f32 v9, 0x3acccccd, v9\n"
"v_fma_f32 v9, -v8, v8, v9\n"
"v_max_f32 v9, 0, v9\n"
"v_cvt_pk_bf16_f32 v9, v9, v9\n"
"v_and_b32 v9, 0xffff0000, v9\n"
"v_add_f32 v9, 0x3727c5ac, v9\n"
"v_cvt_pk_bf16_f32 v9, v9, v9\n"
"v_and_b32 v9, 0xffff0000, v9\n"
"v_sqrt_f32 v9, v9\n"
"s_nop 0\n"
"v_cvt_pk_bf16_f32 v14, v8, v9\n"
"buffer_store_dwordx2 v[14:15], v2, s[32:35], s10 offen sc1\n"
".Lmg_q3e:\n"
"s_barrier\n"
".Lmg_hj:\n"
"s_nop 1\n"
"s_barrier\n"
"s_cmp_lt_u32 s96, 360\n"
"s_cbranch_scc1 .Lmg_hk\n"
"s_add_u32 s97, s96, 24\n"
"s_lshr_b32 s54, s97, 3\n"
"s_and_b32 s55, s97, 7\n"
"s_mul_i32 s53, s54, 40960\n"
"s_mul_i32 s55, s55, 160\n"
"s_add_u32 s53, s53, s55\n"
"s_add_u32 s6, s24, s53\n"
"s_addc_u32 s7, s25, 0\n"
"global_load_dwordx4 v[220:223], v51, s[6:7]\n"
"global_load_dwordx4 v[224:227], v52, s[6:7]\n"
"global_load_dwordx4 v[228:231], v53, s[6:7]\n"
"global_load_dwordx4 v[232:235], v54, s[6:7]\n"
"global_load_dwordx4 v[236:239], v55, s[6:7]\n"
"s_add_u32 s97, s96, 48\n"
"s_lshr_b32 s54, s97, 3\n"
"s_and_b32 s55, s97, 7\n"
"s_mul_i32 s53, s54, 40960\n"
"s_mul_i32 s55, s55, 160\n"
"s_add_u32 s53, s53, s55\n"
"s_add_u32 s6, s24, s53\n"
"s_addc_u32 s7, s25, 0\n"
"global_load_dwordx4 v[128:131], v51, s[6:7]\n"
"global_load_dwordx4 v[132:135], v52, s[6:7]\n"
"global_load_dwordx4 v[136:139], v53, s[6:7]\n"
"global_load_dwordx4 v[140:143], v54, s[6:7]\n"
"global_load_dwordx4 v[144:147], v55, s[6:7]\n"
".Lmg_hk:\n"
"s_lshr_b32 s54, s96, 3\n"
"s_and_b32 s55, s96, 7\n"
"s_mul_i32 s53, s54, 40960\n"
"s_mul_i32 s55, s55, 160\n"
"s_add_u32 s53, s53, s55\n"
"s_add_u32 s6, s24, s53\n"
"s_addc_u32 s7, s25, 0\n"
"global_load_dwordx4 v[200:203], v51, s[6:7]\n"
"global_load_dwordx4 v[204:207], v52, s[6:7]\n"
"global_load_dwordx4 v[208:211], v53, s[6:7]\n"
"global_load_dwordx4 v[212:215], v54, s[6:7]\n"
"global_load_dwordx4 v[216:219], v55, s[6:7]\n"
"s_lshr_b32 s47, s45, 2\n"
"s_lshr_b32 s54, s44, 1\n"
"s_lshl_b32 s54, s54, 3\n"
"s_add_u32 s47, s47, s54\n"
"s_and_b32 s48, s45, 3\n"
"s_and_b32 s54, s44, 1\n"
"s_lshl_b32 s54, s54, 2\n"
"s_add_u32 s48, s48, s54\n"
"s_mul_i32 s55, s48, 3\n"
"s_add_u32 s49, s55, s30\n"
"s_lshl_b32 s50, s48, 2\n"
"s_add_u32 s50, s50, s30\n"
"s_min_u32 s54, s30, 2\n"
"s_add_u32 s54, s54, s55\n"
"s_add_u32 s54, s54, 6\n"
"s_cmp_lt_u32 s48, 6\n"
"s_cselect_b32 s50, s50, s54\n"
"s_cselect_b32 s54, 7, 2\n"
"s_add_u32 s50, s50, 24\n"
"s_mov_b64 s[36:37], s[32:33]\n"
"s_mov_b32 s39, s35\n"
"s_cmp_eq_u32 s54, s30\n"
"s_cselect_b32 s38, 0, s34\n"
"s_lshl_b32 s62, s48, 2\n"
"s_add_u32 s62, s62, s30\n"
"s_mul_i32 s62, s62, 71680\n"
"s_add_u32 s62, s62, 2867200\n"
"s_mul_i32 s60, s49, 40960\n"
"s_add_u32 s60, s60, 655360\n"
"s_mul_i32 s61, s50, 40960\n"
"s_add_u32 s61, s61, 655360\n"
"s_add_u32 s4, s60, 0\n"
"s_add_u32 s5, s60, 4096\n"
"s_add_u32 s6, s60, 8192\n"
"s_add_u32 s7, s60, 12288\n"
"s_add_u32 s8, s60, 16384\n"
"s_add_u32 s9, s60, 20480\n"
"s_add_u32 s10, s60, 24576\n"
"s_add_u32 s11, s60, 28672\n"
"s_add_u32 s12, s60, 32768\n"
"s_add_u32 s13, s60, 36864\n"
"s_add_u32 s14, s61, 0\n"
"s_add_u32 s15, s61, 4096\n"
"s_add_u32 s16, s61, 8192\n"
"s_add_u32 s17, s61, 12288\n"
"s_add_u32 s18, s61, 16384\n"
"s_add_u32 s19, s61, 20480\n"
"s_add_u32 s20, s61, 24576\n"
"s_add_u32 s21, s61, 28672\n"
"s_add_u32 s22, s61, 32768\n"
"s_add_u32 s23, s61, 36864\n"
"s_mul_i32 s54, s47, 54\n"
"s_add_u32 s3, s54, s49\n"
"s_mul_i32 s3, s3, 4352\n"
"s_add_u32 s3, s3, 7782400\n"
"s_add_u32 s60, s54, s50\n"
"s_mul_i32 s60, s60, 4352\n"
"s_add_u32 s60, s60, 7782400\n"
"s_mov_b64 s[56:57], s[32:33]\n"
"s_mov_b32 s59, s35\n"
"s_cmp_eq_u32 s30, 3\n"
"s_cselect_b32 s58, 0, s34\n"
"s_cmp_lt_u32 s30, 2\n"
"s_cbranch_scc1 .Lmg_nd\n"
"s_cmp_ge_u32 s96, 360\n"
"s_waitcnt vmcnt(15)\n"
"s_cbranch_scc1 .Lmg_nd\n"
"s_waitcnt vmcnt(5)\n"
".Lmg_nd:\n"
"v_mov_b32 v66, s31\n"
"s_barrier\n"
"buffer_store_dword v66, v119, s[32:35], s52 offen sc1\n"
"s_waitcnt vmcnt(5)\n"
"ds_write_b128 v102, v[200:203] offset:0\n"
"s_waitcnt vmcnt(4)\n"
"ds_write_b128 v102, v[204:207] offset:1024\n"
"s_waitcnt vmcnt(3)\n"
"ds_write_b128 v102, v[208:211] offset:2048\n"
"s_waitcnt vmcnt(2)\n"
"ds_write_b128 v102, v[212:215] offset:3072\n"
"s_waitcnt vmcnt(1)\n"
"ds_write_b128 v102, v[216:219] offset:4096\n"
"ds_read_b64_tr_b16 v[4:5], v50 offset:0\n"
"ds_read_b64_tr_b16 v[6:7], v50 offset:2560\n"
"ds_read_b64_tr_b16 v[8:9], v50 offset:8\n"
"ds_read_b64_tr_b16 v[10:11], v50 offset:2568\n"
"ds_read_b64_tr_b16 v[12:13], v50 offset:64\n"
"ds_read_b64_tr_b16 v[14:15], v50 offset:2624\n"
"ds_read_b64_tr_b16 v[16:17], v50 offset:72\n"
"ds_read_b64_tr_b16 v[18:19], v50 offset:2632\n"
"ds_read_b64_tr_b16 v[20:21], v56 offset:128\n"
"ds_read_b64_tr_b16 v[22:23], v56 offset:2688\n"
"s_lshr_b32 s54, s96, 3\n"
"s_and_b32 s55, s96, 7\n"
"s_mul_i32 s98, s55, 56\n"
"s_mul_i32 s55, s54, 43\n"
"s_lshr_b32 s55, s55, 8\n"
"s_lshl_b32 s55, s55, 3\n"
"s_add_u32 s55, s55, s54\n"
"s_sub_u32 s63, s54, 18\n"
"s_cmp_ge_u32 s54, 24\n"
"s_cselect_b32 s55, s63, s55\n"
"s_sub_u32 s63, s54, 12\n"
"s_cmp_ge_u32 s54, 32\n"
"s_cselect_b32 s55, s63, s55\n"
"s_sub_u32 s63, s54, 5\n"
"s_cmp_ge_u32 s54, 40\n"
"s_cselect_b32 s55, s63, s55\n"
"s_add_u32 s63, s54, 2\n"
"s_cmp_ge_u32 s54, 47\n"
"s_cselect_b32 s55, s63, s55\n"
"s_add_u32 s98, s98, s55\n"
"s_mul_i32 s98, s98, 5120\n"
"s_add_u32 s98, s98, 2867200\n"
"s_waitcnt lgkmcnt(8)\n"
"buffer_store_dwordx4 v[4:7], v127, s[32:35], s98 offen offset:0 sc1\n"
"s_waitcnt lgkmcnt(6)\n"
"buffer_store_dwordx4 v[8:11], v127, s[32:35], s98 offen offset:1024 sc1\n"
"s_waitcnt lgkmcnt(4)\n"
"buffer_store_dwordx4 v[12:15], v127, s[32:35], s98 offen offset:2048 sc1\n"
"s_waitcnt lgkmcnt(2)\n"
"buffer_store_dwordx4 v[16:19], v127, s[32:35], s98 offen offset:3072 sc1\n"
"s_add_u32 s98, s98, 4096\n"
"s_waitcnt lgkmcnt(0)\n"
"buffer_store_dwordx4 v[20:23], v127, s[32:35], s98 offen offset:0 sc1\n"
"s_cmp_lt_u32 s96, 360\n"
"s_cbranch_scc1 .Lmg_t3e\n"
"ds_write_b128 v102, v[128:131] offset:5120\n"
"ds_write_b128 v102, v[132:135] offset:6144\n"
"ds_write_b128 v102, v[136:139] offset:7168\n"
"ds_write_b128 v102, v[140:143] offset:8192\n"
"ds_write_b128 v102, v[144:147] offset:9216\n"
"ds_read_b64_tr_b16 v[68:69], v50 offset:5120\n"
"ds_read_b64_tr_b16 v[70:71], v50 offset:7680\n"
"ds_read_b64_tr_b16 v[72:73], v50 offset:5128\n"
"ds_read_b64_tr_b16 v[74:75], v50 offset:7688\n"
"ds_read_b64_tr_b16 v[76:77], v50 offset:5184\n"
"ds_read_b64_tr_b16 v[78:79], v50 offset:7744\n"
"ds_read_b64_tr_b16 v[80:81], v50 offset:5192\n"
"ds_read_b64_tr_b16 v[82:83], v50 offset:7752\n"
"ds_read_b64_tr_b16 v[84:85], v56 offset:5248\n"
"ds_read_b64_tr_b16 v[86:87], v56 offset:7808\n"
"s_lshr_b32 s54, s97, 3\n"
"s_and_b32 s55, s97, 7\n"
"s_mul_i32 s99, s55, 56\n"
"s_mul_i32 s55, s54, 43\n"
"s_lshr_b32 s55, s55, 8\n"
"s_lshl_b32 s55, s55, 3\n"
"s_add_u32 s55, s55, s54\n"
"s_sub_u32 s63, s54, 18\n"
"s_cmp_ge_u32 s54, 24\n"
"s_cselect_b32 s55, s63, s55\n"
"s_sub_u32 s63, s54, 12\n"
"s_cmp_ge_u32 s54, 32\n"
"s_cselect_b32 s55, s63, s55\n"
"s_sub_u32 s63, s54, 5\n"
"s_cmp_ge_u32 s54, 40\n"
"s_cselect_b32 s55, s63, s55\n"
"s_add_u32 s63, s54, 2\n"
"s_cmp_ge_u32 s54, 47\n"
"s_cselect_b32 s55, s63, s55\n"
"s_add_u32 s99, s99, s55\n"
"s_mul_i32 s99, s99, 5120\n"
"s_add_u32 s99, s99, 2867200\n"
"s_waitcnt lgkmcnt(8)\n"
"buffer_store_dwordx4 v[68:71], v127, s[32:35], s99 offen offset:0 sc1\n"
"s_waitcnt lgkmcnt(6)\n"
"buffer_store_dwordx4 v[72:75], v127, s[32:35], s99 offen offset:1024 sc1\n"
"s_waitcnt lgkmcnt(4)\n"
"buffer_store_dwordx4 v[76:79], v127, s[32:35], s99 offen offset:2048 sc1\n"
"s_waitcnt lgkmcnt(2)\n"
"buffer_store_dwordx4 v[80:83], v127, s[32:35], s99 offen offset:3072 sc1\n"
"s_add_u32 s99, s99, 4096\n"
"s_waitcnt lgkmcnt(0)\n"
"buffer_store_dwordx4 v[84:87], v127, s[32:35], s99 offen offset:0 sc1\n"
"s_add_u32 s97, s96, 24\n"
"ds_write_b128 v102, v[220:223] offset:10240\n"
"ds_write_b128 v102, v[224:227] offset:11264\n"
"ds_write_b128 v102, v[228:231] offset:12288\n"
"ds_write_b128 v102, v[232:235] offset:13312\n"
"ds_write_b128 v102, v[236:239] offset:14336\n"
"ds_read_b64_tr_b16 v[160:161], v50 offset:10240\n"
"ds_read_b64_tr_b16 v[162:163], v50 offset:12800\n"
"ds_read_b64_tr_b16 v[164:165], v50 offset:10248\n"
"ds_read_b64_tr_b16 v[166:167], v50 offset:12808\n"
"ds_read_b64_tr_b16 v[168:169], v50 offset:10304\n"
"ds_read_b64_tr_b16 v[170:171], v50 offset:12864\n"
"ds_read_b64_tr_b16 v[172:173], v50 offset:10312\n"
"ds_read_b64_tr_b16 v[174:175], v50 offset:12872\n"
"ds_read_b64_tr_b16 v[176:177], v56 offset:10368\n"
"ds_read_b64_tr_b16 v[178:179], v56 offset:12928\n"
"s_lshr_b32 s54, s97, 3\n"
"s_and_b32 s55, s97, 7\n"
"s_mul_i32 s99, s55, 56\n"
"s_mul_i32 s55, s54, 43\n"
"s_lshr_b32 s55, s55, 8\n"
"s_lshl_b32 s55, s55, 3\n"
"s_add_u32 s55, s55, s54\n"
"s_sub_u32 s63, s54, 18\n"
"s_cmp_ge_u32 s54, 24\n"
"s_cselect_b32 s55, s63, s55\n"
"s_sub_u32 s63, s54, 12\n"
"s_cmp_ge_u32 s54, 32\n"
"s_cselect_b32 s55, s63, s55\n"
"s_sub_u32 s63, s54, 5\n"
"s_cmp_ge_u32 s54, 40\n"
"s_cselect_b32 s55, s63, s55\n"
"s_add_u32 s63, s54, 2\n"
"s_cmp_ge_u32 s54, 47\n"
"s_cselect_b32 s55, s63, s55\n"
"s_add_u32 s99, s99, s55\n"
"s_mul_i32 s99, s99, 5120\n"
"s_add_u32 s99, s99, 2867200\n"
"s_waitcnt lgkmcnt(8)\n"
"buffer_store_dwordx4 v[160:163], v127, s[32:35], s99 offen offset:0 sc1\n"
"s_waitcnt lgkmcnt(6)\n"
"buffer_store_dwordx4 v[164:167], v127, s[32:35], s99 offen offset:1024 sc1\n"
"s_waitcnt lgkmcnt(4)\n"
"buffer_store_dwordx4 v[168:171], v127, s[32:35], s99 offen offset:2048 sc1\n"
"s_waitcnt lgkmcnt(2)\n"
"buffer_store_dwordx4 v[172:175], v127, s[32:35], s99 offen offset:3072 sc1\n"
"s_add_u32 s99, s99, 4096\n"
"s_waitcnt lgkmcnt(0)\n"
"buffer_store_dwordx4 v[176:179], v127, s[32:35], s99 offen offset:0 sc1\n"
".Lmg_t3e:\n"
"s_add_u32 s52, s52, 32768\n"
"s_lshl_b32 s54, s44, 11\n"
"s_mul_i32 s55, s30, 384\n"
"s_add_u32 s54, s54, s55\n"
"s_add_u32 s55, s54, 163840\n"
"s_add_u32 s54, s54, 131072\n"
"s_sleep 14\n"
"buffer_load_dword v67, v103, s[32:35], s54 offen sc1\n"
"s_waitcnt vmcnt(0)\n"
"v_cmp_ne_u32 vcc, s31, v67\n"
"s_cbranch_vccnz .Lmg_slow5\n"
".Lmg_ret5:\n"
"v_mov_b32 v66, s31\n"
"s_barrier\n"
"buffer_store_dword v66, v119, s[32:35], s52 offen sc1\n"
"s_branch .Lmg_help2\n"
".Lmg_help2:\n"
"s_mov_b32 s96, s55\n"
"buffer_load_dwordx4 v[192:195], v127, s[36:39], s14 offen offset:0 sc1\n"
"buffer_load_dwordx4 v[196:199], v127, s[36:39], s14 offen offset:1024 sc1\n"
"buffer_load_dwordx4 v[200:203], v127, s[36:39], s14 offen offset:2048 sc1\n"
"buffer_load_dwordx4 v[204:207], v127, s[36:39], s14 offen offset:3072 sc1\n"
"buffer_load_dwordx4 v[208:211], v127, s[36:39], s15 offen offset:0 sc1\n"
"buffer_load_dwordx4 v[212:215], v127, s[36:39], s15 offen offset:1024 sc1\n"
"buffer_load_dwordx4 v[216:219], v127, s[36:39], s15 offen offset:2048 sc1\n"
"buffer_load_dwordx4 v[220:223], v127, s[36:39], s15 offen offset:3072 sc1\n"
"buffer_load_dwordx4 v[224:227], v127, s[36:39], s16 offen offset:0 sc1\n"
"buffer_load_dwordx4 v[228:231], v127, s[36:39], s16 offen offset:1024 sc1\n"
"buffer_load_dwordx4 v[232:235], v127, s[36:39], s16 offen offset:2048 sc1\n"
"buffer_load_dwordx4 v[236:239], v127, s[36:39], s16 offen offset:3072 sc1\n"
"buffer_load_dwordx4 v[240:243], v127, s[36:39], s17 offen offset:0 sc1\n"
"buffer_load_dwordx4 v[244:247], v127, s[36:39], s17 offen offset:1024 sc1\n"
"buffer_load_dwordx4 v[248:251], v127, s[36:39], s17 offen offset:2048 sc1\n"
"buffer_load_dwordx4 v[252:255], v127, s[36:39], s17 offen offset:3072 sc1\n"
"buffer_load_dwordx4 a[160:163], v127, s[36:39], s18 offen offset:0 sc1\n"
"buffer_load_dwordx4 a[164:167], v127, s[36:39], s18 offen offset:1024 sc1\n"
"buffer_load_dwordx4 a[168:171], v127, s[36:39], s18 offen offset:2048 sc1\n"
"buffer_load_dwordx4 a[172:175], v127, s[36:39], s18 offen offset:3072 sc1\n"
"buffer_load_dwordx4 a[176:179], v127, s[36:39], s19 offen offset:0 sc1\n"
"buffer_load_dwordx4 a[180:183], v127, s[36:39], s19 offen offset:1024 sc1\n"
"buffer_load_dwordx4 a[184:187], v127, s[36:39], s19 offen offset:2048 sc1\n"
"buffer_load_dwordx4 a[188:191], v127, s[36:39], s19 offen offset:3072 sc1\n"
"buffer_load_dwordx4 a[192:195], v127, s[36:39], s20 offen offset:0 sc1\n"
"buffer_load_dwordx4 a[196:199], v127, s[36:39], s20 offen offset:1024 sc1\n"
"buffer_load_dwordx4 a[200:203], v127, s[36:39], s20 offen offset:2048 sc1\n"
"buffer_load_dwordx4 a[204:207], v127, s[36:39], s20 offen offset:3072 sc1\n"
"buffer_load_dwordx4 a[208:211], v127, s[36:39], s21 offen offset:0 sc1\n"
"buffer_load_dwordx4 a[212:215], v127, s[36:39], s21 offen offset:1024 sc1\n"
"buffer_load_dwordx4 a[216:219], v127, s[36:39], s21 offen offset:2048 sc1\n"
"buffer_load_dwordx4 a[220:223], v127, s[36:39], s21 offen offset:3072 sc1\n"
"buffer_load_dwordx4 a[224:227], v127, s[36:39], s22 offen offset:0 sc1\n"
"buffer_load_dwordx4 a[228:231], v127, s[36:39], s22 offen offset:1024 sc1\n"
"buffer_load_dwordx4 a[232:235], v127, s[36:39], s22 offen offset:2048 sc1\n"
"buffer_load_dwordx4 a[236:239], v127, s[36:39], s22 offen offset:3072 sc1\n"
"buffer_load_dwordx4 a[240:243], v127, s[36:39], s23 offen offset:0 sc1\n"
"buffer_load_dwordx4 a[244:247], v127, s[36:39], s23 offen offset:1024 sc1\n"
"buffer_load_dwordx4 a[248:251], v127, s[36:39], s23 offen offset:2048 sc1\n"
"buffer_load_dwordx4 a[252:255], v127, s[36:39], s23 offen offset:3072 sc1\n"
"s_mov_b32 m0, 145408\n"
"v_mov_b32 v120, s51\n"
"v_mov_b32 v121, s51\n"
"v_mov_b32 v122, s51\n"
"v_mov_b32 v123, s51\n"
"s_cmp_eq_u32 s30, 3\n"
"s_cselect_b64 vcc, -1, 0\n"
"s_cbranch_vccz 1\n"
"s_waitcnt vmcnt(0)\n"
"s_cbranch_vccnz 2\n"
"buffer_load_dwordx4 a[0:3], v127, s[56:59], s4 offen offset:0 sc1\n"
"s_waitcnt vmcnt(39)\n"
"v_mfma_f32_16x16x32_bf16 v[72:75], v[192:195], v[120:123], 0\n"
"s_add_u32 s14, s62, 0\n"
"s_add_u32 s15, s62, 4096\n"
"v_mfma_f32_16x16x32_bf16 v[76:79], v[196:199], v[120:123], 0\n"
"s_add_u32 s16, s62, 8192\n"
"s_add_u32 s17, s62, 12288\n"
"s_cbranch_vccnz 2\n"
"buffer_load_dwordx4 a[4:7], v127, s[56:59], s4 offen offset:1024 sc1\n"
"s_waitcnt vmcnt(38)\n"
"v_mfma_f32_16x16x32_bf16 v[72:75], v[200:203], v[120:123], v[72:75]\n"
"s_add_u32 s18, s62, 16384\n"
"s_add_u32 s19, s62, 20480\n"
"v_mfma_f32_16x16x32_bf16 v[76:79], v[204:207], v[120:123], v[76:79]\n"
"s_add_u32 s20, s62, 24576\n"
"s_add_u32 s21, s62, 28672\n"
"s_cbranch_vccnz 2\n"
"buffer_load_dwordx4 a[8:11], v127, s[56:59], s4 offen offset:2048 sc1\n"
"s_waitcnt vmcnt(37)\n"
"v_mfma_f32_16x16x32_bf16 v[72:75], v[208:211], v[120:123], v[72:75]\n"
"s_mov_b64 s[40:41], s[32:33]\n"
"s_mov_b32 s43, s35\n"
"v_mfma_f32_16x16x32_bf16 v[76:79], v[212:215], v[120:123], v[76:79]\n"
"s_cmp_lt_u32 s30, 2\n"
"s_cselect_b32 s42, s34, 0\n"
"s_cbranch_vccnz 2\n"
"buffer_load_dwordx4 a[12:15], v127, s[56:59], s4 offen offset:3072 sc1\n"
"s_waitcnt vmcnt(36)\n"
"v_mfma_f32_16x16x32_bf16 v[72:75], v[216:219], v[120:123], v[72:75]\n"
"s_mul_i32 s54, s47, 54\n"
"s_mul_i32 s53, s30, 6\n"
"v_mfma_f32_16x16x32_bf16 v[76:79], v[220:223], v[120:123], v[76:79]\n"
"s_add_u32 s53, s53, s54\n"
"s_mul_i32 s53, s53, 4352\n"
"s_cbranch_vccnz 2\n"
"buffer_load_dwordx4 a[16:19], v127, s[56:59], s5 offen offset:0 sc1\n"
"s_waitcnt vmcnt(35)\n"
"v_mfma_f32_16x16x32_bf16 v[72:75], v[224:227], v[120:123], v[72:75]\n"
"s_add_u32 s53, s53, 7782400\n"
"s_add_u32 s64, s53, 0\n"
"v_mfma_f32_16x16x32_bf16 v[76:79], v[228:231], v[120:123], v[76:79]\n"
"s_add_u32 s65, s53, 4352\n"
"s_add_u32 s66, s53, 8704\n"
"s_cbranch_vccnz 2\n"
"buffer_load_dwordx4 a[20:23], v127, s[56:59], s5 offen offset:1024 sc1\n"
"s_waitcnt vmcnt(34)\n"
"v_mfma_f32_16x16x32_bf16 v[72:75], v[232:235], v[120:123], v[72:75]\n"
"s_add_u32 s67, s53, 13056\n"
"s_add_u32 s68, s53, 17408\n"
"v_mfma_f32_16x16x32_bf16 v[76:79], v[236:239], v[120:123], v[76:79]\n"
"s_add_u32 s69, s53, 21760\n"
"s_mul_i32 s53, s30, 7\n"
"s_cbranch_vccnz 2\n"
"buffer_load_dwordx4 a[24:27], v127, s[56:59], s5 offen offset:2048 sc1\n"
"s_waitcnt vmcnt(33)\n"
"v_mfma_f32_16x16x32_bf16 v[72:75], v[240:243], v[120:123], v[72:75]\n"
"s_min_u32 s55, s30, 1\n"
"s_add_u32 s53, s53, s55\n"
"v_mfma_f32_16x16x32_bf16 v[76:79], v[244:247], v[120:123], v[76:79]\n"
"s_add_u32 s53, s53, s54\n"
"s_add_u32 s53, s53, 24\n"
"s_cbranch_vccnz 2\n"
"buffer_load_dwordx4 a[28:31], v127, s[56:59], s5 offen offset:3072 sc1\n"
"s_waitcnt vmcnt(32)\n"
"v_mfma_f32_16x16x32_bf16 v[72:75], v[248:251], v[120:123], v[72:75]\n"
"s_mul_i32 s53, s53, 4352\n"
"s_add_u32 s53, s53, 7782400\n"
"v_mfma_f32_16x16x32_bf16 v[76:79], v[252:255], v[120:123], v[76:79]\n"
"s_add_u32 s70, s53, 0\n"
"s_add_u32 s71, s53, 4352\n"
"s_cbranch_vccnz 2\n"
"buffer_load_dwordx4 a[32:35], v127, s[56:59], s6 offen offset:0 sc1\n"
"s_waitcnt vmcnt(31)\n"
"v_mfma_f32_16x16x32_bf16 v[72:75], a[160:163], v[120:123], v[72:75]\n"
"s_add_u32 s72, s53, 8704\n"
"s_add_u32 s73, s53, 13056\n"
"v_mfma_f32_16x16x32_bf16 v[76:79], a[164:167], v[120:123], v[76:79]\n"
"s_add_u32 s74, s53, 17408\n"
"s_add_u32 s75, s53, 21760\n"
"s_cbranch_vccnz 2\n"
"buffer_load_dwordx4 a[36:39], v127, s[56:59], s6 offen offset:1024 sc1\n"
"s_waitcnt vmcnt(30)\n"
"v_mfma_f32_16x16x32_bf16 v[72:75], a[168:171], v[120:123], v[72:75]\n"
"s_add_u32 s76, s53, 26112\n"
"s_add_u32 s77, s53, 30464\n"
"v_mfma_f32_16x16x32_bf16 v[76:79], a[172:175], v[120:123], v[76:79]\n"
"v_and_b32 v113, 63, v126\n"
"v_min_u32 v113, 39, v113\n"
"s_cbranch_vccnz 2\n"
"buffer_load_dwordx4 a[40:43], v127, s[56:59], s6 offen offset:2048 sc1\n"
"s_waitcnt vmcnt(29)\n"
"v_mfma_f32_16x16x32_bf16 v[72:75], a[176:179], v[120:123], v[72:75]\n"
"v_lshrrev_b32 v114, 3, v113\n"
"v_and_b32 v113, 7, v113\n"
"v_mfma_f32_16x16x32_bf16 v[76:79], a[180:183], v[120:123], v[76:79]\n"
"v_mul_u32_u24 v114, 512, v114\n"
"v_lshl_add_u32 v113, v113, 4, v114\n"
"s_cbranch_vccnz 2\n"
"buffer_load_dwordx4 a[44:47], v127, s[56:59], s6 offen offset:3072 sc1\n"
"s_waitcnt vmcnt(28)\n"
"v_mfma_f32_16x16x32_bf16 v[72:75], a[184:187], v[120:123], v[72:75]\n"
"s_mul_i32 s98, s44, 4096\n"
"s_mul_i32 s55, s30, 128\n"
"v_mfma_f32_16x16x32_bf16 v[76:79], a[188:191], v[120:123], v[76:79]\n"
"s_add_u32 s98, s98, s55\n"
"s_add_u32 s98, s98, 16777216\n"
"s_cbranch_vccnz 2\n"
"buffer_load_dwordx4 a[48:51], v127, s[56:59], s7 offen offset:0 sc1\n"
"s_waitcnt vmcnt(27)\n"
"v_mfma_f32_16x16x32_bf16 v[72:75], a[192:195], v[120:123], v[72:75]\n"
"v_bfe_u32 v32, v126, 4, 2\n"
"v_bfe_u32 v33, v126, 2, 2\n"
"v_mfma_f32_16x16x32_bf16 v[76:79], a[196:199], v[120:123], v[76:79]\n"
"v_and_b32 v34, 3, v126\n"
"v_lshl_add_u32 v35, v32, 2, v33\n"
"s_cbranch_vccnz 2\n"
"buffer_load_dwordx4 a[52:55], v127, s[56:59], s7 offen offset:1024 sc1\n"
"s_waitcnt vmcnt(26)\n"
"v_mfma_f32_16x16x32_bf16 v[72:75], a[200:203], v[120:123], v[72:75]\n"
"v_bfe_u32 v36, v35, 1, 2\n"
"v_lshlrev_b32 v35, 7, v35\n"
"v_mfma_f32_16x16x32_bf16 v[76:79], a[204:207], v[120:123], v[76:79]\n"
"v_lshl_add_u32 v35, v34, 3, v35\n"
"v_lshl_add_u32 v117, v36, 5, v35\n"
"s_cbranch_vccnz 2\n"
"buffer_load_dwordx4 a[56:59], v127, s[56:59], s7 offen offset:2048 sc1\n"
"s_waitcnt vmcnt(25)\n"
"v_mfma_f32_16x16x32_bf16 v[72:75], a[208:211], v[120:123], v[72:75]\n"
"v_xor_b32 v118, 32, v117\n"
"v_xor_b32 v116, 64, v117\n"
"v_mfma_f32_16x16x32_bf16 v[76:79], a[212:215], v[120:123], v[76:79]\n"
"v_xor_b32 v124, 96, v117\n"
"v_and_b32 v125, 15, v126\n"
"s_cbranch_vccnz 2\n"
"buffer_load_dwordx4 a[60:63], v127, s[56:59], s7 offen offset:3072 sc1\n"
"s_waitcnt vmcnt(24)\n"
"v_mfma_f32_16x16x32_bf16 v[72:75], a[216:219], v[120:123], v[72:75]\n"
"v_lshlrev_b32 v125, 2, v125\n"
"v_add_u32 v125, 81920, v125\n"
"v_mfma_f32_16x16x32_bf16 v[76:79], a[220:223], v[120:123], v[76:79]\n"
"s_mul_i32 s99, s47, 81920\n"
"s_lshl_b32 s55, s30, 12\n"
"s_cbranch_vccnz 2\n"
"buffer_load_dwordx4 a[64:67], v127, s[56:59], s8 offen offset:0 sc1\n"
"s_waitcnt vmcnt(23)\n"
"v_mfma_f32_16x16x32_bf16 v[72:75], a[224:227], v[120:123], v[72:75]\n"
"s_add_u32 s99, s99, 5160960\n"
"s_add_u32 s99, s99, s55\n"
"v_mfma_f32_16x16x32_bf16 v[76:79], a[228:231], v[120:123], v[76:79]\n"
"s_mov_b32 m0, s55\n"
"s_cbranch_vccnz 2\n"
"buffer_load_dwordx4 a[68:71], v127, s[56:59], s8 offen offset:1024 sc1\n"
"s_waitcnt vmcnt(22)\n"
"v_mfma_f32_16x16x32_bf16 v[72:75], a[232:235], v[120:123], v[72:75]\n"
"v_mfma_f32_16x16x32_bf16 v[76:79], a[236:239], v[120:123], v[76:79]\n"
"s_cbranch_vccnz 2\n"
"buffer_load_dwordx4 a[72:75], v127, s[56:59], s8 offen offset:2048 sc1\n"
"s_waitcnt vmcnt(21)\n"
"v_mfma_f32_16x16x32_bf16 v[72:75], a[240:243], v[120:123], v[72:75]\n"
"v_mfma_f32_16x16x32_bf16 v[76:79], a[244:247], v[120:123], v[76:79]\n"
"s_cbranch_vccnz 2\n"
"buffer_load_dwordx4 a[76:79], v127, s[56:59], s8 offen offset:3072 sc1\n"
"s_waitcnt vmcnt(20)\n"
"v_mfma_f32_16x16x32_bf16 v[72:75], a[248:251], v[120:123], v[72:75]\n"
"v_mfma_f32_16x16x32_bf16 v[76:79], a[252:255], v[120:123], v[76:79]\n"
"s_cbranch_vccnz 2\n"
"buffer_load_dwordx4 a[80:83], v127, s[56:59], s9 offen offset:0 sc1\n"
"s_waitcnt vmcnt(19)\n"
"v_mfma_f32_16x16x32_bf16 v[64:67], a[0:3], v[120:123], 0\n"
"v_mfma_f32_16x16x32_bf16 v[68:71], a[4:7], v[120:123], 0\n"
"s_cbranch_vccnz 2\n"
"buffer_load_dwordx4 a[84:87], v127, s[56:59], s9 offen offset:1024 sc1\n"
"s_waitcnt vmcnt(18)\n"
"v_mfma_f32_16x16x32_bf16 v[64:67], a[8:11], v[120:123], v[64:67]\n"
"v_mfma_f32_16x16x32_bf16 v[68:71], a[12:15], v[120:123], v[68:71]\n"
"s_cbranch_vccnz 2\n"
"buffer_load_dwordx4 a[88:91], v127, s[56:59], s9 offen offset:2048 sc1\n"
"s_waitcnt vmcnt(17)\n"
"v_mfma_f32_16x16x32_bf16 v[64:67], a[16:19], v[120:123], v[64:67]\n"
"v_mfma_f32_16x16x32_bf16 v[68:71], a[20:23], v[120:123], v[68:71]\n"
"s_cbranch_vccnz 2\n"
"buffer_load_dwordx4 a[92:95], v127, s[56:59], s9 offen offset:3072 sc1\n"
"s_waitcnt vmcnt(17)\n"
"v_mfma_f32_16x16x32_bf16 v[64:67], a[24:27], v[120:123], v[64:67]\n"
"s_cbranch_vccnz 2\n"
"buffer_load_dwordx4 a[96:99], v127, s[56:59], s10 offen offset:0 sc1\n"
"s_waitcnt vmcnt(17)\n"
"v_mfma_f32_16x16x32_bf16 v[68:71], a[28:31], v[120:123], v[68:71]\n"
"s_cbranch_vccnz 2\n"
"buffer_load_dwordx4 a[100:103], v127, s[56:59], s10 offen offset:1024 sc1\n"
"s_waitcnt vmcnt(17)\n"
"v_mfma_f32_16x16x32_bf16 v[64:67], a[32:35], v[120:123], v[64:67]\n"
"s_cbranch_vccnz 2\n"
"buffer_load_dwordx4 a[104:107], v127, s[56:59], s10 offen offset:2048 sc1\n"
"s_waitcnt vmcnt(17)\n"
"v_mfma_f32_16x16x32_bf16 v[68:71], a[36:39], v[120:123], v[68:71]\n"
"s_cbranch_vccnz 2\n"
"buffer_load_dwordx4 a[108:111], v127, s[56:59], s10 offen offset:3072 sc1\n"
"s_waitcnt vmcnt(17)\n"
"v_mfma_f32_16x16x32_bf16 v[64:67], a[40:43], v[120:123], v[64:67]\n"
"s_cbranch_vccnz 2\n"
"buffer_load_dwordx4 a[112:115], v127, s[56:59], s11 offen offset:0 sc1\n"
"s_waitcnt vmcnt(17)\n"
"v_mfma_f32_16x16x32_bf16 v[68:71], a[44:47], v[120:123], v[68:71]\n"
"s_cbranch_vccnz 2\n"
"buffer_load_dwordx4 a[116:119], v127, s[56:59], s11 offen offset:1024 sc1\n"
"s_waitcnt vmcnt(17)\n"
"v_mfma_f32_16x16x32_bf16 v[64:67], a[48:51], v[120:123], v[64:67]\n"
"s_cbranch_vccnz 2\n"
"buffer_load_dwordx4 a[120:123], v127, s[56:59], s11 offen offset:2048 sc1\n"
"s_waitcnt vmcnt(17)\n"
"v_mfma_f32_16x16x32_bf16 v[68:71], a[52:55], v[120:123], v[68:71]\n"
"s_cbranch_vccnz 2\n"
"buffer_load_dwordx4 a[124:127], v127, s[56:59], s11 offen offset:3072 sc1\n"
"s_waitcnt vmcnt(17)\n"
"v_mfma_f32_16x16x32_bf16 v[64:67], a[56:59], v[120:123], v[64:67]\n"
"s_cbranch_vccnz 2\n"
"buffer_load_dwordx4 a[128:131], v127, s[56:59], s12 offen offset:0 sc1\n"
"s_waitcnt vmcnt(17)\n"
"v_mfma_f32_16x16x32_bf16 v[68:71], a[60:63], v[120:123], v[68:71]\n"
"s_cbranch_vccnz 2\n"
"buffer_load_dwordx4 a[132:135], v127, s[56:59], s12 offen offset:1024 sc1\n"
"s_waitcnt vmcnt(17)\n"
"v_mfma_f32_16x16x32_bf16 v[64:67], a[64:67], v[120:123], v[64:67]\n"
"s_cbranch_vccnz 2\n"
"buffer_load_dwordx4 a[136:139], v127, s[56:59], s12 offen offset:2048 sc1\n"
"s_waitcnt vmcnt(17)\n"
"v_mfma_f32_16x16x32_bf16 v[68:71], a[68:71], v[120:123], v[68:71]\n"
"s_cbranch_vccnz 2\n"
"buffer_load_dwordx4 a[140:143], v127, s[56:59], s12 offen offset:3072 sc1\n"
"s_waitcnt vmcnt(17)\n"
"v_mfma_f32_16x16x32_bf16 v[64:67], a[72:75], v[120:123], v[64:67]\n"
"s_cbranch_vccnz 2\n"
"buffer_load_dwordx4 a[144:147], v127, s[56:59], s13 offen offset:0 sc1\n"
"s_waitcnt vmcnt(17)\n"
"v_mfma_f32_16x16x32_bf16 v[68:71], a[76:79], v[120:123], v[68:71]\n"
"s_cbranch_vccnz 2\n"
"buffer_load_dwordx4 a[148:151], v127, s[56:59], s13 offen offset:1024 sc1\n"
"s_waitcnt vmcnt(17)\n"
"v_mfma_f32_16x16x32_bf16 v[64:67], a[80:83], v[120:123], v[64:67]\n"
"s_cbranch_vccnz 2\n"
"buffer_load_dwordx4 a[152:155], v127, s[56:59], s13 offen offset:2048 sc1\n"
"s_waitcnt vmcnt(17)\n"
"v_mfma_f32_16x16x32_bf16 v[68:71], a[84:87], v[120:123], v[68:71]\n"
"s_cbranch_vccnz 2\n"
"buffer_load_dwordx4 a[156:159], v127, s[56:59], s13 offen offset:3072 sc1\n"
"s_waitcnt vmcnt(17)\n"
"v_mfma_f32_16x16x32_bf16 v[64:67], a[88:91], v[120:123], v[64:67]\n"
"buffer_load_dword v111, v103, s[32:35], s96 offen sc1\n"
"s_waitcnt vmcnt(17)\n"
"v_mfma_f32_16x16x32_bf16 v[68:71], a[92:95], v[120:123], v[68:71]\n"
"s_waitcnt vmcnt(16)\n"
"v_mfma_f32_16x16x32_bf16 v[64:67], a[96:99], v[120:123], v[64:67]\n"
"s_waitcnt vmcnt(15)\n"
"v_mfma_f32_16x16x32_bf16 v[68:71], a[100:103], v[120:123], v[68:71]\n"
"s_waitcnt vmcnt(0)\n"
"v_cmp_ne_u32 vcc, s31, v111\n"
"s_cbranch_vccnz .Lmg_slow7\n"
".Lmg_ret7:\n"
"s_barrier\n"
"v_and_b32 v37, 7, v126\n"
"v_lshlrev_b32 v37, 7, v37\n"
"s_lshl_b32 s97, s44, 10\n"
"s_add_u32 s97, s97, 327680\n"
"buffer_load_dwordx2 v[38:39], v37, s[32:35], s97 offen sc1\n"
"buffer_load_dwordx4 v[128:131], v127, s[32:35], s14 offen offset:0 sc1\n"
"buffer_load_dwordx4 v[132:135], v127, s[32:35], s14 offen offset:1024 sc1\n"
"buffer_load_dwordx4 v[136:139], v127, s[32:35], s14 offen offset:2048 sc1\n"
"buffer_load_dwordx4 v[140:143], v127, s[32:35], s14 offen offset:3072 sc1\n"
"buffer_load_dwordx4 v[144:147], v127, s[32:35], s15 offen offset:0 sc1\n"
"buffer_load_dwordx4 v[148:151], v127, s[32:35], s15 offen offset:1024 sc1\n"
"buffer_load_dwordx4 v[152:155], v127, s[32:35], s15 offen offset:2048 sc1\n"
"buffer_load_dwordx4 v[156:159], v127, s[32:35], s15 offen offset:3072 sc1\n"
"buffer_load_dwordx4 v[160:163], v127, s[32:35], s16 offen offset:0 sc1\n"
"buffer_load_dwordx4 v[164:167], v127, s[32:35], s16 offen offset:1024 sc1\n"
"s_waitcnt vmcnt(10)\n"
"s_nop 7\n"
"s_branch .Lmg_join\n"
".Lmg_gslow:\n"
"s_mov_b32 s63, 0x80000\n"
".Lmg_gspin:\n"
"s_mov_b64 exec, vcc\n"
"s_sleep 2\n"
"buffer_load_dwordx4 v[208:211], v114, s[32:35], s98 offen sc1\n"
"buffer_load_dwordx4 v[212:215], v116, s[32:35], s98 offen sc1\n"
"buffer_load_dwordx4 v[216:219], v114, s[32:35], s99 offen sc1\n"
"buffer_load_dwordx4 v[220:223], v116, s[32:35], s99 offen sc1\n"
"s_waitcnt vmcnt(0)\n"
"v_xor_b32 v124, s31, v209\n"
"v_bitop3_b32 v124, v124, v211, s31 bitop3:0xf6\n"
"v_bitop3_b32 v124, v124, v213, s31 bitop3:0xf6\n"
"v_bitop3_b32 v124, v124, v215, s31 bitop3:0xf6\n"
"v_bitop3_b32 v124, v124, v217, s31 bitop3:0xf6\n"
"v_bitop3_b32 v124, v124, v219, s31 bitop3:0xf6\n"
"v_bitop3_b32 v124, v124, v221, s31 bitop3:0xf6\n"
"v_bitop3_b32 v124, v124, v223, s31 bitop3:0xf6\n"
"v_cmp_ne_u32 vcc, 0, v124\n"
"s_sub_u32 s63, s63, 1\n"
"s_cmp_eq_u32 s63, 0\n"
"s_cbranch_scc1 .Lmg_gout\n"
"s_cbranch_vccnz .Lmg_gspin\n"
".Lmg_gout:\n"
"s_mov_b64 exec, -1\n"
"s_branch .Lmg_gret\n"
".Lmg_slow1:\n"
"s_mov_b32 s63, 0x80000\n"
".Lmg_spin1:\n"
"s_mov_b64 exec, vcc\n"
"s_sleep 2\n"
"buffer_load_dword v124, v127, s[32:35], s54 offen sc1\n"
"s_waitcnt vmcnt(0)\n"
"v_cmp_ne_u32 vcc, s31, v124\n"
"s_sub_u32 s63, s63, 1\n"
"s_cmp_eq_u32 s63, 0\n"
"s_cbranch_scc1 .Lmg_out1\n"
"s_cbranch_vccnz .Lmg_spin1\n"
".Lmg_out1:\n"
"s_mov_b64 exec, -1\n"
"s_branch .Lmg_ret1\n"
".Lmg_slow2:\n"
"s_mov_b32 s63, 0x80000\n"
".Lmg_spin2:\n"
"s_mov_b64 exec, vcc\n"
"s_sleep 2\n"
"buffer_load_dword v125, v116, s[32:35], s54 offen sc1\n"
"s_waitcnt vmcnt(0)\n"
"v_cmp_ne_u32 vcc, s31, v125\n"
"s_sub_u32 s63, s63, 1\n"
"s_cmp_eq_u32 s63, 0\n"
"s_cbranch_scc1 .Lmg_out2\n"
"s_cbranch_vccnz .Lmg_spin2\n"
".Lmg_out2:\n"
"s_mov_b64 exec, -1\n"
"s_branch .Lmg_ret2\n"
".Lmg_slow3:\n"
"s_mov_b32 s63, 0x80000\n"
".Lmg_spin3:\n"
"s_mov_b64 exec, vcc\n"
"s_sleep 2\n"
"buffer_load_dword v124, v116, s[32:35], s55 offen sc1\n"
"s_waitcnt vmcnt(0)\n"
"v_cmp_ne_u32 vcc, s31, v124\n"
"s_sub_u32 s63, s63, 1\n"
"s_cmp_eq_u32 s63, 0\n"
"s_cbranch_scc1 .Lmg_out3\n"
"s_cbranch_vccnz .Lmg_spin3\n"
".Lmg_out3:\n"
"s_mov_b64 exec, -1\n"
"s_branch .Lmg_ret3\n"
".Lmg_slow5:\n"
"s_mov_b32 s63, 0x80000\n"
".Lmg_spin5:\n"
"s_mov_b64 exec, vcc\n"
"s_sleep 2\n"
"buffer_load_dword v67, v103, s[32:35], s54 offen sc1\n"
"s_waitcnt vmcnt(0)\n"
"v_cmp_ne_u32 vcc, s31, v67\n"
"s_sub_u32 s63, s63, 1\n"
"s_cmp_eq_u32 s63, 0\n"
"s_cbranch_scc1 .Lmg_out5\n"
"s_cbranch_vccnz .Lmg_spin5\n"
".Lmg_out5:\n"
"s_mov_b64 exec, -1\n"
"s_branch .Lmg_ret5\n"
".Lmg_slow7:\n"
"s_mov_b32 s63, 0x80000\n"
".Lmg_spin7:\n"
"s_mov_b64 exec, vcc\n"
"s_sleep 2\n"
"buffer_load_dword v111, v103, s[32:35], s96 offen sc1\n"
"s_waitcnt vmcnt(0)\n"
"v_cmp_ne_u32 vcc, s31, v111\n"
"s_sub_u32 s63, s63, 1\n"
"s_cmp_eq_u32 s63, 0\n"
"s_cbranch_scc1 .Lmg_out7\n"
"s_cbranch_vccnz .Lmg_spin7\n"
".Lmg_out7:\n"
"s_mov_b64 exec, -1\n"
"s_branch .Lmg_ret7\n"
".Lmg_slow8:\n"
"s_mov_b32 s63, 0x40000\n"
".Lmg_spin8:\n"
"s_mov_b64 exec, vcc\n"
"s_sleep 8\n"
"buffer_load_dwordx2 v[38:39], v37, s[32:35], s97 offen sc1\n"
"s_waitcnt vmcnt(0)\n"
"v_cmp_ne_u32 vcc, s31, v39\n"
"s_sub_u32 s63, s63, 1\n"
"s_cmp_eq_u32 s63, 0\n"
"s_cbranch_scc1 .Lmg_out8\n"
"s_cbranch_vccnz .Lmg_spin8\n"
".Lmg_out8:\n"
"s_mov_b64 exec, -1\n"
"s_branch .Lmg_ret8\n"
".Lmg_slow9:\n"
"s_mov_b32 s63, 0x80000\n"
".Lmg_spin9:\n"
"s_mov_b64 exec, vcc\n"
"s_sleep 2\n"
"buffer_load_dword v115, v113, s[32:35], s52 offen sc1\n"
"s_waitcnt vmcnt(0)\n"
"v_cmp_ne_u32 vcc, s31, v115\n"
"s_sub_u32 s63, s63, 1\n"
"s_cmp_eq_u32 s63, 0\n"
"s_cbranch_scc1 .Lmg_out9\n"
"s_cbranch_vccnz .Lmg_spin9\n"
".Lmg_out9:\n"
"s_mov_b64 exec, -1\n"
"s_branch .Lmg_ret9\n"
".Lmg_hs11:\n"
"s_mov_b32 s63, 0x40000\n"
".Lmg_hp11:\n"
"s_sleep 3\n"
"buffer_load_dword v100, v119, s[32:35], s64 offen sc1\n"
"buffer_load_dword v101, v119, s[32:35], s65 offen sc1\n"
"buffer_load_dword v108, v119, s[32:35], s66 offen sc1\n"
"buffer_load_dword v109, v119, s[32:35], s67 offen sc1\n"
"buffer_load_dword v110, v119, s[32:35], s68 offen sc1\n"
"buffer_load_dword v111, v119, s[32:35], s69 offen sc1\n"
"buffer_load_dwordx4 v[192:195], v112, s[32:35], s64 offen offset:0 sc1\n"
"buffer_load_dwordx4 v[196:199], v113, s[32:35], s64 offen offset:0 sc1\n"
"buffer_load_dwordx4 v[200:203], v114, s[32:35], s64 offen offset:0 sc1\n"
"buffer_load_dwordx4 v[204:207], v115, s[32:35], s64 offen offset:0 sc1\n"
"buffer_load_dwordx4 v[208:211], v112, s[32:35], s65 offen offset:0 sc1\n"
"buffer_load_dwordx4 v[212:215], v113, s[32:35], s65 offen offset:0 sc1\n"
"buffer_load_dwordx4 v[216:219], v114, s[32:35], s65 offen offset:0 sc1\n"
"buffer_load_dwordx4 v[220:223], v115, s[32:35], s65 offen offset:0 sc1\n"
"buffer_load_dwordx4 v[224:227], v112, s[32:35], s66 offen offset:0 sc1\n"
"buffer_load_dwordx4 v[228:231], v113, s[32:35], s66 offen offset:0 sc1\n"
"buffer_load_dwordx4 v[232:235], v114, s[32:35], s66 offen offset:0 sc1\n"
"buffer_load_dwordx4 v[236:239], v115, s[32:35], s66 offen offset:0 sc1\n"
"buffer_load_dwordx4 v[240:243], v112, s[32:35], s67 offen offset:0 sc1\n"
"buffer_load_dwordx4 v[244:247], v113, s[32:35], s67 offen offset:0 sc1\n"
"buffer_load_dwordx4 v[248:251], v114, s[32:35], s67 offen offset:0 sc1\n"
"buffer_load_dwordx4 v[252:255], v115, s[32:35], s67 offen offset:0 sc1\n"
"s_waitcnt vmcnt(0)\n"
"v_add3_u32 v116, v192, v193, s61\n"
"v_add3_u32 v116, v194, v195, v116\n"
"v_add3_u32 v116, v196, v197, v116\n"
"v_add3_u32 v116, v198, v199, v116\n"
"v_add3_u32 v116, v200, v201, v116\n"
"v_add3_u32 v116, v202, v203, v116\n"
"v_add3_u32 v116, v204, v205, v116\n"
"v_add3_u32 v116, v206, v207, v116\n"
"v_cmp_ne_u32 vcc, v116, v100\n"
"s_cbranch_vccz .Lmg_hr11\n"
"s_sub_u32 s63, s63, 1\n"
"s_cmp_lg_u32 s63, 0\n"
"s_cbranch_scc1 .Lmg_hp11\n"
"s_branch .Lmg_hr11\n"
".Lmg_hs12:\n"
"s_mov_b32 s63, 0x40000\n"
".Lmg_hp12:\n"
"s_sleep 3\n"
"buffer_load_dword v101, v119, s[32:35], s65 offen sc1\n"
"buffer_load_dword v108, v119, s[32:35], s66 offen sc1\n"
"buffer_load_dword v109, v119, s[32:35], s67 offen sc1\n"
"buffer_load_dword v110, v119, s[32:35], s68 offen sc1\n"
"buffer_load_dword v111, v119, s[32:35], s69 offen sc1\n"
"buffer_load_dwordx4 v[208:211], v112, s[32:35], s65 offen offset:0 sc1\n"
"buffer_load_dwordx4 v[212:215], v113, s[32:35], s65 offen offset:0 sc1\n"
"buffer_load_dwordx4 v[216:219], v114, s[32:35], s65 offen offset:0 sc1\n"
"buffer_load_dwordx4 v[220:223], v115, s[32:35], s65 offen offset:0 sc1\n"
"buffer_load_dwordx4 v[224:227], v112, s[32:35], s66 offen offset:0 sc1\n"
"buffer_load_dwordx4 v[228:231], v113, s[32:35], s66 offen offset:0 sc1\n"
"buffer_load_dwordx4 v[232:235], v114, s[32:35], s66 offen offset:0 sc1\n"
"buffer_load_dwordx4 v[236:239], v115, s[32:35], s66 offen offset:0 sc1\n"
"buffer_load_dwordx4 v[240:243], v112, s[32:35], s67 offen offset:0 sc1\n"
"buffer_load_dwordx4 v[244:247], v113, s[32:35], s67 offen offset:0 sc1\n"
"buffer_load_dwordx4 v[248:251], v114, s[32:35], s67 offen offset:0 sc1\n"
"buffer_load_dwordx4 v[252:255], v115, s[32:35], s67 offen offset:0 sc1\n"
"buffer_load_dwordx4 v[40:43], v112, s[32:35], s68 offen offset:0 sc1\n"
"buffer_load_dwordx4 v[44:47], v113, s[32:35], s68 offen offset:0 sc1\n"
"buffer_load_dwordx4 v[48:51], v114, s[32:35], s68 offen offset:0 sc1\n"
"buffer_load_dwordx4 v[52:55], v115, s[32:35], s68 offen offset:0 sc1\n"
"s_waitcnt vmcnt(0)\n"
"v_add3_u32 v116, v208, v209, s61\n"
"v_add3_u32 v116, v210, v211, v116\n"
"v_add3_u32 v116, v212, v213, v116\n"
"v_add3_u32 v116, v214, v215, v116\n"
"v_add3_u32 v116, v216, v217, v116\n"
"v_add3_u32 v116, v218, v219, v116\n"
"v_add3_u32 v116, v220, v221, v116\n"
"v_add3_u32 v116, v222, v223, v116\n"
"v_cmp_ne_u32 vcc, v116, v101\n"
"s_cbranch_vccz .Lmg_hr12\n"
"s_sub_u32 s63, s63, 1\n"
"s_cmp_lg_u32 s63, 0\n"
"s_cbranch_scc1 .Lmg_hp12\n"
"s_branch .Lmg_hr12\n"
".Lmg_hs13:\n"
"s_mov_b32 s63, 0x40000\n"
".Lmg_hp13:\n"
"s_sleep 3\n"
"buffer_load_dword v108, v119, s[32:35], s66 offen sc1\n"
"buffer_load_dword v109, v119, s[32:35], s67 offen sc1\n"
"buffer_load_dword v110, v119, s[32:35], s68 offen sc1\n"
"buffer_load_dword v111, v119, s[32:35], s69 offen sc1\n"
"buffer_load_dwordx4 v[224:227], v112, s[32:35], s66 offen offset:0 sc1\n"
"buffer_load_dwordx4 v[228:231], v113, s[32:35], s66 offen offset:0 sc1\n"
"buffer_load_dwordx4 v[232:235], v114, s[32:35], s66 offen offset:0 sc1\n"
"buffer_load_dwordx4 v[236:239], v115, s[32:35], s66 offen offset:0 sc1\n"
"buffer_load_dwordx4 v[240:243], v112, s[32:35], s67 offen offset:0 sc1\n"
"buffer_load_dwordx4 v[244:247], v113, s[32:35], s67 offen offset:0 sc1\n"
"buffer_load_dwordx4 v[248:251], v114, s[32:35], s67 offen offset:0 sc1\n"
"buffer_load_dwordx4 v[252:255], v115, s[32:35], s67 offen offset:0 sc1\n"
"buffer_load_dwordx4 v[40:43], v112, s[32:35], s68 offen offset:0 sc1\n"
"buffer_load_dwordx4 v[44:47], v113, s[32:35], s68 offen offset:0 sc1\n"
"buffer_load_dwordx4 v[48:51], v114, s[32:35], s68 offen offset:0 sc1\n"
"buffer_load_dwordx4 v[52:55], v115, s[32:35], s68 offen offset:0 sc1\n"
"buffer_load_dwordx4 v[56:59], v112, s[32:35], s69 offen offset:0 sc1\n"
"buffer_load_dwordx4 v[60:63], v113, s[32:35], s69 offen offset:0 sc1\n"
"buffer_load_dwordx4 v[64:67], v114, s[32:35], s69 offen offset:0 sc1\n"
"buffer_load_dwordx4 v[68:71], v115, s[32:35], s69 offen offset:0 sc1\n"
"s_waitcnt vmcnt(0)\n"
"v_add3_u32 v116, v224, v225, s61\n"
"v_add3_u32 v116, v226, v227, v116\n"
"v_add3_u32 v116, v228, v229, v116\n"
"v_add3_u32 v116, v230, v231, v116\n"
"v_add3_u32 v116, v232, v233, v116\n"
"v_add3_u32 v116, v234, v235, v116\n"
"v_add3_u32 v116, v236, v237, v116\n"
"v_add3_u32 v116, v238, v239, v116\n"
"v_cmp_ne_u32 vcc, v116, v108\n"
"s_cbranch_vccz .Lmg_hr13\n"
"s_sub_u32 s63, s63, 1\n"
"s_cmp_lg_u32 s63, 0\n"
"s_cbranch_scc1 .Lmg_hp13\n"
"s_branch .Lmg_hr13\n"
".Lmg_hs14:\n"
"s_mov_b32 s63, 0x40000\n"
".Lmg_hp14:\n"
"s_sleep 3\n"
"buffer_load_dword v109, v119, s[32:35], s67 offen sc1\n"
"buffer_load_dword v110, v119, s[32:35], s68 offen sc1\n"
"buffer_load_dword v111, v119, s[32:35], s69 offen sc1\n"
"buffer_load_dwordx4 v[240:243], v112, s[32:35], s67 offen offset:0 sc1\n"
"buffer_load_dwordx4 v[244:247], v113, s[32:35], s67 offen offset:0 sc1\n"
"buffer_load_dwordx4 v[248:251], v114, s[32:35], s67 offen offset:0 sc1\n"
"buffer_load_dwordx4 v[252:255], v115, s[32:35], s67 offen offset:0 sc1\n"
"buffer_load_dwordx4 v[40:43], v112, s[32:35], s68 offen offset:0 sc1\n"
"buffer_load_dwordx4 v[44:47], v113, s[32:35], s68 offen offset:0 sc1\n"
"buffer_load_dwordx4 v[48:51], v114, s[32:35], s68 offen offset:0 sc1\n"
"buffer_load_dwordx4 v[52:55], v115, s[32:35], s68 offen offset:0 sc1\n"
"buffer_load_dwordx4 v[56:59], v112, s[32:35], s69 offen offset:0 sc1\n"
"buffer_load_dwordx4 v[60:63], v113, s[32:35], s69 offen offset:0 sc1\n"
"buffer_load_dwordx4 v[64:67], v114, s[32:35], s69 offen offset:0 sc1\n"
"buffer_load_dwordx4 v[68:71], v115, s[32:35], s69 offen offset:0 sc1\n"
"s_waitcnt vmcnt(0)\n"
"v_add3_u32 v116, v240, v241, s61\n"
"v_add3_u32 v116, v242, v243, v116\n"
"v_add3_u32 v116, v244, v245, v116\n"
"v_add3_u32 v116, v246, v247, v116\n"
"v_add3_u32 v116, v248, v249, v116\n"
"v_add3_u32 v116, v250, v251, v116\n"
"v_add3_u32 v116, v252, v253, v116\n"
"v_add3_u32 v116, v254, v255, v116\n"
"v_cmp_ne_u32 vcc, v116, v109\n"
"s_cbranch_vccz .Lmg_hr14\n"
"s_sub_u32 s63, s63, 1\n"
"s_cmp_lg_u32 s63, 0\n"
"s_cbranch_scc1 .Lmg_hp14\n"
"s_branch .Lmg_hr14\n"
".Lmg_hs15:\n"
"s_mov_b32 s63, 0x40000\n"
".Lmg_hp15:\n"
"s_sleep 3\n"
"buffer_load_dword v110, v119, s[32:35], s68 offen sc1\n"
"buffer_load_dword v111, v119, s[32:35], s69 offen sc1\n"
"buffer_load_dword v100, v119, s[40:43], s70 offen sc1\n"
"buffer_load_dwordx4 v[40:43], v112, s[32:35], s68 offen offset:0 sc1\n"
"buffer_load_dwordx4 v[44:47], v113, s[32:35], s68 offen offset:0 sc1\n"
"buffer_load_dwordx4 v[48:51], v114, s[32:35], s68 offen offset:0 sc1\n"
"buffer_load_dwordx4 v[52:55], v115, s[32:35], s68 offen offset:0 sc1\n"
"buffer_load_dwordx4 v[56:59], v112, s[32:35], s69 offen offset:0 sc1\n"
"buffer_load_dwordx4 v[60:63], v113, s[32:35], s69 offen offset:0 sc1\n"
"buffer_load_dwordx4 v[64:67], v114, s[32:35], s69 offen offset:0 sc1\n"
"buffer_load_dwordx4 v[68:71], v115, s[32:35], s69 offen offset:0 sc1\n"
"buffer_load_dwordx4 v[192:195], v112, s[40:43], s70 offen offset:0 sc1\n"
"buffer_load_dwordx4 v[196:199], v113, s[40:43], s70 offen offset:0 sc1\n"
"buffer_load_dwordx4 v[200:203], v114, s[40:43], s70 offen offset:0 sc1\n"
"buffer_load_dwordx4 v[204:207], v115, s[40:43], s70 offen offset:0 sc1\n"
"s_waitcnt vmcnt(0)\n"
"v_add3_u32 v116, v40, v41, s61\n"
"v_add3_u32 v116, v42, v43, v116\n"
"v_add3_u32 v116, v44, v45, v116\n"
"v_add3_u32 v116, v46, v47, v116\n"
"v_add3_u32 v116, v48, v49, v116\n"
"v_add3_u32 v116, v50, v51, v116\n"
"v_add3_u32 v116, v52, v53, v116\n"
"v_add3_u32 v116, v54, v55, v116\n"
"v_cmp_ne_u32 vcc, v116, v110\n"
"s_cbranch_vccz .Lmg_hr15\n"
"s_sub_u32 s63, s63, 1\n"
"s_cmp_lg_u32 s63, 0\n"
"s_cbranch_scc1 .Lmg_hp15\n"
"s_branch .Lmg_hr15\n"
".Lmg_hs16:\n"
"s_mov_b32 s63, 0x40000\n"
".Lmg_hp16:\n"
"s_sleep 3\n"
"buffer_load_dword v111, v119, s[32:35], s69 offen sc1\n"
"buffer_load_dword v100, v119, s[40:43], s70 offen sc1\n"
"buffer_load_dword v101, v119, s[32:35], s71 offen sc1\n"
"buffer_load_dword v108, v119, s[32:35], s72 offen sc1\n"
"buffer_load_dwordx4 v[56:59], v112, s[32:35], s69 offen offset:0 sc1\n"
"buffer_load_dwordx4 v[60:63], v113, s[32:35], s69 offen offset:0 sc1\n"
"buffer_load_dwordx4 v[64:67], v114, s[32:35], s69 offen offset:0 sc1\n"
"buffer_load_dwordx4 v[68:71], v115, s[32:35], s69 offen offset:0 sc1\n"
"buffer_load_dwordx4 v[192:195], v112, s[40:43], s70 offen offset:0 sc1\n"
"buffer_load_dwordx4 v[196:199], v113, s[40:43], s70 offen offset:0 sc1\n"
"buffer_load_dwordx4 v[200:203], v114, s[40:43], s70 offen offset:0 sc1\n"
"buffer_load_dwordx4 v[204:207], v115, s[40:43], s70 offen offset:0 sc1\n"
"buffer_load_dwordx4 v[208:211], v112, s[32:35], s71 offen offset:0 sc1\n"
"buffer_load_dwordx4 v[212:215], v113, s[32:35], s71 offen offset:0 sc1\n"
"buffer_load_dwordx4 v[216:219], v114, s[32:35], s71 offen offset:0 sc1\n"
"buffer_load_dwordx4 v[220:223], v115, s[32:35], s71 offen offset:0 sc1\n"
"buffer_load_dwordx4 v[224:227], v112, s[32:35], s72 offen offset:0 sc1\n"
"s_waitcnt vmcnt(0)\n"
"v_add3_u32 v116, v56, v57, s61\n"
"v_add3_u32 v116, v58, v59, v116\n"
"v_add3_u32 v116, v60, v61, v116\n"
"v_add3_u32 v116, v62, v63, v116\n"
"v_add3_u32 v116, v64, v65, v116\n"
"v_add3_u32 v116, v66, v67, v116\n"
"v_add3_u32 v116, v68, v69, v116\n"
"v_add3_u32 v116, v70, v71, v116\n"
"v_cmp_ne_u32 vcc, v116, v111\n"
"s_cbranch_vccz .Lmg_hr16\n"
"s_sub_u32 s63, s63, 1\n"
"s_cmp_lg_u32 s63, 0\n"
"s_cbranch_scc1 .Lmg_hp16\n"
"s_branch .Lmg_hr16\n"
".Lmg_hs17:\n"
"s_mov_b32 s63, 0x40000\n"
".Lmg_hp17:\n"
"s_sleep 3\n"
"buffer_load_dword v100, v119, s[40:43], s70 offen sc1\n"
"buffer_load_dword v101, v119, s[32:35], s71 offen sc1\n"
"buffer_load_dword v108, v119, s[32:35], s72 offen sc1\n"
"buffer_load_dword v109, v119, s[32:35], s73 offen sc1\n"
"buffer_load_dwordx4 v[192:195], v112, s[40:43], s70 offen offset:0 sc1\n"
"buffer_load_dwordx4 v[196:199], v113, s[40:43], s70 offen offset:0 sc1\n"
"buffer_load_dwordx4 v[200:203], v114, s[40:43], s70 offen offset:0 sc1\n"
"buffer_load_dwordx4 v[204:207], v115, s[40:43], s70 offen offset:0 sc1\n"
"buffer_load_dwordx4 v[208:211], v112, s[32:35], s71 offen offset:0 sc1\n"
"buffer_load_dwordx4 v[212:215], v113, s[32:35], s71 offen offset:0 sc1\n"
"buffer_load_dwordx4 v[216:219], v114, s[32:35], s71 offen offset:0 sc1\n"
"buffer_load_dwordx4 v[220:223], v115, s[32:35], s71 offen offset:0 sc1\n"
"buffer_load_dwordx4 v[224:227], v112, s[32:35], s72 offen offset:0 sc1\n"
"buffer_load_dwordx4 v[228:231], v113, s[32:35], s72 offen offset:0 sc1\n"
"buffer_load_dwordx4 v[232:235], v114, s[32:35], s72 offen offset:0 sc1\n"
"buffer_load_dwordx4 v[236:239], v115, s[32:35], s72 offen offset:0 sc1\n"
"buffer_load_dwordx4 v[240:243], v112, s[32:35], s73 offen offset:0 sc1\n"
"buffer_load_dwordx4 v[244:247], v113, s[32:35], s73 offen offset:0 sc1\n"
"buffer_load_dwordx4 v[248:251], v114, s[32:35], s73 offen offset:0 sc1\n"
"s_waitcnt vmcnt(0)\n"
"v_add3_u32 v116, v192, v193, s61\n"
"v_add3_u32 v116, v194, v195, v116\n"
"v_add3_u32 v116, v196, v197, v116\n"
"v_add3_u32 v116, v198, v199, v116\n"
"v_add3_u32 v116, v200, v201, v116\n"
"v_add3_u32 v116, v202, v203, v116\n"
"v_add3_u32 v116, v204, v205, v116\n"
"v_add3_u32 v116, v206, v207, v116\n"
"v_cmp_ne_u32 vcc, v116, v100\n"
"s_cbranch_vccz .Lmg_hr17\n"
"s_sub_u32 s63, s63, 1\n"
"s_cmp_lg_u32 s63, 0\n"
"s_cbranch_scc1 .Lmg_hp17\n"
"s_branch .Lmg_hr17\n"
".Lmg_hs18:\n"
"s_mov_b32 s63, 0x40000\n"
".Lmg_hp18:\n"
"s_sleep 3\n"
"buffer_load_dword v101, v119, s[32:35], s71 offen sc1\n"
"buffer_load_dword v108, v119, s[32:35], s72 offen sc1\n"
"buffer_load_dword v109, v119, s[32:35], s73 offen sc1\n"
"buffer_load_dword v110, v119, s[32:35], s74 offen sc1\n"
"buffer_load_dwordx4 v[208:211], v112, s[32:35], s71 offen offset:0 sc1\n"
"buffer_load_dwordx4 v[212:215], v113, s[32:35], s71 offen offset:0 sc1\n"
"buffer_load_dwordx4 v[216:219], v114, s[32:35], s71 offen offset:0 sc1\n"
"buffer_load_dwordx4 v[220:223], v115, s[32:35], s71 offen offset:0 sc1\n"
"buffer_load_dwordx4 v[224:227], v112, s[32:35], s72 offen offset:0 sc1\n"
"buffer_load_dwordx4 v[228:231], v113, s[32:35], s72 offen offset:0 sc1\n"
"buffer_load_dwordx4 v[232:235], v114, s[32:35], s72 offen offset:0 sc1\n"
"buffer_load_dwordx4 v[236:239], v115, s[32:35], s72 offen offset:0 sc1\n"
"buffer_load_dwordx4 v[240:243], v112, s[32:35], s73 offen offset:0 sc1\n"
"buffer_load_dwordx4 v[244:247], v113, s[32:35], s73 offen offset:0 sc1\n"
"buffer_load_dwordx4 v[248:251], v114, s[32:35], s73 offen offset:0 sc1\n"
"buffer_load_dwordx4 v[252:255], v115, s[32:35], s73 offen offset:0 sc1\n"
"buffer_load_dwordx4 v[40:43], v112, s[32:35], s74 offen offset:0 sc1\n"
"buffer_load_dwordx4 v[44:47], v113, s[32:35], s74 offen offset:0 sc1\n"
"buffer_load_dwordx4 v[48:51], v114, s[32:35], s74 offen offset:0 sc1\n"
"s_waitcnt vmcnt(0)\n"
"v_add3_u32 v116, v208, v209, s61\n"
"v_add3_u32 v116, v210, v211, v116\n"
"v_add3_u32 v116, v212, v213, v116\n"
"v_add3_u32 v116, v214, v215, v116\n"
"v_add3_u32 v116, v216, v217, v116\n"
"v_add3_u32 v116, v218, v219, v116\n"
"v_add3_u32 v116, v220, v221, v116\n"
"v_add3_u32 v116, v222, v223, v116\n"
"v_cmp_ne_u32 vcc, v116, v101\n"
"s_cbranch_vccz .Lmg_hr18\n"
"s_sub_u32 s63, s63, 1\n"
"s_cmp_lg_u32 s63, 0\n"
"s_cbranch_scc1 .Lmg_hp18\n"
"s_branch .Lmg_hr18\n"
".Lmg_hs19:\n"
"s_mov_b32 s63, 0x40000\n"
".Lmg_hp19:\n"
"s_sleep 3\n"
"buffer_load_dword v101, v119, s[32:35], s71 offen sc1\n"
"buffer_load_dword v108, v119, s[32:35], s72 offen sc1\n"
"buffer_load_dword v109, v119, s[32:35], s73 offen sc1\n"
"buffer_load_dword v110, v119, s[32:35], s74 offen sc1\n"
"buffer_load_dwordx4 v[208:211], v112, s[32:35], s71 offen offset:0 sc1\n"
"buffer_load_dwordx4 v[212:215], v113, s[32:35], s71 offen offset:0 sc1\n"
"buffer_load_dwordx4 v[216:219], v114, s[32:35], s71 offen offset:0 sc1\n"
"buffer_load_dwordx4 v[220:223], v115, s[32:35], s71 offen offset:0 sc1\n"
"buffer_load_dwordx4 v[224:227], v112, s[32:35], s72 offen offset:0 sc1\n"
"buffer_load_dwordx4 v[228:231], v113, s[32:35], s72 offen offset:0 sc1\n"
"buffer_load_dwordx4 v[232:235], v114, s[32:35], s72 offen offset:0 sc1\n"
"buffer_load_dwordx4 v[236:239], v115, s[32:35], s72 offen offset:0 sc1\n"
"buffer_load_dwordx4 v[240:243], v112, s[32:35], s73 offen offset:0 sc1\n"
"buffer_load_dwordx4 v[244:247], v113, s[32:35], s73 offen offset:0 sc1\n"
"buffer_load_dwordx4 v[248:251], v114, s[32:35], s73 offen offset:0 sc1\n"
"buffer_load_dwordx4 v[252:255], v115, s[32:35], s73 offen offset:0 sc1\n"
"buffer_load_dwordx4 v[40:43], v112, s[32:35], s74 offen offset:0 sc1\n"
"buffer_load_dwordx4 v[44:47], v113, s[32:35], s74 offen offset:0 sc1\n"
"buffer_load_dwordx4 v[48:51], v114, s[32:35], s74 offen offset:0 sc1\n"
"buffer_load_dwordx4 v[52:55], v115, s[32:35], s74 offen offset:0 sc1\n"
"s_waitcnt vmcnt(0)\n"
"v_add3_u32 v116, v208, v209, s61\n"
"v_add3_u32 v116, v210, v211, v116\n"
"v_add3_u32 v116, v212, v213, v116\n"
"v_add3_u32 v116, v214, v215, v116\n"
"v_add3_u32 v116, v216, v217, v116\n"
"v_add3_u32 v116, v218, v219, v116\n"
"v_add3_u32 v116, v220, v221, v116\n"
"v_add3_u32 v116, v222, v223, v116\n"
"v_cmp_ne_u32 vcc, v116, v101\n"
"s_cbranch_vccz .Lmg_hr19\n"
"s_sub_u32 s63, s63, 1\n"
"s_cmp_lg_u32 s63, 0\n"
"s_cbranch_scc1 .Lmg_hp19\n"
"s_branch .Lmg_hr19\n"
".Lmg_skip6:\n"
"buffer_load_dwordx4 v[252:255], v115, s[32:35], s73 offen offset:0 sc1\n"
"buffer_load_dword v110, v119, s[32:35], s74 offen sc1\n"
"buffer_load_dwordx4 v[40:43], v112, s[32:35], s74 offen offset:0 sc1\n"
"buffer_load_dwordx4 v[44:47], v113, s[32:35], s74 offen offset:0 sc1\n"
"buffer_load_dwordx4 v[48:51], v114, s[32:35], s74 offen offset:0 sc1\n"
"buffer_load_dwordx4 v[52:55], v115, s[32:35], s74 offen offset:0 sc1\n"
"s_waitcnt vmcnt(15)\n"
"v_add3_u32 v116, v208, v209, s61\n"
"v_add3_u32 v116, v210, v211, v116\n"
"v_add3_u32 v116, v212, v213, v116\n"
"v_add3_u32 v116, v214, v215, v116\n"
"v_add3_u32 v116, v216, v217, v116\n"
"v_add3_u32 v116, v218, v219, v116\n"
"v_add3_u32 v116, v220, v221, v116\n"
"v_add3_u32 v116, v222, v223, v116\n"
"v_cmp_ne_u32 vcc, v116, v101\n"
"s_cbranch_vccnz .Lmg_hs19\n"
".Lmg_hr19:\n"
"s_branch .Lmg_back6\n"
".Lmg_hs20:\n"
"s_mov_b32 s63, 0x40000\n"
".Lmg_hp20:\n"
"s_sleep 3\n"
"buffer_load_dword v108, v119, s[32:35], s72 offen sc1\n"
"buffer_load_dword v109, v119, s[32:35], s73 offen sc1\n"
"buffer_load_dword v110, v119, s[32:35], s74 offen sc1\n"
"buffer_load_dword v111, v119, s[32:35], s75 offen sc1\n"
"buffer_load_dwordx4 v[224:227], v112, s[32:35], s72 offen offset:0 sc1\n"
"buffer_load_dwordx4 v[228:231], v113, s[32:35], s72 offen offset:0 sc1\n"
"buffer_load_dwordx4 v[232:235], v114, s[32:35], s72 offen offset:0 sc1\n"
"buffer_load_dwordx4 v[236:239], v115, s[32:35], s72 offen offset:0 sc1\n"
"buffer_load_dwordx4 v[240:243], v112, s[32:35], s73 offen offset:0 sc1\n"
"buffer_load_dwordx4 v[244:247], v113, s[32:35], s73 offen offset:0 sc1\n"
"buffer_load_dwordx4 v[248:251], v114, s[32:35], s73 offen offset:0 sc1\n"
"buffer_load_dwordx4 v[252:255], v115, s[32:35], s73 offen offset:0 sc1\n"
"buffer_load_dwordx4 v[40:43], v112, s[32:35], s74 offen offset:0 sc1\n"
"buffer_load_dwordx4 v[44:47], v113, s[32:35], s74 offen offset:0 sc1\n"
"buffer_load_dwordx4 v[48:51], v114, s[32:35], s74 offen offset:0 sc1\n"
"buffer_load_dwordx4 v[52:55], v115, s[32:35], s74 offen offset:0 sc1\n"
"buffer_load_dwordx4 v[56:59], v112, s[32:35], s75 offen offset:0 sc1\n"
"buffer_load_dwordx4 v[60:63], v113, s[32:35], s75 offen offset:0 sc1\n"
"buffer_load_dwordx4 v[64:67], v114, s[32:35], s75 offen offset:0 sc1\n"
"buffer_load_dwordx4 v[68:71], v115, s[32:35], s75 offen offset:0 sc1\n"
"s_waitcnt vmcnt(0)\n"
"v_add3_u32 v116, v224, v225, s61\n"
"v_add3_u32 v116, v226, v227, v116\n"
"v_add3_u32 v116, v228, v229, v116\n"
"v_add3_u32 v116, v230, v231, v116\n"
"v_add3_u32 v116, v232, v233, v116\n"
"v_add3_u32 v116, v234, v235, v116\n"
"v_add3_u32 v116, v236, v237, v116\n"
"v_add3_u32 v116, v238, v239, v116\n"
"v_cmp_ne_u32 vcc, v116, v108\n"
"s_cbranch_vccz .Lmg_hr20\n"
"s_sub_u32 s63, s63, 1\n"
"s_cmp_lg_u32 s63, 0\n"
"s_cbranch_scc1 .Lmg_hp20\n"
"s_branch .Lmg_hr20\n"
".Lmg_hs21:\n"
"s_mov_b32 s63, 0x40000\n"
".Lmg_hp21:\n"
"s_sleep 3\n"
"buffer_load_dword v109, v119, s[32:35], s73 offen sc1\n"
"buffer_load_dword v110, v119, s[32:35], s74 offen sc1\n"
"buffer_load_dword v111, v119, s[32:35], s75 offen sc1\n"
"buffer_load_dword v100, v119, s[32:35], s76 offen sc1\n"
"buffer_load_dwordx4 v[240:243], v112, s[32:35], s73 offen offset:0 sc1\n"
"buffer_load_dwordx4 v[244:247], v113, s[32:35], s73 offen offset:0 sc1\n"
"buffer_load_dwordx4 v[248:251], v114, s[32:35], s73 offen offset:0 sc1\n"
"buffer_load_dwordx4 v[252:255], v115, s[32:35], s73 offen offset:0 sc1\n"
"buffer_load_dwordx4 v[40:43], v112, s[32:35], s74 offen offset:0 sc1\n"
"buffer_load_dwordx4 v[44:47], v113, s[32:35], s74 offen offset:0 sc1\n"
"buffer_load_dwordx4 v[48:51], v114, s[32:35], s74 offen offset:0 sc1\n"
"buffer_load_dwordx4 v[52:55], v115, s[32:35], s74 offen offset:0 sc1\n"
"buffer_load_dwordx4 v[56:59], v112, s[32:35], s75 offen offset:0 sc1\n"
"buffer_load_dwordx4 v[60:63], v113, s[32:35], s75 offen offset:0 sc1\n"
"buffer_load_dwordx4 v[64:67], v114, s[32:35], s75 offen offset:0 sc1\n"
"buffer_load_dwordx4 v[68:71], v115, s[32:35], s75 offen offset:0 sc1\n"
"buffer_load_dwordx4 v[192:195], v112, s[32:35], s76 offen offset:0 sc1\n"
"buffer_load_dwordx4 v[196:199], v113, s[32:35], s76 offen offset:0 sc1\n"
"buffer_load_dwordx4 v[200:203], v114, s[32:35], s76 offen offset:0 sc1\n"
"buffer_load_dwordx4 v[204:207], v115, s[32:35], s76 offen offset:0 sc1\n"
"s_waitcnt vmcnt(0)\n"
"v_add3_u32 v116, v240, v241, s61\n"
"v_add3_u32 v116, v242, v243, v116\n"
"v_add3_u32 v116, v244, v245, v116\n"
"v_add3_u32 v116, v246, v247, v116\n"
"v_add3_u32 v116, v248, v249, v116\n"
"v_add3_u32 v116, v250, v251, v116\n"
"v_add3_u32 v116, v252, v253, v116\n"
"v_add3_u32 v116, v254, v255, v116\n"
"v_cmp_ne_u32 vcc, v116, v109\n"
"s_cbranch_vccz .Lmg_hr21\n"
"s_sub_u32 s63, s63, 1\n"
"s_cmp_lg_u32 s63, 0\n"
"s_cbranch_scc1 .Lmg_hp21\n"
"s_branch .Lmg_hr21\n"
".Lmg_hs22:\n"
"s_mov_b32 s63, 0x40000\n"
".Lmg_hp22:\n"
"s_sleep 3\n"
"buffer_load_dword v110, v119, s[32:35], s74 offen sc1\n"
"buffer_load_dword v111, v119, s[32:35], s75 offen sc1\n"
"buffer_load_dword v100, v119, s[32:35], s76 offen sc1\n"
"buffer_load_dword v101, v119, s[32:35], s77 offen sc1\n"
"buffer_load_dwordx4 v[40:43], v112, s[32:35], s74 offen offset:0 sc1\n"
"buffer_load_dwordx4 v[44:47], v113, s[32:35], s74 offen offset:0 sc1\n"
"buffer_load_dwordx4 v[48:51], v114, s[32:35], s74 offen offset:0 sc1\n"
"buffer_load_dwordx4 v[52:55], v115, s[32:35], s74 offen offset:0 sc1\n"
"buffer_load_dwordx4 v[56:59], v112, s[32:35], s75 offen offset:0 sc1\n"
"buffer_load_dwordx4 v[60:63], v113, s[32:35], s75 offen offset:0 sc1\n"
"buffer_load_dwordx4 v[64:67], v114, s[32:35], s75 offen offset:0 sc1\n"
"buffer_load_dwordx4 v[68:71], v115, s[32:35], s75 offen offset:0 sc1\n"
"buffer_load_dwordx4 v[192:195], v112, s[32:35], s76 offen offset:0 sc1\n"
"buffer_load_dwordx4 v[196:199], v113, s[32:35], s76 offen offset:0 sc1\n"
"buffer_load_dwordx4 v[200:203], v114, s[32:35], s76 offen offset:0 sc1\n"
"buffer_load_dwordx4 v[204:207], v115, s[32:35], s76 offen offset:0 sc1\n"
"buffer_load_dwordx4 v[208:211], v112, s[32:35], s77 offen offset:0 sc1\n"
"buffer_load_dwordx4 v[212:215], v113, s[32:35], s77 offen offset:0 sc1\n"
"buffer_load_dwordx4 v[216:219], v114, s[32:35], s77 offen offset:0 sc1\n"
"buffer_load_dwordx4 v[220:223], v115, s[32:35], s77 offen offset:0 sc1\n"
"s_waitcnt vmcnt(0)\n"
"v_add3_u32 v116, v40, v41, s61\n"
"v_add3_u32 v116, v42, v43, v116\n"
"v_add3_u32 v116, v44, v45, v116\n"
"v_add3_u32 v116, v46, v47, v116\n"
"v_add3_u32 v116, v48, v49, v116\n"
"v_add3_u32 v116, v50, v51, v116\n"
"v_add3_u32 v116, v52, v53, v116\n"
"v_add3_u32 v116, v54, v55, v116\n"
"v_cmp_ne_u32 vcc, v116, v110\n"
"s_cbranch_vccz .Lmg_hr22\n"
"s_sub_u32 s63, s63, 1\n"
"s_cmp_lg_u32 s63, 0\n"
"s_cbranch_scc1 .Lmg_hp22\n"
"s_branch .Lmg_hr22\n"
".Lmg_hs23:\n"
"s_mov_b32 s63, 0x40000\n"
".Lmg_hp23:\n"
"s_sleep 3\n"
"buffer_load_dword v111, v119, s[32:35], s75 offen sc1\n"
"buffer_load_dword v100, v119, s[32:35], s76 offen sc1\n"
"buffer_load_dword v101, v119, s[32:35], s77 offen sc1\n"
"buffer_load_dwordx4 v[56:59], v112, s[32:35], s75 offen offset:0 sc1\n"
"buffer_load_dwordx4 v[60:63], v113, s[32:35], s75 offen offset:0 sc1\n"
"buffer_load_dwordx4 v[64:67], v114, s[32:35], s75 offen offset:0 sc1\n"
"buffer_load_dwordx4 v[68:71], v115, s[32:35], s75 offen offset:0 sc1\n"
"buffer_load_dwordx4 v[192:195], v112, s[32:35], s76 offen offset:0 sc1\n"
"buffer_load_dwordx4 v[196:199], v113, s[32:35], s76 offen offset:0 sc1\n"
"buffer_load_dwordx4 v[200:203], v114, s[32:35], s76 offen offset:0 sc1\n"
"buffer_load_dwordx4 v[204:207], v115, s[32:35], s76 offen offset:0 sc1\n"
"buffer_load_dwordx4 v[208:211], v112, s[32:35], s77 offen offset:0 sc1\n"
"buffer_load_dwordx4 v[212:215], v113, s[32:35], s77 offen offset:0 sc1\n"
"buffer_load_dwordx4 v[216:219], v114, s[32:35], s77 offen offset:0 sc1\n"
"buffer_load_dwordx4 v[220:223], v115, s[32:35], s77 offen offset:0 sc1\n"
"s_waitcnt vmcnt(0)\n"
"v_add3_u32 v116, v56, v57, s61\n"
"v_add3_u32 v116, v58, v59, v116\n"
"v_add3_u32 v116, v60, v61, v116\n"
"v_add3_u32 v116, v62, v63, v116\n"
"v_add3_u32 v116, v64, v65, v116\n"
"v_add3_u32 v116, v66, v67, v116\n"
"v_add3_u32 v116, v68, v69, v116\n"
"v_add3_u32 v116, v70, v71, v116\n"
"v_cmp_ne_u32 vcc, v116, v111\n"
"s_cbranch_vccz .Lmg_hr23\n"
"s_sub_u32 s63, s63, 1\n"
"s_cmp_lg_u32 s63, 0\n"
"s_cbranch_scc1 .Lmg_hp23\n"
"s_branch .Lmg_hr23\n"
".Lmg_hs24:\n"
"s_mov_b32 s63, 0x40000\n"
".Lmg_hp24:\n"
"s_sleep 3\n"
"buffer_load_dword v100, v119, s[32:35], s76 offen sc1\n"
"buffer_load_dword v101, v119, s[32:35], s77 offen sc1\n"
"buffer_load_dwordx4 v[192:195], v112, s[32:35], s76 offen offset:0 sc1\n"
"buffer_load_dwordx4 v[196:199], v113, s[32:35], s76 offen offset:0 sc1\n"
"buffer_load_dwordx4 v[200:203], v114, s[32:35], s76 offen offset:0 sc1\n"
"buffer_load_dwordx4 v[204:207], v115, s[32:35], s76 offen offset:0 sc1\n"
"buffer_load_dwordx4 v[208:211], v112, s[32:35], s77 offen offset:0 sc1\n"
"buffer_load_dwordx4 v[212:215], v113, s[32:35], s77 offen offset:0 sc1\n"
"buffer_load_dwordx4 v[216:219], v114, s[32:35], s77 offen offset:0 sc1\n"
"buffer_load_dwordx4 v[220:223], v115, s[32:35], s77 offen offset:0 sc1\n"
"s_waitcnt vmcnt(0)\n"
"v_add3_u32 v116, v192, v193, s61\n"
"v_add3_u32 v116, v194, v195, v116\n"
"v_add3_u32 v116, v196, v197, v116\n"
"v_add3_u32 v116, v198, v199, v116\n"
"v_add3_u32 v116, v200, v201, v116\n"
"v_add3_u32 v116, v202, v203, v116\n"
"v_add3_u32 v116, v204, v205, v116\n"
"v_add3_u32 v116, v206, v207, v116\n"
"v_cmp_ne_u32 vcc, v116, v100\n"
"s_cbranch_vccz .Lmg_hr24\n"
"s_sub_u32 s63, s63, 1\n"
"s_cmp_lg_u32 s63, 0\n"
"s_cbranch_scc1 .Lmg_hp24\n"
"s_branch .Lmg_hr24\n"
".Lmg_hs25:\n"
"s_mov_b32 s63, 0x40000\n"
".Lmg_hp25:\n"
"s_sleep 3\n"
"buffer_load_dword v101, v119, s[32:35], s77 offen sc1\n"
"buffer_load_dwordx4 v[208:211], v112, s[32:35], s77 offen offset:0 sc1\n"
"buffer_load_dwordx4 v[212:215], v113, s[32:35], s77 offen offset:0 sc1\n"
"buffer_load_dwordx4 v[216:219], v114, s[32:35], s77 offen offset:0 sc1\n"
"buffer_load_dwordx4 v[220:223], v115, s[32:35], s77 offen offset:0 sc1\n"
"s_waitcnt vmcnt(0)\n"
"v_add3_u32 v116, v208, v209, s61\n"
"v_add3_u32 v116, v210, v211, v116\n"
"v_add3_u32 v116, v212, v213, v116\n"
"v_add3_u32 v116, v214, v215, v116\n"
"v_add3_u32 v116, v216, v217, v116\n"
"v_add3_u32 v116, v218, v219, v116\n"
"v_add3_u32 v116, v220, v221, v116\n"
"v_add3_u32 v116, v222, v223, v116\n"
"v_cmp_ne_u32 vcc, v116, v101\n"
"s_cbranch_vccz .Lmg_hr25\n"
"s_sub_u32 s63, s63, 1\n"
"s_cmp_lg_u32 s63, 0\n"
"s_cbranch_scc1 .Lmg_hp25\n"
"s_branch .Lmg_hr25\n"
".Lmg_sqs:\n"
"s_mov_b32 s63, 0x40000\n"
".Lmg_sqp:\n"
"s_sleep 4\n"
"buffer_load_dwordx2 v[72:73], v106, s[32:35], s22 offen sc1\n"
"buffer_load_dwordx2 v[74:75], v106, s[32:35], s22 offen offset:128 sc1\n"
"s_waitcnt vmcnt(0)\n"
"v_xor_b32 v104, s31, v73\n"
"v_bitop3_b32 v104, v104, v75, s31 bitop3:0xf6\n"
"v_cmp_ne_u32 vcc, 0, v104\n"
"s_cbranch_vccz .Lmg_sqr\n"
"s_sub_u32 s63, s63, 1\n"
"s_cmp_lg_u32 s63, 0\n"
"s_cbranch_scc1 .Lmg_sqp\n"
"s_branch .Lmg_sqr\n"
".Lmg_end:\n"
"s_endpgm\n"
        : "+{v0}"(tid) : "{s[0:1]}"(__builtin_amdgcn_kernarg_segment_ptr()), "{s2}"(blockIdx.x)
        : "memory", "v255", "a255", "s99", "vcc");
}


extern "C" void kernel_launch(void* const* d_in, const int* in_sizes, int n_in,
                              void* d_out, int out_size, void* d_ws, size_t ws_size,
                              hipStream_t stream) {
    Params P;
    P.x = d_in[0]; P.Are = d_in[1]; P.Aim = d_in[2]; P.Bre = d_in[3]; P.Bim = d_in[4]; P.Cre = d_in[5]; P.Cim = d_in[6];
    P.logdt = d_in[7]; P.Dskip = d_in[8]; P.w1 = d_in[10]; P.w2 = d_in[11]; P.out = d_out; P.ws = d_ws;
    (void)hipFuncSetAttribute((const void*)mega, hipFuncAttributeMaxDynamicSharedMemorySize, 145664);
    mega<<<256, 256, 145664, stream>>>(P);
}
```
